```python
import torch

def _softmax(x, axis=-1):
    return torch.softmax(x, dim=axis)
def _gelu(x):
    return torch.nn.functional.gelu(x, approximate="tanh")
def _silu(x):
    return torch.nn.functional.silu(x)
def _sigmoid(x):
    return torch.sigmoid(x)
def _layernorm(x, axis=-1, eps=1e-5):
    m = x.mean(dim=axis, keepdim=True)
    v = x.var(dim=axis, keepdim=True, unbiased=False)
    return (x - m) / torch.sqrt(v + eps)
def _scatter_add(tgt, idx, src, axis=0):
    return tgt.clone().index_add_(axis, idx, src)
def _segment_sum(src, seg, n):
    out = torch.zeros((n,) + src.shape[1:], dtype=src.dtype)
    return out.index_add_(0, seg, src)
def _pad(x, pw):
    flat = tuple(v for pair in reversed(pw) for v in pair)
    return torch.nn.functional.pad(x, flat)

kernel_name = 'gated_mix__linear_kernel_hedgehog__swiglu_ffn__prompt'

def setup_inputs():

    _w = (lambda a: (lambda t: t.to(torch.bfloat16) if t.is_floating_point() else t)(torch.as_tensor(a)))
    def _A_setup():
        B, S, D, H = 1, 2048, 640, 32
        Dh = D // H
        return {
            "x": _w((lambda *s: torch.randn(*s, dtype=torch.bfloat16) * 0.05)(B, S, D)),
            "wqkv": _w((lambda *s: torch.randn(*s, dtype=torch.bfloat16) * 0.8)(D, 3 * D)),
            "w_phi": _w((lambda *s: torch.randn(*s, dtype=torch.bfloat16) * 0.8)(H, Dh, Dh) * (Dh ** -0.5)),
            "wo": _w((lambda *s: torch.randn(*s, dtype=torch.bfloat16) * 0.05)(D, D)),
        }
    _A_in = _A_setup()

    def _B_setup():
        B, S, D, F = 1, 2048, 640, 1728
        return {
            "x": _w((lambda *s: torch.randn(*s, dtype=torch.bfloat16) * 0.05)(B, S, D)),
            "w_gate": _w((lambda *s: torch.randn(*s, dtype=torch.bfloat16) * 0.05)(D, F)),
            "w_up": _w((lambda *s: torch.randn(*s, dtype=torch.bfloat16) * 0.05)(D, F)),
            "w_down": _w((lambda *s: torch.randn(*s, dtype=torch.bfloat16) * 0.05)(F, D)),
        }
    _B_in = _B_setup()

    return {
        "A_x": _A_in["x"],
        "A_wqkv": _A_in["wqkv"],
        "A_w_phi": _A_in["w_phi"],
        "A_wo": _A_in["wo"],
        "B_x": _B_in["x"],
        "B_w_gate": _B_in["w_gate"],
        "B_w_up": _B_in["w_up"],
        "B_w_down": _B_in["w_down"],
        "gate_w": _w((lambda *s: torch.randn(*s, dtype=torch.bfloat16) * 0.05)(640) * 16.0),
    }

def reference(A_x, A_wqkv, A_w_phi, A_wo, B_x, B_w_gate, B_w_up, B_w_down, gate_w):
    def _A_ref(x, wqkv, w_phi, wo):
        B, S, D = x.shape
        H = 32; Dh = D // H
        qkv = (lambda x, *d: x.permute(*d))((x @ wqkv).reshape(B, S, 3, H, Dh), 2, 0, 3, 1, 4)
        q, k, v = qkv[0], qkv[1], qkv[2]
        pq = torch.einsum("bhsd,hdf->bhsf", q, w_phi)
        pk = torch.einsum("bhsd,hdf->bhsf", k, w_phi)
        phi_q = torch.concatenate([_softmax(pq, axis=-1), _softmax(-pq, axis=-1)], axis=-1)
        phi_k = torch.concatenate([_softmax(pk, axis=-1), _softmax(-pk, axis=-1)], axis=-1)
        kv = torch.einsum("bhsf,bhsd->bhfd", phi_k, v)
        z  = torch.einsum("bhsf->bhf", phi_k)
        num = torch.einsum("bhqf,bhfd->bhqd", phi_q, kv)
        den = torch.einsum("bhqf,bhf->bhq", phi_q, z)[..., None] + 1e-6
        o = num / den
        return (lambda x, *d: x.permute(*d))(o, 0, 2, 1, 3).reshape(B, S, D) @ wo

    def _B_ref(x, w_gate, w_up, w_down):
        g = _silu(x @ w_gate)
        u = x @ w_up
        return (g * u) @ w_down

    x = A_x
    g = _sigmoid((x * gate_w).sum(-1, keepdims=True))
    a = _A_ref(x, A_wqkv, A_w_phi, A_wo)
    b = _B_ref(x, B_w_gate, B_w_up, B_w_down)
    return g * a + (1 - g) * b
```

```cpp
#include <hip/hip_runtime.h>
#include <hip/hip_bf16.h>
typedef unsigned short u16;
__device__ __forceinline__ float bf2f(u16 v){ return __uint_as_float(((unsigned)v)<<16); }
__device__ __forceinline__ u16 f2bf(float f){ unsigned u=__float_as_uint(f); u += 0x7fffu + ((u>>16)&1u); return (u16)(u>>16); }
constexpr int S=2048,D=640,H=32,DH=20,F=1728;
__global__ void gemm_naive(const u16* A,const u16* B,float* C,int M,int N,int K){
  int n=blockIdx.x*64+threadIdx.x; int m=blockIdx.y*4+threadIdx.y;
  if(n>=N||m>=M) return;
  float acc=0; for(int k=0;k<K;++k) acc+=bf2f(A[(long)m*K+k])*bf2f(B[(long)k*N+n]);
  C[(long)m*N+n]=acc;
}
__global__ void phi_kernel(const float* qkv,const u16* wphi,float* phiq,float* phik){
  int idx=blockIdx.x*blockDim.x+threadIdx.x; if(idx>=S*H*2) return;
  int which=idx/(S*H); int r=idx%(S*H); int s=r/H,h=r%H;
  const float* q=qkv+(long)s*3*D+which*D+h*DH;
  float* o=(which?phik:phiq)+((long)s*H+h)*40;
  float mx=-1e30f,mn=1e30f;
  for(int f=0;f<DH;++f){ float a=0; for(int d=0;d<DH;++d) a+=bf2f(f2bf(q[d]))*bf2f(wphi[(h*DH+d)*DH+f]); o[f]=a; mx=fmaxf(mx,a); mn=fminf(mn,a);}
  float s1=0,s2=0;
  for(int f=0;f<DH;++f){ float a=o[f]; float e1=__expf(a-mx), e2=__expf(mn-a); o[f]=e1; o[20+f]=e2; s1+=e1; s2+=e2;}
  for(int f=0;f<DH;++f){ o[f]/=s1; o[20+f]/=s2; }
}
__global__ void kv_kernel(const float* phik,const float* qkv,float* kv){
  int idx=blockIdx.x*blockDim.x+threadIdx.x; if(idx>=H*40*21) return;
  int h=idx/(40*21); int r=idx%(40*21); int f=r/21,d=r%21;
  float a=0; for(int s=0;s<S;++s){ float v=d<20?qkv[(long)s*3*D+2*D+h*DH+d]:1.f; a+=phik[((long)s*H+h)*40+f]*v; }
  kv[idx]=a;
}
__global__ void o_kernel(const float* phiq,const float* kv,u16* o){
  int idx=blockIdx.x*blockDim.x+threadIdx.x; if(idx>=S*D) return;
  int s=idx/D,c=idx%D,h=c/DH,d=c%DH;
  float num=0,den=0; for(int f=0;f<40;++f){ float p=phiq[((long)s*H+h)*40+f]; num+=p*kv[(h*40+f)*21+d]; den+=p*kv[(h*40+f)*21+20]; }
  o[idx]=f2bf(num/(den+1e-6f));
}
__global__ void h_kernel(const float* g,const float* u,u16* hh){
  int idx=blockIdx.x*blockDim.x+threadIdx.x; if(idx>=S*F) return;
  float a=g[idx]; hh[idx]=f2bf(a/(1.f+__expf(-a))*u[idx]);
}
__global__ void out_kernel(const u16* x,const u16* gw,const float* a,const float* b,u16* out){
  int s=blockIdx.x; __shared__ float red[64];
  float p=0; for(int k=threadIdx.x;k<D;k+=64) p+=bf2f(x[s*D+k])*bf2f(gw[k]);
  red[threadIdx.x]=p; __syncthreads();
  float t=0; for(int i=0;i<64;++i) t+=red[i];
  float g=1.f/(1.f+__expf(-t));
  for(int k=threadIdx.x;k<D;k+=64) out[s*D+k]=f2bf(g*a[s*D+k]+(1.f-g)*b[s*D+k]);
}
extern "C" void kernel_launch(void* const* d_in, const int* in_sizes, int n_in, void* d_out, int out_size, void* d_ws, size_t ws_size, hipStream_t stream) {
  const u16* x=(const u16*)d_in[0]; const u16* wqkv=(const u16*)d_in[1]; const u16* wphi=(const u16*)d_in[2]; const u16* wo=(const u16*)d_in[3];
  const u16* wg=(const u16*)d_in[5]; const u16* wu=(const u16*)d_in[6]; const u16* wd=(const u16*)d_in[7]; const u16* gw=(const u16*)d_in[8];
  float* ws=(float*)d_ws;
  float* qkv=ws; ws+=S*3*D; float* phiq=ws; ws+=S*H*40; float* phik=ws; ws+=S*H*40; float* kv=ws; ws+=H*40*21+64;
  float* g=ws; ws+=S*F; float* u=ws; ws+=S*F; float* a=ws; ws+=S*D; float* b=ws; ws+=S*D; u16* o=(u16*)ws; ws+=S*D; u16* hh=(u16*)ws;
  dim3 blk(64,4);
  gemm_naive<<<dim3(3*D/64,S/4),blk,0,stream>>>(x,wqkv,qkv,S,3*D,D);
  gemm_naive<<<dim3(F/64,S/4),blk,0,stream>>>(x,wg,g,S,F,D);
  gemm_naive<<<dim3(F/64,S/4),blk,0,stream>>>(x,wu,u,S,F,D);
  phi_kernel<<<S*H*2/256,256,0,stream>>>(qkv,wphi,phiq,phik);
  kv_kernel<<<(H*40*21+63)/64,64,0,stream>>>(phik,qkv,kv);
  o_kernel<<<S*D/256,256,0,stream>>>(phiq,kv,o);
  h_kernel<<<S*F/256,256,0,stream>>>(g,u,hh);
  gemm_naive<<<dim3(D/64,S/4),blk,0,stream>>>(o,wo,a,S,D,D);
  gemm_naive<<<dim3(D/64,S/4),blk,0,stream>>>(hh,wd,b,S,D,F);
  out_kernel<<<S,64,0,stream>>>(x,gw,a,b,(u16*)d_out);
}
```

```cpp
#include <hip/hip_runtime.h>
#include <hip/hip_bf16.h>
typedef unsigned short u16;
__device__ __forceinline__ float bf2f(u16 v){ return __uint_as_float(((unsigned)v)<<16); }
__device__ __forceinline__ u16 f2bf(float f){ unsigned u=__float_as_uint(f); u += 0x7fffu + ((u>>16)&1u); return (u16)(u>>16); }
constexpr int S=2048,D=640,H=32,DH=20,F=1728;
__global__ void gemm_naive(const u16* A,const u16* B,float* C,int M,int N,int K){
  int n=blockIdx.x*64+threadIdx.x; int m=blockIdx.y*4+threadIdx.y;
  if(n>=N||m>=M) return;
  float acc=0; for(int k=0;k<K;++k) acc+=bf2f(A[(long)m*K+k])*bf2f(B[(long)k*N+n]);
  C[(long)m*N+n]=acc;
}
__global__ void phi_kernel(const float* qkv,const u16* wphi,float* phiq,float* phik){
  int idx=blockIdx.x*blockDim.x+threadIdx.x; if(idx>=S*H*2) return;
  int which=idx/(S*H); int r=idx%(S*H); int s=r/H,h=r%H;
  const float* q=qkv+(long)s*3*D+which*D+h*DH;
  float* o=(which?phik:phiq)+((long)s*H+h)*40;
  float mx=-1e30f,mn=1e30f;
  for(int f=0;f<DH;++f){ float a=0; for(int d=0;d<DH;++d) a+=bf2f(f2bf(q[d]))*bf2f(wphi[(h*DH+d)*DH+f]); o[f]=a; mx=fmaxf(mx,a); mn=fminf(mn,a);}
  float s1=0,s2=0;
  for(int f=0;f<DH;++f){ float a=o[f]; float e1=__expf(a-mx), e2=__expf(mn-a); o[f]=e1; o[20+f]=e2; s1+=e1; s2+=e2;}
  for(int f=0;f<DH;++f){ o[f]/=s1; o[20+f]/=s2; }
}
__global__ void kv_kernel(const float* phik,const float* qkv,float* kv){
  int idx=blockIdx.x*blockDim.x+threadIdx.x; if(idx>=H*40*21) return;
  int h=idx/(40*21); int r=idx%(40*21); int f=r/21,d=r%21;
  float a=0; for(int s=0;s<S;++s){ float v=d<20?qkv[(long)s*3*D+2*D+h*DH+d]:1.f; a+=phik[((long)s*H+h)*40+f]*v; }
  kv[idx]=a;
}
__global__ void o_kernel(const float* phiq,const float* kv,u16* o){
  int idx=blockIdx.x*blockDim.x+threadIdx.x; if(idx>=S*D) return;
  int s=idx/D,c=idx%D,h=c/DH,d=c%DH;
  float num=0,den=0; for(int f=0;f<40;++f){ float p=phiq[((long)s*H+h)*40+f]; num+=p*kv[(h*40+f)*21+d]; den+=p*kv[(h*40+f)*21+20]; }
  o[idx]=f2bf(num/(den+1e-6f));
}
__global__ void h_kernel(const float* g,const float* u,u16* hh){
  int idx=blockIdx.x*blockDim.x+threadIdx.x; if(idx>=S*F) return;
  float a=g[idx]; hh[idx]=f2bf(a/(1.f+__expf(-a))*u[idx]);
}
__global__ void out_kernel(const u16* x,const u16* gw,const float* a,const float* b,u16* out){
  int s=blockIdx.x; __shared__ float red[64];
  float p=0; for(int k=threadIdx.x;k<D;k+=64) p+=bf2f(x[s*D+k])*bf2f(gw[k]);
  red[threadIdx.x]=p; __syncthreads();
  float t=0; for(int i=0;i<64;++i) t+=red[i];
  float g=1.f/(1.f+__expf(-t));
  for(int k=threadIdx.x;k<D;k+=64) out[s*D+k]=f2bf(g*a[s*D+k]+(1.f-g)*b[s*D+k]);
}

typedef unsigned long long u64;
typedef __attribute__((ext_vector_type(4))) unsigned u32x4;
struct Seg { const char* p; unsigned n; unsigned pad; };
template<int AUX,int SHARE>
__global__ __launch_bounds__(256) void probe(const char* p0,const char* p1,const char* p2,const char* p3,const char* p4,const char* p5,unsigned* sink){
  extern __shared__ __attribute__((aligned(16))) char lds[];
  u64 r0=__builtin_amdgcn_s_memrealtime();
  int tid=threadIdx.x; int w=__builtin_amdgcn_readfirstlane(tid>>6);
  const char* ps[6]={p0,p1,p2,p3,p4,p5};
  const unsigned ns[6]={2621440u,2457600u,819200u,2211840u,2211840u,2211840u};
  int slot=0;
  #pragma unroll
  for(int s=0;s<6;++s){
    unsigned per, start;
    if(SHARE==0){ per=ns[s]/256; start=per*blockIdx.x; }
    else { int x=blockIdx.x%8, j=blockIdx.x/8;
      if(s==0){ per=ns[s]/64; start=per*((x&1)*32+j);} else if(s==2||s==5){ per=ns[s]/256; start=per*blockIdx.x; } else { per=ns[s]/128; start=per*((x>>1)*32+j);} }
    for(unsigned o=tid*16;o<per+4096-16;o+=4096){
      unsigned oo=o<per?o:(per-16);
      __builtin_amdgcn_global_load_lds((const unsigned*)(ps[s]+start+oo),(unsigned*)(lds+((slot*4+w)*1024)),16,0,AUX);
      slot=(slot+1)&31;
    }
  }
  asm volatile("s_waitcnt vmcnt(0)":::"memory");
  __syncthreads();
  u64 r1=__builtin_amdgcn_s_memrealtime();
  u64 dt=r1-r0; if(dt>3000) dt=3000;
  u64 end=r1+dt*500;
  while(__builtin_amdgcn_s_memrealtime()<end) __builtin_amdgcn_s_sleep(10);
  if(dt==0xffffffffull) sink[0]=1;
}
extern "C" void kernel_launch(void* const* d_in, const int* in_sizes, int n_in, void* d_out, int out_size, void* d_ws, size_t ws_size, hipStream_t stream) {
  const u16* x=(const u16*)d_in[0]; const u16* wqkv=(const u16*)d_in[1]; const u16* wphi=(const u16*)d_in[2]; const u16* wo=(const u16*)d_in[3];
  const u16* wg=(const u16*)d_in[5]; const u16* wu=(const u16*)d_in[6]; const u16* wd=(const u16*)d_in[7]; const u16* gw=(const u16*)d_in[8];
  float* ws=(float*)d_ws;
  float* qkv=ws; ws+=S*3*D; float* phiq=ws; ws+=S*H*40; float* phik=ws; ws+=S*H*40; float* kv=ws; ws+=H*40*21+64;
  float* g=ws; ws+=S*F; float* u=ws; ws+=S*F; float* a=ws; ws+=S*D; float* b=ws; ws+=S*D; u16* o=(u16*)ws; ws+=S*D; u16* hh=(u16*)ws;
  dim3 blk(64,4);
  gemm_naive<<<dim3(3*D/64,S/4),blk,0,stream>>>(x,wqkv,qkv,S,3*D,D);
  gemm_naive<<<dim3(F/64,S/4),blk,0,stream>>>(x,wg,g,S,F,D);
  gemm_naive<<<dim3(F/64,S/4),blk,0,stream>>>(x,wu,u,S,F,D);
  phi_kernel<<<S*H*2/256,256,0,stream>>>(qkv,wphi,phiq,phik);
  kv_kernel<<<(H*40*21+63)/64,64,0,stream>>>(phik,qkv,kv);
  o_kernel<<<S*D/256,256,0,stream>>>(phiq,kv,o);
  h_kernel<<<S*F/256,256,0,stream>>>(g,u,hh);
  gemm_naive<<<dim3(D/64,S/4),blk,0,stream>>>(o,wo,a,S,D,D);
  gemm_naive<<<dim3(D/64,S/4),blk,0,stream>>>(hh,wd,b,S,D,F);
  out_kernel<<<S,64,0,stream>>>(x,gw,a,b,(u16*)d_out);
}
```

```cpp
#include <hip/hip_runtime.h>
#include <hip/hip_bf16.h>
typedef unsigned short u16;
__device__ __forceinline__ float bf2f(u16 v){ return __uint_as_float(((unsigned)v)<<16); }
__device__ __forceinline__ u16 f2bf(float f){ unsigned u=__float_as_uint(f); u += 0x7fffu + ((u>>16)&1u); return (u16)(u>>16); }
constexpr int S=2048,D=640,H=32,DH=20,F=1728;
__global__ void gemm_naive(const u16* A,const u16* B,float* C,int M,int N,int K){
  int n=blockIdx.x*64+threadIdx.x; int m=blockIdx.y*4+threadIdx.y;
  if(n>=N||m>=M) return;
  float acc=0; for(int k=0;k<K;++k) acc+=bf2f(A[(long)m*K+k])*bf2f(B[(long)k*N+n]);
  C[(long)m*N+n]=acc;
}
__global__ void phi_kernel(const float* qkv,const u16* wphi,float* phiq,float* phik){
  int idx=blockIdx.x*blockDim.x+threadIdx.x; if(idx>=S*H*2) return;
  int which=idx/(S*H); int r=idx%(S*H); int s=r/H,h=r%H;
  const float* q=qkv+(long)s*3*D+which*D+h*DH;
  float* o=(which?phik:phiq)+((long)s*H+h)*40;
  float mx=-1e30f,mn=1e30f;
  for(int f=0;f<DH;++f){ float a=0; for(int d=0;d<DH;++d) a+=bf2f(f2bf(q[d]))*bf2f(wphi[(h*DH+d)*DH+f]); o[f]=a; mx=fmaxf(mx,a); mn=fminf(mn,a);}
  float s1=0,s2=0;
  for(int f=0;f<DH;++f){ float a=o[f]; float e1=__expf(a-mx), e2=__expf(mn-a); o[f]=e1; o[20+f]=e2; s1+=e1; s2+=e2;}
  for(int f=0;f<DH;++f){ o[f]/=s1; o[20+f]/=s2; }
}
__global__ void kv_kernel(const float* phik,const float* qkv,float* kv){
  int idx=blockIdx.x*blockDim.x+threadIdx.x; if(idx>=H*40*21) return;
  int h=idx/(40*21); int r=idx%(40*21); int f=r/21,d=r%21;
  float a=0; for(int s=0;s<S;++s){ float v=d<20?qkv[(long)s*3*D+2*D+h*DH+d]:1.f; a+=phik[((long)s*H+h)*40+f]*v; }
  kv[idx]=a;
}
__global__ void o_kernel(const float* phiq,const float* kv,u16* o){
  int idx=blockIdx.x*blockDim.x+threadIdx.x; if(idx>=S*D) return;
  int s=idx/D,c=idx%D,h=c/DH,d=c%DH;
  float num=0,den=0; for(int f=0;f<40;++f){ float p=phiq[((long)s*H+h)*40+f]; num+=p*kv[(h*40+f)*21+d]; den+=p*kv[(h*40+f)*21+20]; }
  o[idx]=f2bf(num/(den+1e-6f));
}
__global__ void h_kernel(const float* g,const float* u,u16* hh){
  int idx=blockIdx.x*blockDim.x+threadIdx.x; if(idx>=S*F) return;
  float a=g[idx]; hh[idx]=f2bf(a/(1.f+__expf(-a))*u[idx]);
}
__global__ void out_kernel(const u16* x,const u16* gw,const float* a,const float* b,u16* out){
  int s=blockIdx.x; __shared__ float red[64];
  float p=0; for(int k=threadIdx.x;k<D;k+=64) p+=bf2f(x[s*D+k])*bf2f(gw[k]);
  red[threadIdx.x]=p; __syncthreads();
  float t=0; for(int i=0;i<64;++i) t+=red[i];
  float g=1.f/(1.f+__expf(-t));
  for(int k=threadIdx.x;k<D;k+=64) out[s*D+k]=f2bf(g*a[s*D+k]+(1.f-g)*b[s*D+k]);
}

typedef unsigned long long u64;
template<int MODE>
__global__ __launch_bounds__(256) void probe(const char* p,unsigned* sink){
  extern __shared__ __attribute__((aligned(16))) char lds[];
  int tid=threadIdx.x; int w=__builtin_amdgcn_readfirstlane(tid>>6);
  u64 r0=__builtin_amdgcn_s_memrealtime();
  int x=blockIdx.x&3;
  for(int i=0;i<120;i+=8){
    #pragma unroll
    for(int j=0;j<8;++j){
      int idx=(i+j)*256+tid; unsigned off;
      if(MODE==0) off=x*491520u+idx*16u; else { int row=idx/48, c=idx%48; off=row*3456u+x*768u+c*16u; }
      __builtin_amdgcn_global_load_lds((const unsigned*)(p+off),(unsigned*)(lds+((((i>>3)&1)*8+j)*4+w)*1024),16,0,0);
    }
    asm volatile("s_waitcnt vmcnt(8)":::"memory");
  }
  asm volatile("s_waitcnt vmcnt(0)":::"memory");
  __syncthreads();
  u64 r1=__builtin_amdgcn_s_memrealtime();
  u64 dt=r1-r0; if(dt>5000) dt=5000;
  u64 end=r1+dt*200;
  while(__builtin_amdgcn_s_memrealtime()<end) __builtin_amdgcn_s_sleep(10);
  if(dt==0xffffffffull) sink[0]=1;
}
extern "C" void kernel_launch(void* const* d_in, const int* in_sizes, int n_in, void* d_out, int out_size, void* d_ws, size_t ws_size, hipStream_t stream) {
  const u16* x=(const u16*)d_in[0]; const u16* wqkv=(const u16*)d_in[1]; const u16* wphi=(const u16*)d_in[2]; const u16* wo=(const u16*)d_in[3];
  const u16* wg=(const u16*)d_in[5]; const u16* wu=(const u16*)d_in[6]; const u16* wd=(const u16*)d_in[7]; const u16* gw=(const u16*)d_in[8];
  float* ws=(float*)d_ws;
  float* qkv=ws; ws+=S*3*D; float* phiq=ws; ws+=S*H*40; float* phik=ws; ws+=S*H*40; float* kv=ws; ws+=H*40*21+64;
  float* g=ws; ws+=S*F; float* u=ws; ws+=S*F; float* a=ws; ws+=S*D; float* b=ws; ws+=S*D; u16* o=(u16*)ws; ws+=S*D; u16* hh=(u16*)ws;
  hipFuncSetAttribute((const void*)probe<0>,hipFuncAttributeMaxDynamicSharedMemorySize,131072);
  probe<0><<<256,256,131072,stream>>>((const char*)wg,(unsigned*)d_ws);
  dim3 blk(64,4);
  gemm_naive<<<dim3(3*D/64,S/4),blk,0,stream>>>(x,wqkv,qkv,S,3*D,D);
  gemm_naive<<<dim3(F/64,S/4),blk,0,stream>>>(x,wg,g,S,F,D);
  gemm_naive<<<dim3(F/64,S/4),blk,0,stream>>>(x,wu,u,S,F,D);
  phi_kernel<<<S*H*2/256,256,0,stream>>>(qkv,wphi,phiq,phik);
  kv_kernel<<<(H*40*21+63)/64,64,0,stream>>>(phik,qkv,kv);
  o_kernel<<<S*D/256,256,0,stream>>>(phiq,kv,o);
  h_kernel<<<S*F/256,256,0,stream>>>(g,u,hh);
  gemm_naive<<<dim3(D/64,S/4),blk,0,stream>>>(o,wo,a,S,D,D);
  gemm_naive<<<dim3(D/64,S/4),blk,0,stream>>>(hh,wd,b,S,D,F);
  out_kernel<<<S,64,0,stream>>>(x,gw,a,b,(u16*)d_out);
}
```

```cpp
#include <hip/hip_runtime.h>
#include <hip/hip_bf16.h>

__global__ __launch_bounds__(256, 1) void gm_main(const void* x, const void* wqkv, const void* wphi, const void* wo, const void* wg,
    const void* wu, const void* wd, const void* gw, void* out, void* ws) {
  asm volatile(
"L_begin:\n"
"s_load_dwordx16 s[16:31], %0, 0\n"
"s_load_dwordx4 s[32:35], %0, 64\n"
"s_getpc_b64 s[6:7]\n"
"s_lshr_b32 s4, %1, 3\n"
"v_lshrrev_b32 v12, 6, %2\n"
"v_and_b32 v13, 63, %2\n"
"v_min_u32 v13, 4, v13\n"
"v_lshl_add_u32 v12, s4, 2, v12\n"
"v_mad_u32_u24 v12, v12, 5, v13\n"
"v_lshlrev_b32 v12, 7, v12\n"
"v_min_u32 v12, L_end-L_begin-64, v12\n"
"global_load_dword v255, v12, s[6:7]\n"
"v_and_b32 v1, 63, %2\n"
"v_lshrrev_b32 v12, 6, %2\n"
"s_and_b32 s101, %1, 7\n"
"v_readfirstlane_b32 s76, v12\n"
"s_lshr_b32 s100, %1, 3\n"
"s_and_b32 s82, s101, 1\n"
"s_lshl_b32 s82, s82, 3\n"
"s_and_b32 s83, s100, 7\n"
"s_add_u32 s77, s82, s83\n"
"s_lshr_b32 s82, s100, 3\n"
"s_and_b32 s90, s101, 6\n"
"s_and_b32 s91, s82, 1\n"
"s_add_u32 s78, s90, s91\n"
"s_lshr_b32 s12, s82, 1\n"
"s_mul_i32 s82, s82, 5\n"
"s_mul_i32 s83, s83, 5\n"
"s_add_u32 s82, s82, s83\n"
"s_mul_i32 s83, s101, 3\n"
"s_add_u32 s82, s82, s83\n"
"s_mul_i32 s83, s82, 205\n"
"s_lshr_b32 s83, s83, 11\n"
"s_mul_i32 s83, s83, 10\n"
"s_sub_u32 s82, s82, s83\n"
"s_lshl_b32 s56, s82, 7\n"
"s_lshl_b32 s80, s76, 11\n"
"s_lshl_b32 s81, s76, 12\n"
"v_lshlrev_b32 v6, 4, v1\n"
"v_lshrrev_b32 v12, 3, v1\n"
"v_mul_u32_u24 v12, 1280, v12\n"
"v_and_b32 v13, 7, v1\n"
"v_lshrrev_b32 v14, 4, v1\n"
"v_xor_b32 v13, v13, v14\n"
"v_lshl_add_u32 v28, v13, 4, v12\n"
"v_xor_b32 v13, 4, v13\n"
"v_lshl_add_u32 v29, v13, 4, v12\n"
"v_add_u32 v29, 9216, v29\n"
"v_add_u32 v30, 18432, v28\n"
"v_add_u32 v31, 18432, v29\n"
"s_cmp_eq_u32 s12, 0\n"
"s_cbranch_scc0 L_att\n"
"L_ffn:\n"
"v_lshrrev_b32 v12, 3, v1\n"
"s_lshl_b32 s82, s76, 4\n"
"v_add_u32 v12, s82, v12\n"
"v_mul_u32_u24 v12, 3456, v12\n"
"v_and_b32 v13, 1, v1\n"
"v_lshl_add_u32 v12, v13, 4, v12\n"
"v_bfe_u32 v13, v1, 1, 2\n"
"v_bfe_u32 v14, v1, 4, 1\n"
"v_xor_b32 v13, v13, v14\n"
"v_lshl_add_u32 v16, v13, 5, v12\n"
"v_xor_b32 v13, 2, v13\n"
"v_lshl_add_u32 v18, v13, 5, v12\n"
"v_add_u32 v17, 26624, v16\n"
"v_add_u32 v19, 26624, v18\n"
"v_lshrrev_b32 v12, 2, v1\n"
"s_lshl_b32 s82, s76, 4\n"
"v_add_u32 v12, s82, v12\n"
"v_mul_u32_u24 v12, 3456, v12\n"
"v_and_b32 v13, 3, v1\n"
"v_lshl_add_u32 v24, v13, 4, v12\n"
"v_xor_b32 v13, 2, v13\n"
"v_lshl_add_u32 v25, v13, 4, v12\n"
"v_add_u32 v25, 1024, v25\n"
"s_mul_i32 s82, s78, 320\n"
"s_sub_u32 s82, 3072, s82\n"
"s_cmp_ge_u32 s78, 6\n"
"s_cselect_b32 s99, 0x40000000, s82\n"
"s_mul_i32 s66, s56, 1728\n"
"s_waitcnt lgkmcnt(0)\n"
"s_lshl_b32 s82, s77, 2\n"
"s_add_u32 s82, s82, s76\n"
"s_mul_i32 s82, s82, 40960\n"
"s_add_u32 s36, s16, s82\n"
"s_addc_u32 s37, s17, 0\n"
"s_and_b32 s37, s37, 0xffff\n"
"s_mov_b32 s38, 0xa000\n"
"s_mov_b32 s39, 0x20000\n"
"s_add_u32 m0, s81, 0x1e000\n"
"s_nop 0\n"
"buffer_load_dwordx4 v28, s[36:39], s56 offen lds\n"
"buffer_load_dwordx4 v29, s[36:39], s56 offen offset:1024 lds\n"
"buffer_load_dwordx4 v30, s[36:39], s56 offen offset:2048 lds\n"
"buffer_load_dwordx4 v31, s[36:39], s56 offen offset:3072 lds\n"
"s_mul_i32 s90, s78, 3\n"
"s_lshl_b32 s82, s90, 7\n"
"s_add_u32 s40, s24, s82\n"
"s_addc_u32 s41, s25, 0\n"
"s_and_b32 s41, s41, 0xffff\n"
"s_sub_u32 s42, 0x21c000, s82\n"
"s_mov_b32 s43, 0x20000\n"
"s_add_u32 s44, s26, s82\n"
"s_addc_u32 s45, s27, 0\n"
"s_and_b32 s45, s45, 0xffff\n"
"s_sub_u32 s46, 0x21c000, s82\n"
"s_mov_b32 s47, 0x20000\n"
"s_sub_u32 s44, s44, 2048\n"
"s_subb_u32 s45, s45, 0\n"
"s_add_u32 s46, s46, 2048\n"
"s_add_u32 m0, s81, 0x0\n"
"s_nop 0\n"
"buffer_load_dwordx4 v16, s[40:43], s66 offen lds\n"
"buffer_load_dwordx4 v17, s[40:43], s66 offen offset:1024 lds\n"
"buffer_load_dwordx4 v18, s[44:47], s66 offen offset:2048 lds\n"
"buffer_load_dwordx4 v19, s[44:47], s66 offen offset:3072 lds\n"
"s_add_u32 s79, s66, 128\n"
"s_add_u32 m0, s81, 0x4000\n"
"s_nop 0\n"
"buffer_load_dwordx4 v16, s[40:43], s79 offen lds\n"
"buffer_load_dwordx4 v17, s[40:43], s79 offen offset:1024 lds\n"
"buffer_load_dwordx4 v18, s[44:47], s79 offen offset:2048 lds\n"
"buffer_load_dwordx4 v19, s[44:47], s79 offen offset:3072 lds\n"
"s_add_u32 s79, s66, s99\n"
"s_add_u32 m0, s80, 0x8000\n"
"s_nop 0\n"
"buffer_load_dwordx4 v24, s[40:43], s79 offen lds\n"
"buffer_load_dwordx4 v25, s[44:47], s79 offen offset:1024 lds\n"
"s_branch L_ffn_body\n"
"L_att:\n"
"s_mul_i32 s82, s76, 61440\n"
"s_add_u32 s82, s82, 1280\n"
"v_add_u32 v12, 0, v1\n"
"v_mul_u32_u24 v13, 3277, v12\n"
"v_lshrrev_b32 v13, 16, v13\n"
"v_mul_u32_u24 v14, 20, v13\n"
"v_sub_u32 v12, v12, v14\n"
"v_mul_u32_u24 v13, 3840, v13\n"
"v_lshl_add_u32 v13, v12, 4, v13\n"
"v_cmp_lt_u32 vcc, 9, v12\n"
"v_mov_b32 v14, 1120\n"
"v_add_u32 v13, s82, v13\n"
"v_cndmask_b32 v14, 0, v14, vcc\n"
"v_add_u32 v112, v13, v14\n"
"v_add_u32 v12, 64, v1\n"
"v_mul_u32_u24 v13, 3277, v12\n"
"v_lshrrev_b32 v13, 16, v13\n"
"v_mul_u32_u24 v14, 20, v13\n"
"v_sub_u32 v12, v12, v14\n"
"v_mul_u32_u24 v13, 3840, v13\n"
"v_lshl_add_u32 v13, v12, 4, v13\n"
"v_cmp_lt_u32 vcc, 9, v12\n"
"v_mov_b32 v14, 1120\n"
"v_add_u32 v13, s82, v13\n"
"v_cndmask_b32 v14, 0, v14, vcc\n"
"v_add_u32 v113, v13, v14\n"
"v_subrev_u32 v113, 1024, v113\n"
"v_add_u32 v12, 128, v1\n"
"v_mul_u32_u24 v13, 3277, v12\n"
"v_lshrrev_b32 v13, 16, v13\n"
"v_mul_u32_u24 v14, 20, v13\n"
"v_sub_u32 v12, v12, v14\n"
"v_mul_u32_u24 v13, 3840, v13\n"
"v_lshl_add_u32 v13, v12, 4, v13\n"
"v_cmp_lt_u32 vcc, 9, v12\n"
"v_mov_b32 v14, 1120\n"
"v_add_u32 v13, s82, v13\n"
"v_cndmask_b32 v14, 0, v14, vcc\n"
"v_add_u32 v114, v13, v14\n"
"v_subrev_u32 v114, 2048, v114\n"
"v_add_u32 v12, 192, v1\n"
"v_mul_u32_u24 v13, 3277, v12\n"
"v_lshrrev_b32 v13, 16, v13\n"
"v_mul_u32_u24 v14, 20, v13\n"
"v_sub_u32 v12, v12, v14\n"
"v_mul_u32_u24 v13, 3840, v13\n"
"v_lshl_add_u32 v13, v12, 4, v13\n"
"v_cmp_lt_u32 vcc, 9, v12\n"
"v_mov_b32 v14, 1120\n"
"v_add_u32 v13, s82, v13\n"
"v_cndmask_b32 v14, 0, v14, vcc\n"
"v_add_u32 v115, v13, v14\n"
"v_subrev_u32 v115, 3072, v115\n"
"v_add_u32 v12, 256, v1\n"
"v_mul_u32_u24 v13, 3277, v12\n"
"v_lshrrev_b32 v13, 16, v13\n"
"v_mul_u32_u24 v14, 20, v13\n"
"v_sub_u32 v12, v12, v14\n"
"v_mul_u32_u24 v13, 3840, v13\n"
"v_lshl_add_u32 v13, v12, 4, v13\n"
"v_cmp_lt_u32 vcc, 9, v12\n"
"v_mov_b32 v14, 1120\n"
"v_add_u32 v13, s82, v13\n"
"v_cndmask_b32 v14, 0, v14, vcc\n"
"v_add_u32 v116, v13, v14\n"
"v_subrev_u32 v116, 1024, v116\n"
"s_mul_i32 s93, s76, 5120\n"
"s_mul_i32 s66, s56, 1920\n"
"s_waitcnt lgkmcnt(0)\n"
"s_lshl_b32 s82, s77, 2\n"
"s_add_u32 s82, s82, s76\n"
"s_mul_i32 s82, s82, 40960\n"
"s_add_u32 s36, s16, s82\n"
"s_addc_u32 s37, s17, 0\n"
"s_and_b32 s37, s37, 0xffff\n"
"s_mov_b32 s38, 0xa000\n"
"s_mov_b32 s39, 0x20000\n"
"s_add_u32 m0, s81, 0x0\n"
"s_nop 0\n"
"buffer_load_dwordx4 v28, s[36:39], s56 offen lds\n"
"buffer_load_dwordx4 v29, s[36:39], s56 offen offset:1024 lds\n"
"buffer_load_dwordx4 v30, s[36:39], s56 offen offset:2048 lds\n"
"buffer_load_dwordx4 v31, s[36:39], s56 offen offset:3072 lds\n"
"s_mul_i32 s90, s78, 160\n"
"s_add_u32 s40, s18, s90\n"
"s_addc_u32 s41, s19, 0\n"
"s_and_b32 s41, s41, 0xffff\n"
"s_sub_u32 s42, 0x258000, s90\n"
"s_mov_b32 s43, 0x20000\n"
"s_add_u32 m0, s93, 0x1a000\n"
"s_nop 0\n"
"buffer_load_dwordx4 v112, s[40:43], s66 offen lds\n"
"buffer_load_dwordx4 v113, s[40:43], s66 offen offset:1024 lds\n"
"buffer_load_dwordx4 v114, s[40:43], s66 offen offset:2048 lds\n"
"buffer_load_dwordx4 v115, s[40:43], s66 offen offset:3072 lds\n"
"s_add_u32 m0, s93, 0x1ac00\n"
"s_nop 0\n"
"buffer_load_dwordx4 v116, s[40:43], s66 offen offset:1024 lds\n"
"s_mul_i32 s90, s78, 3200\n"
"s_add_u32 s52, s20, s90\n"
"s_addc_u32 s53, s21, 0\n"
"s_and_b32 s53, s53, 0xffff\n"
"s_mov_b32 s54, 0xc80\n"
"s_mov_b32 s55, 0x20000\n"
"s_mov_b32 s44, s30\n"
"s_and_b32 s45, s31, 0xffff\n"
"s_mov_b32 s46, 0x500\n"
"s_mov_b32 s47, 0x20000\n"
"s_lshl_b32 s90, s76, 10\n"
"s_add_u32 m0, s90, 0x24000\n"
"s_nop 0\n"
"buffer_load_dwordx4 v6, s[52:55], s90 offen lds\n"
"s_add_u32 m0, s90, 0x25000\n"
"s_nop 0\n"
"buffer_load_dwordx4 v6, s[44:47], s90 offen lds\n"
"s_mov_b32 s48, s34\n"
"s_and_b32 s49, s35, 0xffff\n"
"s_mov_b32 s50, 0xe00000\n"
"s_mov_b32 s51, 0x20000\n"
"v_mov_b32 v2, 0\n"
"buffer_load_dwordx2 v[2:3], v2, s[48:51], 0 offen sc1\n"
"v_cmp_eq_u32 vcc, 0, %2\n"
"v_mov_b32 v13, 0x40000000\n"
"s_nop 0\n"
"v_cndmask_b32 v4, v13, v1, vcc\n"
"s_and_b32 s89, s100, 7\n"
"s_xor_b32 s82, s12, s100\n"
"s_and_b32 s82, s82, 1\n"
"s_bfe_u32 s83, s100, 0x10003\n"
"s_lshl_b32 s83, s83, 1\n"
"s_add_u32 s82, s82, s83\n"
"s_lshl_b32 s88, s101, 2\n"
"s_add_u32 s88, s88, s82\n"
"s_lshr_b32 s82, s100, 1\n"
"s_lshr_b32 s83, s100, 2\n"
"s_and_b32 s83, s83, 2\n"
"s_add_u32 s82, s82, s83\n"
"s_and_b32 s0, s82, 0\n"
"s_lshr_b32 s0, s101, 0\n"
"s_and_b32 s0, s0, 3\n"
"s_add_u32 s57, s56, 128\n"
"s_cmp_eq_u32 s57, 1280\n"
"s_cselect_b32 s57, 0, s57\n"
"s_mul_i32 s67, s57, 1920\n"
"s_add_u32 s58, s57, 128\n"
"s_cmp_eq_u32 s58, 1280\n"
"s_cselect_b32 s58, 0, s58\n"
"s_mul_i32 s68, s58, 1920\n"
"s_add_u32 s59, s58, 128\n"
"s_cmp_eq_u32 s59, 1280\n"
"s_cselect_b32 s59, 0, s59\n"
"s_mul_i32 s69, s59, 1920\n"
"s_add_u32 s60, s59, 128\n"
"s_cmp_eq_u32 s60, 1280\n"
"s_cselect_b32 s60, 0, s60\n"
"s_mul_i32 s70, s60, 1920\n"
"s_add_u32 s61, s60, 128\n"
"s_cmp_eq_u32 s61, 1280\n"
"s_cselect_b32 s61, 0, s61\n"
"s_mul_i32 s71, s61, 1920\n"
"s_add_u32 s62, s61, 128\n"
"s_cmp_eq_u32 s62, 1280\n"
"s_cselect_b32 s62, 0, s62\n"
"s_mul_i32 s72, s62, 1920\n"
"s_add_u32 s63, s62, 128\n"
"s_cmp_eq_u32 s63, 1280\n"
"s_cselect_b32 s63, 0, s63\n"
"s_mul_i32 s73, s63, 1920\n"
"s_add_u32 s64, s63, 128\n"
"s_cmp_eq_u32 s64, 1280\n"
"s_cselect_b32 s64, 0, s64\n"
"s_mul_i32 s74, s64, 1920\n"
"s_add_u32 s65, s64, 128\n"
"s_cmp_eq_u32 s65, 1280\n"
"s_cselect_b32 s65, 0, s65\n"
"s_mul_i32 s75, s65, 1920\n"
"v_and_b32 v12, 31, v1\n"
"v_bfe_u32 v13, v1, 1, 3\n"
"v_lshrrev_b32 v14, 5, v1\n"
"v_xor_b32 v13, v13, v14\n"
"v_lshlrev_b32 v12, 7, v12\n"
"v_add_u32 v12, s81, v12\n"
"v_xor_b32 v14, 0, v13\n"
"v_lshl_add_u32 v32, v14, 4, v12\n"
"v_xor_b32 v14, 2, v13\n"
"v_lshl_add_u32 v33, v14, 4, v12\n"
"v_xor_b32 v14, 4, v13\n"
"v_lshl_add_u32 v34, v14, 4, v12\n"
"v_xor_b32 v14, 6, v13\n"
"v_lshl_add_u32 v35, v14, 4, v12\n"
"v_lshrrev_b32 v12, 5, v1\n"
"v_bfe_u32 v13, v1, 2, 2\n"
"v_bfe_u32 v14, v1, 4, 1\n"
"v_lshlrev_b32 v14, 5, v14\n"
"v_and_b32 v15, 3, v1\n"
"v_lshl_add_u32 v14, v15, 3, v14\n"
"v_lshl_add_u32 v15, v12, 3, v13\n"
"v_mul_u32_u24 v15, 320, v15\n"
"v_add_u32 v15, v15, v14\n"
"v_add_u32 v122, 0x10000, v15\n"
"v_lshl_add_u32 v15, v12, 2, v13\n"
"v_mul_u32_u24 v15, 320, v15\n"
"v_add_u32 v15, v15, v14\n"
"v_add_u32 v124, 0x10000, v15\n"
"v_and_b32 v13, 15, v1\n"
"v_bfe_u32 v14, v1, 4, 1\n"
"v_cmp_eq_u32 vcc, v13, v14\n"
"v_lshlrev_b32 v12, 4, v12\n"
"v_add_u32 v12, 0x25000, v12\n"
"v_mov_b32 v13, 0x25500\n"
"v_cndmask_b32 v126, v13, v12, vcc\n"
"v_and_b32 v12, 31, v1\n"
"s_lshl_b32 s82, s76, 5\n"
"v_add_u32 v12, s82, v12\n"
"v_lshrrev_b32 v13, 5, v1\n"
"v_mul_u32_u24 v14, 176, v12\n"
"v_lshl_add_u32 v14, v13, 3, v14\n"
"v_add_u32 v40, 0xa800, v14\n"
"v_mul_u32_u24 v14, 336, v12\n"
"v_mul_u32_u24 v13, 80, v13\n"
"v_add_u32 v41, v14, v13\n"
"s_and_b32 s82, s76, 1\n"
"s_mul_i32 s82, s82, 122880\n"
"v_add_u32 v12, 0, v1\n"
"v_mul_u32_u24 v13, 6554, v12\n"
"v_lshrrev_b32 v13, 16, v13\n"
"v_mul_u32_u24 v14, 10, v13\n"
"v_sub_u32 v12, v12, v14\n"
"v_and_b32 v14, 24, v13\n"
"v_and_b32 v15, 1, v13\n"
"v_lshl_add_u32 v14, v15, 2, v14\n"
"v_bfe_u32 v15, v13, 1, 2\n"
"v_add_u32 v14, v14, v15\n"
"v_mul_u32_u24 v14, 3840, v14\n"
"v_lshl_add_u32 v14, v12, 4, v14\n"
"v_add_u32 v117, s82, v14\n"
"v_add_u32 v12, 64, v1\n"
"v_mul_u32_u24 v13, 6554, v12\n"
"v_lshrrev_b32 v13, 16, v13\n"
"v_mul_u32_u24 v14, 10, v13\n"
"v_sub_u32 v12, v12, v14\n"
"v_and_b32 v14, 24, v13\n"
"v_and_b32 v15, 1, v13\n"
"v_lshl_add_u32 v14, v15, 2, v14\n"
"v_bfe_u32 v15, v13, 1, 2\n"
"v_add_u32 v14, v14, v15\n"
"v_mul_u32_u24 v14, 3840, v14\n"
"v_lshl_add_u32 v14, v12, 4, v14\n"
"v_add_u32 v118, s82, v14\n"
"v_subrev_u32 v118, 1024, v118\n"
"v_add_u32 v12, 128, v1\n"
"v_mul_u32_u24 v13, 6554, v12\n"
"v_lshrrev_b32 v13, 16, v13\n"
"v_mul_u32_u24 v14, 10, v13\n"
"v_sub_u32 v12, v12, v14\n"
"v_and_b32 v14, 24, v13\n"
"v_and_b32 v15, 1, v13\n"
"v_lshl_add_u32 v14, v15, 2, v14\n"
"v_bfe_u32 v15, v13, 1, 2\n"
"v_add_u32 v14, v14, v15\n"
"v_mul_u32_u24 v14, 3840, v14\n"
"v_lshl_add_u32 v14, v12, 4, v14\n"
"v_add_u32 v119, s82, v14\n"
"v_subrev_u32 v119, 2048, v119\n"
"v_add_u32 v12, 192, v1\n"
"v_mul_u32_u24 v13, 6554, v12\n"
"v_lshrrev_b32 v13, 16, v13\n"
"v_mul_u32_u24 v14, 10, v13\n"
"v_sub_u32 v12, v12, v14\n"
"v_and_b32 v14, 24, v13\n"
"v_and_b32 v15, 1, v13\n"
"v_lshl_add_u32 v14, v15, 2, v14\n"
"v_bfe_u32 v15, v13, 1, 2\n"
"v_add_u32 v14, v14, v15\n"
"v_mul_u32_u24 v14, 3840, v14\n"
"v_lshl_add_u32 v14, v12, 4, v14\n"
"v_add_u32 v120, s82, v14\n"
"v_subrev_u32 v120, 3072, v120\n"
"v_add_u32 v12, 256, v1\n"
"v_mul_u32_u24 v13, 6554, v12\n"
"v_lshrrev_b32 v13, 16, v13\n"
"v_mul_u32_u24 v14, 10, v13\n"
"v_sub_u32 v12, v12, v14\n"
"v_and_b32 v14, 24, v13\n"
"v_and_b32 v15, 1, v13\n"
"v_lshl_add_u32 v14, v15, 2, v14\n"
"v_bfe_u32 v15, v13, 1, 2\n"
"v_add_u32 v14, v14, v15\n"
"v_mul_u32_u24 v14, 3840, v14\n"
"v_lshl_add_u32 v14, v12, 4, v14\n"
"v_add_u32 v121, s82, v14\n"
"v_subrev_u32 v121, 1024, v121\n"
"v_bfe_u32 v12, v1, 4, 1\n"
"v_mul_u32_u24 v12, 0x12800, v12\n"
"v_and_b32 v13, 15, v1\n"
"v_lshrrev_b32 v14, 5, v1\n"
"v_lshl_add_u32 v13, v14, 4, v13\n"
"v_lshl_add_u32 v5, v13, 4, v12\n"
"s_lshl_b32 s82, s77, 2\n"
"s_add_u32 s82, s82, s76\n"
"s_mul_i32 s82, s82, 151552\n"
"s_add_u32 s86, s82, 0x400000\n"
"s_lshl_b32 s82, s77, 9\n"
"s_add_u32 s87, s82, 1024\n"
"s_mov_b32 s98, 0x3fb8aa3b\n"
"s_lshl_b32 s82, s78, 2\n"
"s_add_u32 s82, s82, s76\n"
"s_lshl_b32 s82, s82, 4\n"
"s_add_u32 s82, s82, s77\n"
"s_mul_i32 s82, s82, 1792\n"
"s_add_u32 s14, s82, 81920\n"
"s_lshl_b32 s82, s78, 13\n"
"s_lshl_b32 s83, s77, 9\n"
"s_add_u32 s82, s82, s83\n"
"s_lshl_b32 s83, s76, 7\n"
"s_add_u32 s82, s82, s83\n"
"s_add_u32 s15, s82, 16384\n"
"s_lshl_b32 s82, s78, 5\n"
"s_add_u32 s87, s87, s82\n"
"s_add_u32 s87, s87, 256\n"
"s_lshl_b32 s82, s78, 7\n"
"s_lshl_b32 s83, s77, 3\n"
"s_add_u32 s82, s82, s83\n"
"s_add_u32 s92, s82, 9216\n"
"s_mul_i32 s82, s78, 2560\n"
"s_add_u32 s86, s86, s82\n"
"s_add_u32 s86, s86, 55296\n"
"v_lshrrev_b32 v80, 5, v1\n"
"v_bfe_u32 v81, v1, 2, 2\n"
"v_bfe_u32 v82, v1, 4, 1\n"
"v_lshlrev_b32 v82, 5, v82\n"
"v_and_b32 v83, 3, v1\n"
"v_lshl_add_u32 v82, v83, 3, v82\n"
"v_bfe_u32 v82, v1, 4, 1\n"
"v_bfe_u32 v83, v1, 1, 1\n"
"v_lshl_add_u32 v82, v82, 1, v83\n"
"v_and_b32 v83, 1, v1\n"
"v_mul_u32_u24 v92, 160, v80\n"
"v_mul_u32_u24 v93, 40, v81\n"
"v_add_u32 v92, v92, v93\n"
"v_add_u32 v92, 0x24000, v92\n"
"v_mul_u32_u24 v93, 5, v83\n"
"v_sub_u32 v93, v80, v93\n"
"v_mov_b32 v94, 0x24c80\n"
"v_add_u32 v95, 0, v82\n"
"v_cmp_lt_u32 vcc, 4, v95\n"
"s_nop 1\n"
"v_cndmask_b32 v96, 0, 5, vcc\n"
"v_sub_u32 v97, v95, v96\n"
"v_lshl_add_u32 v88, v97, 3, v92\n"
"v_lshlrev_b32 v96, 1, v96\n"
"v_sub_u32 v84, v93, v96\n"
"v_add_u32 v95, 4, v82\n"
"v_cmp_lt_u32 vcc, 4, v95\n"
"s_nop 1\n"
"v_cndmask_b32 v96, 0, 5, vcc\n"
"v_sub_u32 v97, v95, v96\n"
"v_lshl_add_u32 v89, v97, 3, v92\n"
"v_lshlrev_b32 v96, 1, v96\n"
"v_sub_u32 v85, v93, v96\n"
"v_add_u32 v95, 8, v82\n"
"v_cmp_lt_u32 vcc, 4, v95\n"
"s_nop 1\n"
"v_cndmask_b32 v96, 0, 5, vcc\n"
"v_sub_u32 v97, v95, v96\n"
"v_lshl_add_u32 v90, v97, 3, v92\n"
"v_lshlrev_b32 v96, 1, v96\n"
"v_sub_u32 v86, v93, v96\n"
"v_cmp_lt_u32 vcc, 9, v95\n"
"v_mov_b32 v96, 1000\n"
"s_nop 0\n"
"v_cndmask_b32 v86, v86, v96, vcc\n"
"v_add_u32 v56, 0, v84\n"
"v_cmp_gt_u32 vcc, 5, v56\n"
"v_add_u32 v98, 0, v88\n"
"s_nop 0\n"
"v_cndmask_b32 v56, v94, v98, vcc\n"
"v_add_u32 v57, 2, v84\n"
"v_cmp_gt_u32 vcc, 5, v57\n"
"v_add_u32 v98, 320, v88\n"
"s_nop 0\n"
"v_cndmask_b32 v57, v94, v98, vcc\n"
"v_add_u32 v58, 4, v84\n"
"v_cmp_gt_u32 vcc, 5, v58\n"
"v_add_u32 v98, 640, v88\n"
"s_nop 0\n"
"v_cndmask_b32 v58, v94, v98, vcc\n"
"v_add_u32 v59, 6, v84\n"
"v_cmp_gt_u32 vcc, 5, v59\n"
"v_add_u32 v98, 960, v88\n"
"s_nop 0\n"
"v_cndmask_b32 v59, v94, v98, vcc\n"
"v_add_u32 v60, 8, v84\n"
"v_cmp_gt_u32 vcc, 5, v60\n"
"v_add_u32 v98, 1280, v88\n"
"s_nop 0\n"
"v_cndmask_b32 v60, v94, v98, vcc\n"
"v_add_u32 v61, 10, v84\n"
"v_cmp_gt_u32 vcc, 5, v61\n"
"v_add_u32 v98, 1600, v88\n"
"s_nop 0\n"
"v_cndmask_b32 v61, v94, v98, vcc\n"
"v_add_u32 v62, 0, v85\n"
"v_cmp_gt_u32 vcc, 5, v62\n"
"v_add_u32 v98, 0, v89\n"
"s_nop 0\n"
"v_cndmask_b32 v62, v94, v98, vcc\n"
"v_add_u32 v63, 2, v85\n"
"v_cmp_gt_u32 vcc, 5, v63\n"
"v_add_u32 v98, 320, v89\n"
"s_nop 0\n"
"v_cndmask_b32 v63, v94, v98, vcc\n"
"v_add_u32 v64, 4, v85\n"
"v_cmp_gt_u32 vcc, 5, v64\n"
"v_add_u32 v98, 640, v89\n"
"s_nop 0\n"
"v_cndmask_b32 v64, v94, v98, vcc\n"
"v_add_u32 v65, 6, v85\n"
"v_cmp_gt_u32 vcc, 5, v65\n"
"v_add_u32 v98, 960, v89\n"
"s_nop 0\n"
"v_cndmask_b32 v65, v94, v98, vcc\n"
"v_add_u32 v66, 8, v85\n"
"v_cmp_gt_u32 vcc, 5, v66\n"
"v_add_u32 v98, 1280, v89\n"
"s_nop 0\n"
"v_cndmask_b32 v66, v94, v98, vcc\n"
"v_add_u32 v67, 10, v85\n"
"v_cmp_gt_u32 vcc, 5, v67\n"
"v_add_u32 v98, 1600, v89\n"
"s_nop 0\n"
"v_cndmask_b32 v67, v94, v98, vcc\n"
"v_add_u32 v68, 12, v85\n"
"v_cmp_gt_u32 vcc, 5, v68\n"
"v_add_u32 v98, 1920, v89\n"
"s_nop 0\n"
"v_cndmask_b32 v68, v94, v98, vcc\n"
"v_add_u32 v69, 14, v85\n"
"v_cmp_gt_u32 vcc, 5, v69\n"
"v_add_u32 v98, 2240, v89\n"
"s_nop 0\n"
"v_cndmask_b32 v69, v94, v98, vcc\n"
"v_add_u32 v70, 16, v85\n"
"v_cmp_gt_u32 vcc, 5, v70\n"
"v_add_u32 v98, 2560, v89\n"
"s_nop 0\n"
"v_cndmask_b32 v70, v94, v98, vcc\n"
"v_add_u32 v71, 18, v85\n"
"v_cmp_gt_u32 vcc, 5, v71\n"
"v_add_u32 v98, 2880, v89\n"
"s_nop 0\n"
"v_cndmask_b32 v71, v94, v98, vcc\n"
"v_add_u32 v72, 8, v86\n"
"v_cmp_gt_u32 vcc, 5, v72\n"
"v_add_u32 v98, 1280, v90\n"
"s_nop 0\n"
"v_cndmask_b32 v72, v94, v98, vcc\n"
"v_add_u32 v73, 10, v86\n"
"v_cmp_gt_u32 vcc, 5, v73\n"
"v_add_u32 v98, 1600, v90\n"
"s_nop 0\n"
"v_cndmask_b32 v73, v94, v98, vcc\n"
"v_add_u32 v74, 12, v86\n"
"v_cmp_gt_u32 vcc, 5, v74\n"
"v_add_u32 v98, 1920, v90\n"
"s_nop 0\n"
"v_cndmask_b32 v74, v94, v98, vcc\n"
"v_add_u32 v75, 14, v86\n"
"v_cmp_gt_u32 vcc, 5, v75\n"
"v_add_u32 v98, 2240, v90\n"
"s_nop 0\n"
"v_cndmask_b32 v75, v94, v98, vcc\n"
"v_add_u32 v76, 16, v86\n"
"v_cmp_gt_u32 vcc, 5, v76\n"
"v_add_u32 v98, 2560, v90\n"
"s_nop 0\n"
"v_cndmask_b32 v76, v94, v98, vcc\n"
"v_add_u32 v77, 18, v86\n"
"v_cmp_gt_u32 vcc, 5, v77\n"
"v_add_u32 v98, 2880, v90\n"
"s_nop 0\n"
"v_cndmask_b32 v77, v94, v98, vcc\n"
"v_lshrrev_b32 v20, 5, v1\n"
"v_bfe_u32 v21, v1, 2, 2\n"
"v_bfe_u32 v22, v1, 4, 1\n"
"v_lshlrev_b32 v22, 5, v22\n"
"v_and_b32 v23, 3, v1\n"
"v_lshl_add_u32 v22, v23, 3, v22\n"
"v_lshlrev_b32 v20, 1, v20\n"
"v_lshl_add_u32 v20, v21, 2, v20\n"
"v_and_b32 v21, 1, v1\n"
"v_bfe_u32 v23, v1, 1, 1\n"
"v_lshl_add_u32 v21, v21, 1, v23\n"
"v_bfe_u32 v23, v1, 4, 1\n"
"v_lshl_add_u32 v21, v23, 2, v21\n"
"v_mul_u32_u24 v24, 336, v20\n"
"v_lshl_add_u32 v24, v21, 3, v24\n"
"s_mul_i32 s82, s76, 80\n"
"v_add_u32 v24, s82, v24\n"
"s_mul_i32 s82, s76, 40\n"
"v_cmp_gt_u32 vcc, 40, v22\n"
"v_add_u32 v22, s82, v22\n"
"v_mov_b32 v21, 160\n"
"v_cndmask_b32 v22, v21, v22, vcc\n"
"v_mul_u32_u24 v25, 176, v20\n"
"v_add_u32 v25, v25, v22\n"
"v_add_u32 v25, 0xa800, v25\n"
"v_and_b32 v20, 31, v1\n"
"v_lshrrev_b32 v21, 5, v1\n"
"v_mul_u32_u24 v21, 336, v21\n"
"v_lshl_add_u32 v21, v20, 4, v21\n"
"v_mov_b32 v22, 0x40000000\n"
"v_cmp_gt_u32 vcc, 21, v20\n"
"s_nop 1\n"
"v_cndmask_b32 v26, v22, v21, vcc\n"
"v_cmp_gt_u32 vcc, 21, v1\n"
"s_nop 1\n"
"v_cndmask_b32 v27, v22, v21, vcc\n"
"s_add_u32 m0, s81, 0x4000\n"
"s_nop 0\n"
"buffer_load_dwordx4 v28, s[36:39], s57 offen lds\n"
"buffer_load_dwordx4 v29, s[36:39], s57 offen offset:1024 lds\n"
"buffer_load_dwordx4 v30, s[36:39], s57 offen offset:2048 lds\n"
"buffer_load_dwordx4 v31, s[36:39], s57 offen offset:3072 lds\n"
"s_add_u32 m0, s93, 0x10000\n"
"s_nop 0\n"
"buffer_load_dwordx4 v112, s[40:43], s67 offen lds\n"
"buffer_load_dwordx4 v113, s[40:43], s67 offen offset:1024 lds\n"
"buffer_load_dwordx4 v114, s[40:43], s67 offen offset:2048 lds\n"
"buffer_load_dwordx4 v115, s[40:43], s67 offen offset:3072 lds\n"
"s_add_u32 m0, s93, 0x10c00\n"
"s_nop 0\n"
"buffer_load_dwordx4 v116, s[40:43], s67 offen offset:1024 lds\n"
"s_waitcnt vmcnt(10)\n"
"s_barrier\n"
"ds_read_b128 a[0:3], v32 offset:0\n"
"ds_read_b64_tr_b16 v[80:81], v122 offset:40960\n"
"ds_read_b64_tr_b16 v[82:83], v122 offset:42240\n"
"ds_read_b64_tr_b16 v[84:85], v122 offset:41024\n"
"ds_read_b64_tr_b16 v[86:87], v122 offset:42304\n"
"v_add_u32 v127, s56, v126\n"
"ds_read_b64_tr_b16 v[88:89], v122 offset:41088\n"
"ds_read_b64_tr_b16 v[90:91], v122 offset:42368\n"
"s_add_u32 m0, s81, 0x8000\n"
"s_nop 0\n"
"buffer_load_dwordx4 v28, s[36:39], s58 offen lds\n"
"ds_read_b64_tr_b16 v[92:93], v122 offset:41152\n"
"ds_read_b64_tr_b16 v[94:95], v122 offset:42432\n"
"s_waitcnt lgkmcnt(4)\n"
"v_mfma_f32_32x32x16_bf16 v[128:143], v[80:83], a[0:3], 0\n"
"ds_read_b128 v[48:51], v127 offset:0\n"
"ds_read_b64_tr_b16 v[96:97], v122 offset:41216\n"
"ds_read_b64_tr_b16 v[98:99], v122 offset:42496\n"
"v_mfma_f32_32x32x16_bf16 v[144:159], v[84:87], a[0:3], 0\n"
"ds_read_b128 a[4:7], v33 offset:0\n"
"ds_read_b64_tr_b16 v[100:101], v122 offset:46080\n"
"ds_read_b64_tr_b16 v[102:103], v122 offset:47360\n"
"ds_read_b64_tr_b16 v[104:105], v122 offset:46144\n"
"ds_read_b64_tr_b16 v[106:107], v122 offset:47424\n"
"buffer_load_dwordx4 v29, s[36:39], s58 offen offset:1024 lds\n"
"s_waitcnt lgkmcnt(8)\n"
"v_mfma_f32_32x32x16_bf16 v[160:175], v[88:91], a[0:3], 0\n"
"ds_read_b64_tr_b16 v[108:109], v122 offset:46208\n"
"ds_read_b64_tr_b16 v[110:111], v122 offset:47488\n"
"v_mfma_f32_32x32x16_bf16 v[176:191], v[92:95], a[0:3], 0\n"
"ds_read_b64_tr_b16 v[80:81], v122 offset:46272\n"
"ds_read_b64_tr_b16 v[82:83], v122 offset:47552\n"
"buffer_load_dwordx4 v30, s[36:39], s58 offen offset:2048 lds\n"
"s_waitcnt lgkmcnt(6)\n"
"v_mfma_f32_32x32x16_bf16 v[192:207], v[96:99], a[0:3], 0\n"
"ds_read_b128 v[52:55], v127 offset:32\n"
"ds_read_b64_tr_b16 v[84:85], v122 offset:46336\n"
"ds_read_b64_tr_b16 v[86:87], v122 offset:47616\n"
"v_mfma_f32_16x16x32_bf16 v[44:47], v[48:51], a[0:3], 0\n"
"v_mfma_f32_32x32x16_bf16 v[128:143], v[100:103], a[4:7], v[128:143]\n"
"ds_read_b128 a[8:11], v34 offset:0\n"
"ds_read_b64_tr_b16 v[88:89], v122 offset:51200\n"
"ds_read_b64_tr_b16 v[90:91], v122 offset:52480\n"
"buffer_load_dwordx4 v31, s[36:39], s58 offen offset:3072 lds\n"
"s_waitcnt lgkmcnt(8)\n"
"v_mfma_f32_32x32x16_bf16 v[144:159], v[104:107], a[4:7], v[144:159]\n"
"ds_read_b64_tr_b16 v[92:93], v122 offset:51264\n"
"ds_read_b64_tr_b16 v[94:95], v122 offset:52544\n"
"v_mfma_f32_32x32x16_bf16 v[160:175], v[108:111], a[4:7], v[160:175]\n"
"s_add_u32 m0, s93, 0x15000\n"
"ds_read_b64_tr_b16 v[96:97], v122 offset:51328\n"
"ds_read_b64_tr_b16 v[98:99], v122 offset:52608\n"
"ds_read_b64_tr_b16 v[100:101], v122 offset:51392\n"
"ds_read_b64_tr_b16 v[102:103], v122 offset:52672\n"
"buffer_load_dwordx4 v112, s[40:43], s68 offen lds\n"
"s_waitcnt lgkmcnt(9)\n"
"v_mfma_f32_32x32x16_bf16 v[176:191], v[80:83], a[4:7], v[176:191]\n"
"ds_read_b128 v[48:51], v127 offset:64\n"
"ds_read_b64_tr_b16 v[104:105], v122 offset:51456\n"
"ds_read_b64_tr_b16 v[106:107], v122 offset:52736\n"
"v_mfma_f32_32x32x16_bf16 v[192:207], v[84:87], a[4:7], v[192:207]\n"
"ds_read_b128 a[12:15], v35 offset:0\n"
"ds_read_b64_tr_b16 v[108:109], v122 offset:56320\n"
"ds_read_b64_tr_b16 v[110:111], v122 offset:57600\n"
"buffer_load_dwordx4 v113, s[40:43], s68 offen offset:1024 lds\n"
"v_mfma_f32_16x16x32_bf16 v[44:47], v[52:55], a[4:7], v[44:47]\n"
"s_waitcnt lgkmcnt(10)\n"
"v_mfma_f32_32x32x16_bf16 v[128:143], v[88:91], a[8:11], v[128:143]\n"
"ds_read_b64_tr_b16 v[80:81], v122 offset:56384\n"
"ds_read_b64_tr_b16 v[82:83], v122 offset:57664\n"
"v_mfma_f32_32x32x16_bf16 v[144:159], v[92:95], a[8:11], v[144:159]\n"
"ds_read_b64_tr_b16 v[84:85], v122 offset:56448\n"
"ds_read_b64_tr_b16 v[86:87], v122 offset:57728\n"
"buffer_load_dwordx4 v114, s[40:43], s68 offen offset:2048 lds\n"
"s_waitcnt lgkmcnt(10)\n"
"v_mfma_f32_32x32x16_bf16 v[160:175], v[96:99], a[8:11], v[160:175]\n"
"ds_read_b64_tr_b16 v[88:89], v122 offset:56512\n"
"ds_read_b64_tr_b16 v[90:91], v122 offset:57792\n"
"v_mfma_f32_32x32x16_bf16 v[176:191], v[100:103], a[8:11], v[176:191]\n"
"ds_read_b128 v[52:55], v127 offset:96\n"
"ds_read_b64_tr_b16 v[92:93], v122 offset:56576\n"
"ds_read_b64_tr_b16 v[94:95], v122 offset:57856\n"
"buffer_load_dwordx4 v115, s[40:43], s68 offen offset:3072 lds\n"
"s_waitcnt lgkmcnt(9)\n"
"v_mfma_f32_32x32x16_bf16 v[192:207], v[104:107], a[8:11], v[192:207]\n"
"v_mfma_f32_16x16x32_bf16 v[44:47], v[48:51], a[8:11], v[44:47]\n"
"v_mfma_f32_32x32x16_bf16 v[128:143], v[108:111], a[12:15], v[128:143]\n"
"s_add_u32 m0, s93, 0x15c00\n"
"s_nop 0\n"
"buffer_load_dwordx4 v116, s[40:43], s68 offen offset:1024 lds\n"
"s_waitcnt lgkmcnt(5)\n"
"v_mfma_f32_32x32x16_bf16 v[144:159], v[80:83], a[12:15], v[144:159]\n"
"s_waitcnt vmcnt(9) lgkmcnt(0)\n"
"s_barrier\n"
"ds_read_b128 a[16:19], v32 offset:16384\n"
"ds_read_b64_tr_b16 v[96:97], v122 offset:0\n"
"ds_read_b64_tr_b16 v[98:99], v122 offset:1280\n"
"v_mfma_f32_32x32x16_bf16 v[160:175], v[84:87], a[12:15], v[160:175]\n"
"ds_read_b64_tr_b16 v[100:101], v122 offset:64\n"
"ds_read_b64_tr_b16 v[102:103], v122 offset:1344\n"
"v_mfma_f32_32x32x16_bf16 v[176:191], v[88:91], a[12:15], v[176:191]\n"
"v_add_u32 v127, s57, v126\n"
"ds_read_b64_tr_b16 v[104:105], v122 offset:128\n"
"ds_read_b64_tr_b16 v[106:107], v122 offset:1408\n"
"v_mfma_f32_32x32x16_bf16 v[192:207], v[92:95], a[12:15], v[192:207]\n"
"s_add_u32 m0, s81, 0x0\n"
"ds_read_b64_tr_b16 v[108:109], v122 offset:192\n"
"ds_read_b64_tr_b16 v[110:111], v122 offset:1472\n"
"buffer_load_dwordx4 v28, s[36:39], s59 offen lds\n"
"v_mfma_f32_16x16x32_bf16 v[44:47], v[52:55], a[12:15], v[44:47]\n"
"s_waitcnt lgkmcnt(4)\n"
"v_mfma_f32_32x32x16_bf16 v[128:143], v[96:99], a[16:19], v[128:143]\n"
"ds_read_b128 v[48:51], v127 offset:0\n"
"ds_read_b64_tr_b16 v[80:81], v122 offset:256\n"
"ds_read_b64_tr_b16 v[82:83], v122 offset:1536\n"
"v_readfirstlane_b32 s90, v2\n"
"v_mfma_f32_32x32x16_bf16 v[144:159], v[100:103], a[16:19], v[144:159]\n"
"ds_read_b128 a[20:23], v33 offset:16384\n"
"ds_read_b64_tr_b16 v[84:85], v122 offset:5120\n"
"ds_read_b64_tr_b16 v[86:87], v122 offset:6400\n"
"ds_read_b64_tr_b16 v[88:89], v122 offset:5184\n"
"ds_read_b64_tr_b16 v[90:91], v122 offset:6464\n"
"buffer_load_dwordx4 v29, s[36:39], s59 offen offset:1024 lds\n"
"ds_read_b64_tr_b16 a[160:161], v56\n"
"s_waitcnt lgkmcnt(9)\n"
"v_mfma_f32_32x32x16_bf16 v[160:175], v[104:107], a[16:19], v[160:175]\n"
"ds_read_b64_tr_b16 v[92:93], v122 offset:5248\n"
"ds_read_b64_tr_b16 v[94:95], v122 offset:6528\n"
"v_readfirstlane_b32 s91, v3\n"
"v_mfma_f32_32x32x16_bf16 v[176:191], v[108:111], a[16:19], v[176:191]\n"
"ds_read_b64_tr_b16 v[96:97], v122 offset:5312\n"
"ds_read_b64_tr_b16 v[98:99], v122 offset:6592\n"
"buffer_load_dwordx4 v30, s[36:39], s59 offen offset:2048 lds\n"
"ds_read_b64_tr_b16 a[162:163], v57\n"
"s_waitcnt lgkmcnt(8)\n"
"v_mfma_f32_32x32x16_bf16 v[192:207], v[80:83], a[16:19], v[192:207]\n"
"ds_read_b128 v[52:55], v127 offset:32\n"
"ds_read_b64_tr_b16 v[100:101], v122 offset:5376\n"
"ds_read_b64_tr_b16 v[102:103], v122 offset:6656\n"
"s_mul_i32 s84, s90, 0x4c957f2d\n"
"v_mfma_f32_16x16x32_bf16 v[44:47], v[48:51], a[16:19], v[44:47]\n"
"v_mfma_f32_32x32x16_bf16 v[128:143], v[84:87], a[20:23], v[128:143]\n"
"ds_read_b128 a[24:27], v34 offset:16384\n"
"ds_read_b64_tr_b16 v[104:105], v122 offset:10240\n"
"ds_read_b64_tr_b16 v[106:107], v122 offset:11520\n"
"buffer_load_dwordx4 v31, s[36:39], s59 offen offset:3072 lds\n"
"ds_read_b64_tr_b16 a[164:165], v58\n"
"s_waitcnt lgkmcnt(10)\n"
"v_mfma_f32_32x32x16_bf16 v[144:159], v[88:91], a[20:23], v[144:159]\n"
"ds_read_b64_tr_b16 v[108:109], v122 offset:10304\n"
"ds_read_b64_tr_b16 v[110:111], v122 offset:11584\n"
"s_mul_hi_u32 s85, s90, 0x4c957f2d\n"
"s_mul_i32 s90, s90, 0x5851f42d\n"
"v_mfma_f32_32x32x16_bf16 v[160:175], v[92:95], a[20:23], v[160:175]\n"
"s_add_u32 m0, s93, 0x1a000\n"
"ds_read_b64_tr_b16 v[80:81], v122 offset:10368\n"
"ds_read_b64_tr_b16 v[82:83], v122 offset:11648\n"
"ds_read_b64_tr_b16 v[84:85], v122 offset:10432\n"
"s_waitcnt lgkmcnt(13)\n"
"ds_read_b64_tr_b16 v[86:87], v122 offset:11712\n"
"buffer_load_dwordx4 v112, s[40:43], s69 offen lds\n"
"ds_read_b64_tr_b16 a[166:167], v59\n"
"s_waitcnt lgkmcnt(11)\n"
"v_mfma_f32_32x32x16_bf16 v[176:191], v[96:99], a[20:23], v[176:191]\n"
"ds_read_b128 v[48:51], v127 offset:64\n"
"ds_read_b64_tr_b16 v[88:89], v122 offset:10496\n"
"ds_read_b64_tr_b16 v[90:91], v122 offset:11776\n"
"s_mul_i32 s91, s91, 0x4c957f2d\n"
"v_mfma_f32_32x32x16_bf16 v[192:207], v[100:103], a[20:23], v[192:207]\n"
"ds_read_b128 a[28:31], v35 offset:16384\n"
"s_waitcnt lgkmcnt(13)\n"
"ds_read_b64_tr_b16 v[92:93], v122 offset:15360\n"
"ds_read_b64_tr_b16 v[94:95], v122 offset:16640\n"
"buffer_load_dwordx4 v113, s[40:43], s69 offen offset:1024 lds\n"
"v_mfma_f32_16x16x32_bf16 v[44:47], v[52:55], a[20:23], v[44:47]\n"
"s_waitcnt lgkmcnt(11)\n"
"v_mfma_f32_32x32x16_bf16 v[128:143], v[104:107], a[24:27], v[128:143]\n"
"ds_read_b64_tr_b16 v[96:97], v122 offset:15424\n"
"ds_read_b64_tr_b16 v[98:99], v122 offset:16704\n"
"ds_read_b64_tr_b16 a[168:169], v60\n"
"v_mfma_f32_32x32x16_bf16 v[144:159], v[108:111], a[24:27], v[144:159]\n"
"ds_read_b64_tr_b16 v[100:101], v122 offset:15488\n"
"s_waitcnt lgkmcnt(13)\n"
"ds_read_b64_tr_b16 v[102:103], v122 offset:16768\n"
"buffer_load_dwordx4 v114, s[40:43], s69 offen offset:2048 lds\n"
"ds_read_b64_tr_b16 a[170:171], v61\n"
"s_add_u32 s85, s85, s90\n"
"s_waitcnt lgkmcnt(13)\n"
"v_mfma_f32_32x32x16_bf16 v[160:175], v[80:83], a[24:27], v[160:175]\n"
"ds_read_b64_tr_b16 v[104:105], v122 offset:15552\n"
"ds_read_b64_tr_b16 v[106:107], v122 offset:16832\n"
"s_add_u32 s85, s85, s91\n"
"v_mfma_f32_32x32x16_bf16 v[176:191], v[84:87], a[24:27], v[176:191]\n"
"s_waitcnt lgkmcnt(13)\n"
"ds_read_b128 v[52:55], v127 offset:96\n"
"ds_read_b64_tr_b16 v[108:109], v122 offset:15616\n"
"s_waitcnt lgkmcnt(13)\n"
"ds_read_b64_tr_b16 v[110:111], v122 offset:16896\n"
"buffer_load_dwordx4 v115, s[40:43], s69 offen offset:3072 lds\n"
"s_waitcnt lgkmcnt(11)\n"
"v_mfma_f32_32x32x16_bf16 v[192:207], v[88:91], a[24:27], v[192:207]\n"
"ds_read_b64_tr_b16 a[172:173], v62\n"
"ds_read_b64_tr_b16 a[174:175], v63\n"
"s_add_u32 s84, s84, 0xf767814f\n"
"s_addc_u32 s85, s85, 0x14057b7e\n"
"v_mfma_f32_16x16x32_bf16 v[44:47], v[48:51], a[24:27], v[44:47]\n"
"v_mfma_f32_32x32x16_bf16 v[128:143], v[92:95], a[28:31], v[128:143]\n"
"s_add_u32 m0, s93, 0x1ac00\n"
"s_nop 0\n"
"buffer_load_dwordx4 v116, s[40:43], s69 offen offset:1024 lds\n"
"ds_read_b64_tr_b16 a[176:177], v64\n"
"s_waitcnt lgkmcnt(9)\n"
"v_mfma_f32_32x32x16_bf16 v[144:159], v[96:99], a[28:31], v[144:159]\n"
"s_waitcnt vmcnt(9) lgkmcnt(0)\n"
"s_barrier\n"
"ds_read_b128 a[32:35], v32 offset:32768\n"
"ds_read_b64_tr_b16 v[80:81], v122 offset:20480\n"
"ds_read_b64_tr_b16 v[82:83], v122 offset:21760\n"
"v_mfma_f32_32x32x16_bf16 v[160:175], v[100:103], a[28:31], v[160:175]\n"
"ds_read_b64_tr_b16 v[84:85], v122 offset:20544\n"
"ds_read_b64_tr_b16 v[86:87], v122 offset:21824\n"
"ds_read_b64_tr_b16 a[178:179], v65\n"
"v_mov_b32 v2, s84\n"
"v_mov_b32 v3, s85\n"
"v_mfma_f32_32x32x16_bf16 v[176:191], v[104:107], a[28:31], v[176:191]\n"
"v_add_u32 v127, s58, v126\n"
"ds_read_b64_tr_b16 v[88:89], v122 offset:20608\n"
"ds_read_b64_tr_b16 v[90:91], v122 offset:21888\n"
"ds_read_b64_tr_b16 a[180:181], v66\n"
"v_mfma_f32_32x32x16_bf16 v[192:207], v[108:111], a[28:31], v[192:207]\n"
"s_add_u32 m0, s81, 0x4000\n"
"ds_read_b64_tr_b16 v[92:93], v122 offset:20672\n"
"ds_read_b64_tr_b16 v[94:95], v122 offset:21952\n"
"buffer_load_dwordx4 v28, s[36:39], s60 offen lds\n"
"v_mfma_f32_16x16x32_bf16 v[44:47], v[52:55], a[28:31], v[44:47]\n"
"s_waitcnt lgkmcnt(6)\n"
"v_mfma_f32_32x32x16_bf16 v[128:143], v[80:83], a[32:35], v[128:143]\n"
"ds_read_b128 v[48:51], v127 offset:0\n"
"ds_read_b64_tr_b16 v[96:97], v122 offset:20736\n"
"ds_read_b64_tr_b16 v[98:99], v122 offset:22016\n"
"v_mfma_f32_32x32x16_bf16 v[144:159], v[84:87], a[32:35], v[144:159]\n"
"ds_read_b128 a[36:39], v33 offset:32768\n"
"ds_read_b64_tr_b16 v[100:101], v122 offset:25600\n"
"ds_read_b64_tr_b16 v[102:103], v122 offset:26880\n"
"ds_read_b64_tr_b16 v[104:105], v122 offset:25664\n"
"ds_read_b64_tr_b16 v[106:107], v122 offset:26944\n"
"buffer_load_dwordx4 v29, s[36:39], s60 offen offset:1024 lds\n"
"ds_read_b64_tr_b16 a[182:183], v67\n"
"s_waitcnt lgkmcnt(9)\n"
"v_mfma_f32_32x32x16_bf16 v[160:175], v[88:91], a[32:35], v[160:175]\n"
"ds_read_b64_tr_b16 v[108:109], v122 offset:25728\n"
"ds_read_b64_tr_b16 v[110:111], v122 offset:27008\n"
"v_mfma_f32_32x32x16_bf16 v[176:191], v[92:95], a[32:35], v[176:191]\n"
"ds_read_b64_tr_b16 v[80:81], v122 offset:25792\n"
"ds_read_b64_tr_b16 v[82:83], v122 offset:27072\n"
"buffer_load_dwordx4 v30, s[36:39], s60 offen offset:2048 lds\n"
"ds_read_b64_tr_b16 a[184:185], v68\n"
"s_waitcnt lgkmcnt(8)\n"
"v_mfma_f32_32x32x16_bf16 v[192:207], v[96:99], a[32:35], v[192:207]\n"
"ds_read_b128 v[52:55], v127 offset:32\n"
"ds_read_b64_tr_b16 v[84:85], v122 offset:25856\n"
"ds_read_b64_tr_b16 v[86:87], v122 offset:27136\n"
"v_mfma_f32_16x16x32_bf16 v[44:47], v[48:51], a[32:35], v[44:47]\n"
"v_mfma_f32_32x32x16_bf16 v[128:143], v[100:103], a[36:39], v[128:143]\n"
"ds_read_b128 a[40:43], v34 offset:32768\n"
"ds_read_b64_tr_b16 v[88:89], v122 offset:30720\n"
"ds_read_b64_tr_b16 v[90:91], v122 offset:32000\n"
"buffer_load_dwordx4 v31, s[36:39], s60 offen offset:3072 lds\n"
"ds_read_b64_tr_b16 a[186:187], v69\n"
"s_waitcnt lgkmcnt(10)\n"
"v_mfma_f32_32x32x16_bf16 v[144:159], v[104:107], a[36:39], v[144:159]\n"
"ds_read_b64_tr_b16 v[92:93], v122 offset:30784\n"
"ds_read_b64_tr_b16 v[94:95], v122 offset:32064\n"
"v_mfma_f32_32x32x16_bf16 v[160:175], v[108:111], a[36:39], v[160:175]\n"
"s_add_u32 m0, s93, 0x10000\n"
"ds_read_b64_tr_b16 v[96:97], v122 offset:30848\n"
"ds_read_b64_tr_b16 v[98:99], v122 offset:32128\n"
"ds_read_b64_tr_b16 v[100:101], v122 offset:30912\n"
"s_waitcnt lgkmcnt(13)\n"
"ds_read_b64_tr_b16 v[102:103], v122 offset:32192\n"
"buffer_load_dwordx4 v112, s[40:43], s70 offen lds\n"
"ds_read_b64_tr_b16 a[188:189], v70\n"
"s_waitcnt lgkmcnt(11)\n"
"v_mfma_f32_32x32x16_bf16 v[176:191], v[80:83], a[36:39], v[176:191]\n"
"ds_read_b128 v[48:51], v127 offset:64\n"
"ds_read_b64_tr_b16 v[104:105], v122 offset:30976\n"
"ds_read_b64_tr_b16 v[106:107], v122 offset:32256\n"
"v_mfma_f32_32x32x16_bf16 v[192:207], v[84:87], a[36:39], v[192:207]\n"
"ds_read_b128 a[44:47], v35 offset:32768\n"
"s_waitcnt lgkmcnt(13)\n"
"ds_read_b64_tr_b16 v[108:109], v122 offset:35840\n"
"ds_read_b64_tr_b16 v[110:111], v122 offset:37120\n"
"buffer_load_dwordx4 v113, s[40:43], s70 offen offset:1024 lds\n"
"v_mfma_f32_16x16x32_bf16 v[44:47], v[52:55], a[36:39], v[44:47]\n"
"s_waitcnt lgkmcnt(11)\n"
"v_mfma_f32_32x32x16_bf16 v[128:143], v[88:91], a[40:43], v[128:143]\n"
"ds_read_b64_tr_b16 v[80:81], v122 offset:35904\n"
"ds_read_b64_tr_b16 v[82:83], v122 offset:37184\n"
"ds_read_b64_tr_b16 a[190:191], v71\n"
"v_mfma_f32_32x32x16_bf16 v[144:159], v[92:95], a[40:43], v[144:159]\n"
"ds_read_b64_tr_b16 v[84:85], v122 offset:35968\n"
"s_waitcnt lgkmcnt(13)\n"
"ds_read_b64_tr_b16 v[86:87], v122 offset:37248\n"
"buffer_load_dwordx4 v114, s[40:43], s70 offen offset:2048 lds\n"
"ds_read_b64_tr_b16 a[192:193], v72\n"
"s_waitcnt lgkmcnt(13)\n"
"v_mfma_f32_32x32x16_bf16 v[160:175], v[96:99], a[40:43], v[160:175]\n"
"ds_read_b64_tr_b16 v[88:89], v122 offset:36032\n"
"ds_read_b64_tr_b16 v[90:91], v122 offset:37312\n"
"v_mfma_f32_32x32x16_bf16 v[176:191], v[100:103], a[40:43], v[176:191]\n"
"s_waitcnt lgkmcnt(13)\n"
"ds_read_b128 v[52:55], v127 offset:96\n"
"ds_read_b64_tr_b16 v[92:93], v122 offset:36096\n"
"s_waitcnt lgkmcnt(13)\n"
"ds_read_b64_tr_b16 v[94:95], v122 offset:37376\n"
"buffer_load_dwordx4 v115, s[40:43], s70 offen offset:3072 lds\n"
"s_waitcnt lgkmcnt(11)\n"
"v_mfma_f32_32x32x16_bf16 v[192:207], v[104:107], a[40:43], v[192:207]\n"
"ds_read_b64_tr_b16 a[194:195], v73\n"
"ds_read_b64_tr_b16 a[196:197], v74\n"
"v_mfma_f32_16x16x32_bf16 v[44:47], v[48:51], a[40:43], v[44:47]\n"
"v_mfma_f32_32x32x16_bf16 v[128:143], v[108:111], a[44:47], v[128:143]\n"
"s_add_u32 m0, s93, 0x10c00\n"
"s_nop 0\n"
"buffer_load_dwordx4 v116, s[40:43], s70 offen offset:1024 lds\n"
"ds_read_b64_tr_b16 a[198:199], v75\n"
"s_waitcnt lgkmcnt(9)\n"
"v_mfma_f32_32x32x16_bf16 v[144:159], v[80:83], a[44:47], v[144:159]\n"
"s_waitcnt vmcnt(9) lgkmcnt(0)\n"
"s_barrier\n"
"ds_read_b128 a[48:51], v32 offset:0\n"
"ds_read_b64_tr_b16 v[96:97], v122 offset:40960\n"
"ds_read_b64_tr_b16 v[98:99], v122 offset:42240\n"
"v_mfma_f32_32x32x16_bf16 v[160:175], v[84:87], a[44:47], v[160:175]\n"
"ds_read_b64_tr_b16 v[100:101], v122 offset:41024\n"
"ds_read_b64_tr_b16 v[102:103], v122 offset:42304\n"
"ds_read_b64_tr_b16 a[200:201], v76\n"
"v_mfma_f32_32x32x16_bf16 v[176:191], v[88:91], a[44:47], v[176:191]\n"
"v_add_u32 v127, s59, v126\n"
"ds_read_b64_tr_b16 v[104:105], v122 offset:41088\n"
"ds_read_b64_tr_b16 v[106:107], v122 offset:42368\n"
"ds_read_b64_tr_b16 a[202:203], v77\n"
"v_mfma_f32_32x32x16_bf16 v[192:207], v[92:95], a[44:47], v[192:207]\n"
"s_add_u32 m0, s81, 0x8000\n"
"ds_read_b64_tr_b16 v[108:109], v122 offset:41152\n"
"ds_read_b64_tr_b16 v[110:111], v122 offset:42432\n"
"buffer_load_dwordx4 v28, s[36:39], s61 offen lds\n"
"v_mfma_f32_16x16x32_bf16 v[44:47], v[52:55], a[44:47], v[44:47]\n"
"s_waitcnt lgkmcnt(6)\n"
"v_mfma_f32_32x32x16_bf16 v[128:143], v[96:99], a[48:51], v[128:143]\n"
"ds_read_b128 v[48:51], v127 offset:0\n"
"ds_read_b64_tr_b16 v[80:81], v122 offset:41216\n"
"ds_read_b64_tr_b16 v[82:83], v122 offset:42496\n"
"v_mfma_f32_32x32x16_bf16 v[144:159], v[100:103], a[48:51], v[144:159]\n"
"ds_read_b128 a[52:55], v33 offset:0\n"
"ds_read_b64_tr_b16 v[84:85], v122 offset:46080\n"
"ds_read_b64_tr_b16 v[86:87], v122 offset:47360\n"
"ds_read_b64_tr_b16 v[88:89], v122 offset:46144\n"
"ds_read_b64_tr_b16 v[90:91], v122 offset:47424\n"
"buffer_load_dwordx4 v29, s[36:39], s61 offen offset:1024 lds\n"
"s_waitcnt lgkmcnt(8)\n"
"v_mfma_f32_32x32x16_bf16 v[160:175], v[104:107], a[48:51], v[160:175]\n"
"ds_read_b64_tr_b16 v[92:93], v122 offset:46208\n"
"ds_read_b64_tr_b16 v[94:95], v122 offset:47488\n"
"v_mfma_f32_32x32x16_bf16 v[176:191], v[108:111], a[48:51], v[176:191]\n"
"ds_read_b64_tr_b16 v[96:97], v122 offset:46272\n"
"ds_read_b64_tr_b16 v[98:99], v122 offset:47552\n"
"buffer_load_dwordx4 v30, s[36:39], s61 offen offset:2048 lds\n"
"s_waitcnt lgkmcnt(6)\n"
"v_mfma_f32_32x32x16_bf16 v[192:207], v[80:83], a[48:51], v[192:207]\n"
"ds_read_b128 v[52:55], v127 offset:32\n"
"ds_read_b64_tr_b16 v[100:101], v122 offset:46336\n"
"ds_read_b64_tr_b16 v[102:103], v122 offset:47616\n"
"v_mfma_f32_16x16x32_bf16 v[44:47], v[48:51], a[48:51], v[44:47]\n"
"v_mfma_f32_32x32x16_bf16 v[128:143], v[84:87], a[52:55], v[128:143]\n"
"ds_read_b128 a[56:59], v34 offset:0\n"
"ds_read_b64_tr_b16 v[104:105], v122 offset:51200\n"
"ds_read_b64_tr_b16 v[106:107], v122 offset:52480\n"
"buffer_load_dwordx4 v31, s[36:39], s61 offen offset:3072 lds\n"
"s_waitcnt lgkmcnt(8)\n"
"v_mfma_f32_32x32x16_bf16 v[144:159], v[88:91], a[52:55], v[144:159]\n"
"ds_read_b64_tr_b16 v[108:109], v122 offset:51264\n"
"ds_read_b64_tr_b16 v[110:111], v122 offset:52544\n"
"v_mfma_f32_32x32x16_bf16 v[160:175], v[92:95], a[52:55], v[160:175]\n"
"s_add_u32 m0, s93, 0x15000\n"
"ds_read_b64_tr_b16 v[80:81], v122 offset:51328\n"
"ds_read_b64_tr_b16 v[82:83], v122 offset:52608\n"
"ds_read_b64_tr_b16 v[84:85], v122 offset:51392\n"
"ds_read_b64_tr_b16 v[86:87], v122 offset:52672\n"
"buffer_load_dwordx4 v112, s[40:43], s71 offen lds\n"
"s_waitcnt lgkmcnt(9)\n"
"v_mfma_f32_32x32x16_bf16 v[176:191], v[96:99], a[52:55], v[176:191]\n"
"ds_read_b128 v[48:51], v127 offset:64\n"
"ds_read_b64_tr_b16 v[88:89], v122 offset:51456\n"
"ds_read_b64_tr_b16 v[90:91], v122 offset:52736\n"
"v_mfma_f32_32x32x16_bf16 v[192:207], v[100:103], a[52:55], v[192:207]\n"
"ds_read_b128 a[60:63], v35 offset:0\n"
"ds_read_b64_tr_b16 v[92:93], v122 offset:56320\n"
"ds_read_b64_tr_b16 v[94:95], v122 offset:57600\n"
"buffer_load_dwordx4 v113, s[40:43], s71 offen offset:1024 lds\n"
"v_mfma_f32_16x16x32_bf16 v[44:47], v[52:55], a[52:55], v[44:47]\n"
"s_waitcnt lgkmcnt(10)\n"
"v_mfma_f32_32x32x16_bf16 v[128:143], v[104:107], a[56:59], v[128:143]\n"
"ds_read_b64_tr_b16 v[96:97], v122 offset:56384\n"
"ds_read_b64_tr_b16 v[98:99], v122 offset:57664\n"
"v_mfma_f32_32x32x16_bf16 v[144:159], v[108:111], a[56:59], v[144:159]\n"
"ds_read_b64_tr_b16 v[100:101], v122 offset:56448\n"
"ds_read_b64_tr_b16 v[102:103], v122 offset:57728\n"
"buffer_load_dwordx4 v114, s[40:43], s71 offen offset:2048 lds\n"
"s_waitcnt lgkmcnt(10)\n"
"v_mfma_f32_32x32x16_bf16 v[160:175], v[80:83], a[56:59], v[160:175]\n"
"ds_read_b64_tr_b16 v[104:105], v122 offset:56512\n"
"ds_read_b64_tr_b16 v[106:107], v122 offset:57792\n"
"v_mfma_f32_32x32x16_bf16 v[176:191], v[84:87], a[56:59], v[176:191]\n"
"ds_read_b128 v[52:55], v127 offset:96\n"
"ds_read_b64_tr_b16 v[108:109], v122 offset:56576\n"
"ds_read_b64_tr_b16 v[110:111], v122 offset:57856\n"
"buffer_load_dwordx4 v115, s[40:43], s71 offen offset:3072 lds\n"
"s_waitcnt lgkmcnt(9)\n"
"v_mfma_f32_32x32x16_bf16 v[192:207], v[88:91], a[56:59], v[192:207]\n"
"v_mfma_f32_16x16x32_bf16 v[44:47], v[48:51], a[56:59], v[44:47]\n"
"v_mfma_f32_32x32x16_bf16 v[128:143], v[92:95], a[60:63], v[128:143]\n"
"s_add_u32 m0, s93, 0x15c00\n"
"s_nop 0\n"
"buffer_load_dwordx4 v116, s[40:43], s71 offen offset:1024 lds\n"
"s_waitcnt lgkmcnt(5)\n"
"v_mfma_f32_32x32x16_bf16 v[144:159], v[96:99], a[60:63], v[144:159]\n"
"s_waitcnt vmcnt(9) lgkmcnt(0)\n"
"s_barrier\n"
"ds_read_b128 a[64:67], v32 offset:16384\n"
"ds_read_b64_tr_b16 v[80:81], v122 offset:0\n"
"ds_read_b64_tr_b16 v[82:83], v122 offset:1280\n"
"v_mfma_f32_32x32x16_bf16 v[160:175], v[100:103], a[60:63], v[160:175]\n"
"ds_read_b64_tr_b16 v[84:85], v122 offset:64\n"
"ds_read_b64_tr_b16 v[86:87], v122 offset:1344\n"
"v_mfma_f32_32x32x16_bf16 v[176:191], v[104:107], a[60:63], v[176:191]\n"
"v_add_u32 v127, s60, v126\n"
"ds_read_b64_tr_b16 v[88:89], v122 offset:128\n"
"ds_read_b64_tr_b16 v[90:91], v122 offset:1408\n"
"v_mfma_f32_32x32x16_bf16 v[192:207], v[108:111], a[60:63], v[192:207]\n"
"s_add_u32 m0, s81, 0x0\n"
"ds_read_b64_tr_b16 v[92:93], v122 offset:192\n"
"ds_read_b64_tr_b16 v[94:95], v122 offset:1472\n"
"buffer_load_dwordx4 v28, s[36:39], s62 offen lds\n"
"v_mfma_f32_16x16x32_bf16 v[44:47], v[52:55], a[60:63], v[44:47]\n"
"s_waitcnt lgkmcnt(4)\n"
"v_mfma_f32_32x32x16_bf16 v[128:143], v[80:83], a[64:67], v[128:143]\n"
"ds_read_b128 v[48:51], v127 offset:0\n"
"ds_read_b64_tr_b16 v[96:97], v122 offset:256\n"
"ds_read_b64_tr_b16 v[98:99], v122 offset:1536\n"
"v_mfma_f32_32x32x16_bf16 v[144:159], v[84:87], a[64:67], v[144:159]\n"
"ds_read_b128 a[68:71], v33 offset:16384\n"
"ds_read_b64_tr_b16 v[100:101], v122 offset:5120\n"
"ds_read_b64_tr_b16 v[102:103], v122 offset:6400\n"
"ds_read_b64_tr_b16 v[104:105], v122 offset:5184\n"
"ds_read_b64_tr_b16 v[106:107], v122 offset:6464\n"
"buffer_load_dwordx4 v29, s[36:39], s62 offen offset:1024 lds\n"
"s_waitcnt lgkmcnt(8)\n"
"v_mfma_f32_32x32x16_bf16 v[160:175], v[88:91], a[64:67], v[160:175]\n"
"ds_read_b64_tr_b16 v[108:109], v122 offset:5248\n"
"ds_read_b64_tr_b16 v[110:111], v122 offset:6528\n"
"v_mfma_f32_32x32x16_bf16 v[176:191], v[92:95], a[64:67], v[176:191]\n"
"ds_read_b64_tr_b16 v[80:81], v122 offset:5312\n"
"ds_read_b64_tr_b16 v[82:83], v122 offset:6592\n"
"buffer_load_dwordx4 v30, s[36:39], s62 offen offset:2048 lds\n"
"s_waitcnt lgkmcnt(6)\n"
"v_mfma_f32_32x32x16_bf16 v[192:207], v[96:99], a[64:67], v[192:207]\n"
"ds_read_b128 v[52:55], v127 offset:32\n"
"ds_read_b64_tr_b16 v[84:85], v122 offset:5376\n"
"ds_read_b64_tr_b16 v[86:87], v122 offset:6656\n"
"v_mfma_f32_16x16x32_bf16 v[44:47], v[48:51], a[64:67], v[44:47]\n"
"v_mfma_f32_32x32x16_bf16 v[128:143], v[100:103], a[68:71], v[128:143]\n"
"ds_read_b128 a[72:75], v34 offset:16384\n"
"ds_read_b64_tr_b16 v[88:89], v122 offset:10240\n"
"ds_read_b64_tr_b16 v[90:91], v122 offset:11520\n"
"buffer_load_dwordx4 v31, s[36:39], s62 offen offset:3072 lds\n"
"s_waitcnt lgkmcnt(8)\n"
"v_mfma_f32_32x32x16_bf16 v[144:159], v[104:107], a[68:71], v[144:159]\n"
"ds_read_b64_tr_b16 v[92:93], v122 offset:10304\n"
"ds_read_b64_tr_b16 v[94:95], v122 offset:11584\n"
"v_mfma_f32_32x32x16_bf16 v[160:175], v[108:111], a[68:71], v[160:175]\n"
"s_add_u32 m0, s93, 0x1a000\n"
"ds_read_b64_tr_b16 v[96:97], v122 offset:10368\n"
"ds_read_b64_tr_b16 v[98:99], v122 offset:11648\n"
"ds_read_b64_tr_b16 v[100:101], v122 offset:10432\n"
"ds_read_b64_tr_b16 v[102:103], v122 offset:11712\n"
"buffer_load_dwordx4 v112, s[40:43], s72 offen lds\n"
"s_waitcnt lgkmcnt(9)\n"
"v_mfma_f32_32x32x16_bf16 v[176:191], v[80:83], a[68:71], v[176:191]\n"
"ds_read_b128 v[48:51], v127 offset:64\n"
"ds_read_b64_tr_b16 v[104:105], v122 offset:10496\n"
"ds_read_b64_tr_b16 v[106:107], v122 offset:11776\n"
"v_mfma_f32_32x32x16_bf16 v[192:207], v[84:87], a[68:71], v[192:207]\n"
"ds_read_b128 a[76:79], v35 offset:16384\n"
"ds_read_b64_tr_b16 v[108:109], v122 offset:15360\n"
"ds_read_b64_tr_b16 v[110:111], v122 offset:16640\n"
"buffer_load_dwordx4 v113, s[40:43], s72 offen offset:1024 lds\n"
"v_mfma_f32_16x16x32_bf16 v[44:47], v[52:55], a[68:71], v[44:47]\n"
"s_waitcnt lgkmcnt(10)\n"
"v_mfma_f32_32x32x16_bf16 v[128:143], v[88:91], a[72:75], v[128:143]\n"
"ds_read_b64_tr_b16 v[80:81], v122 offset:15424\n"
"ds_read_b64_tr_b16 v[82:83], v122 offset:16704\n"
"v_mfma_f32_32x32x16_bf16 v[144:159], v[92:95], a[72:75], v[144:159]\n"
"ds_read_b64_tr_b16 v[84:85], v122 offset:15488\n"
"ds_read_b64_tr_b16 v[86:87], v122 offset:16768\n"
"buffer_load_dwordx4 v114, s[40:43], s72 offen offset:2048 lds\n"
"s_waitcnt lgkmcnt(10)\n"
"v_mfma_f32_32x32x16_bf16 v[160:175], v[96:99], a[72:75], v[160:175]\n"
"ds_read_b64_tr_b16 v[88:89], v122 offset:15552\n"
"ds_read_b64_tr_b16 v[90:91], v122 offset:16832\n"
"v_mfma_f32_32x32x16_bf16 v[176:191], v[100:103], a[72:75], v[176:191]\n"
"ds_read_b128 v[52:55], v127 offset:96\n"
"ds_read_b64_tr_b16 v[92:93], v122 offset:15616\n"
"ds_read_b64_tr_b16 v[94:95], v122 offset:16896\n"
"buffer_load_dwordx4 v115, s[40:43], s72 offen offset:3072 lds\n"
"s_waitcnt lgkmcnt(9)\n"
"v_mfma_f32_32x32x16_bf16 v[192:207], v[104:107], a[72:75], v[192:207]\n"
"v_mfma_f32_16x16x32_bf16 v[44:47], v[48:51], a[72:75], v[44:47]\n"
"v_mfma_f32_32x32x16_bf16 v[128:143], v[108:111], a[76:79], v[128:143]\n"
"s_add_u32 m0, s93, 0x1ac00\n"
"s_nop 0\n"
"buffer_load_dwordx4 v116, s[40:43], s72 offen offset:1024 lds\n"
"s_waitcnt lgkmcnt(5)\n"
"v_mfma_f32_32x32x16_bf16 v[144:159], v[80:83], a[76:79], v[144:159]\n"
"s_waitcnt vmcnt(9) lgkmcnt(0)\n"
"s_barrier\n"
"ds_read_b128 a[80:83], v32 offset:32768\n"
"ds_read_b64_tr_b16 v[96:97], v122 offset:20480\n"
"ds_read_b64_tr_b16 v[98:99], v122 offset:21760\n"
"v_mfma_f32_32x32x16_bf16 v[160:175], v[84:87], a[76:79], v[160:175]\n"
"ds_read_b64_tr_b16 v[100:101], v122 offset:20544\n"
"ds_read_b64_tr_b16 v[102:103], v122 offset:21824\n"
"v_mfma_f32_32x32x16_bf16 v[176:191], v[88:91], a[76:79], v[176:191]\n"
"v_add_u32 v127, s61, v126\n"
"ds_read_b64_tr_b16 v[104:105], v122 offset:20608\n"
"ds_read_b64_tr_b16 v[106:107], v122 offset:21888\n"
"v_mfma_f32_32x32x16_bf16 v[192:207], v[92:95], a[76:79], v[192:207]\n"
"s_add_u32 m0, s81, 0x4000\n"
"ds_read_b64_tr_b16 v[108:109], v122 offset:20672\n"
"ds_read_b64_tr_b16 v[110:111], v122 offset:21952\n"
"buffer_load_dwordx4 v28, s[36:39], s63 offen lds\n"
"v_mfma_f32_16x16x32_bf16 v[44:47], v[52:55], a[76:79], v[44:47]\n"
"s_waitcnt lgkmcnt(4)\n"
"v_mfma_f32_32x32x16_bf16 v[128:143], v[96:99], a[80:83], v[128:143]\n"
"ds_read_b128 v[48:51], v127 offset:0\n"
"ds_read_b64_tr_b16 v[80:81], v122 offset:20736\n"
"ds_read_b64_tr_b16 v[82:83], v122 offset:22016\n"
"v_mfma_f32_32x32x16_bf16 v[144:159], v[100:103], a[80:83], v[144:159]\n"
"ds_read_b128 a[84:87], v33 offset:32768\n"
"ds_read_b64_tr_b16 v[84:85], v122 offset:25600\n"
"ds_read_b64_tr_b16 v[86:87], v122 offset:26880\n"
"ds_read_b64_tr_b16 v[88:89], v122 offset:25664\n"
"ds_read_b64_tr_b16 v[90:91], v122 offset:26944\n"
"buffer_load_dwordx4 v29, s[36:39], s63 offen offset:1024 lds\n"
"s_waitcnt lgkmcnt(8)\n"
"v_mfma_f32_32x32x16_bf16 v[160:175], v[104:107], a[80:83], v[160:175]\n"
"ds_read_b64_tr_b16 v[92:93], v122 offset:25728\n"
"ds_read_b64_tr_b16 v[94:95], v122 offset:27008\n"
"v_mfma_f32_32x32x16_bf16 v[176:191], v[108:111], a[80:83], v[176:191]\n"
"ds_read_b64_tr_b16 v[96:97], v122 offset:25792\n"
"ds_read_b64_tr_b16 v[98:99], v122 offset:27072\n"
"buffer_load_dwordx4 v30, s[36:39], s63 offen offset:2048 lds\n"
"s_waitcnt lgkmcnt(6)\n"
"v_mfma_f32_32x32x16_bf16 v[192:207], v[80:83], a[80:83], v[192:207]\n"
"ds_read_b128 v[52:55], v127 offset:32\n"
"ds_read_b64_tr_b16 v[100:101], v122 offset:25856\n"
"ds_read_b64_tr_b16 v[102:103], v122 offset:27136\n"
"v_mfma_f32_16x16x32_bf16 v[44:47], v[48:51], a[80:83], v[44:47]\n"
"v_mfma_f32_32x32x16_bf16 v[128:143], v[84:87], a[84:87], v[128:143]\n"
"ds_read_b128 a[88:91], v34 offset:32768\n"
"ds_read_b64_tr_b16 v[104:105], v122 offset:30720\n"
"ds_read_b64_tr_b16 v[106:107], v122 offset:32000\n"
"buffer_load_dwordx4 v31, s[36:39], s63 offen offset:3072 lds\n"
"s_waitcnt lgkmcnt(8)\n"
"v_mfma_f32_32x32x16_bf16 v[144:159], v[88:91], a[84:87], v[144:159]\n"
"ds_read_b64_tr_b16 v[108:109], v122 offset:30784\n"
"ds_read_b64_tr_b16 v[110:111], v122 offset:32064\n"
"v_mfma_f32_32x32x16_bf16 v[160:175], v[92:95], a[84:87], v[160:175]\n"
"s_add_u32 m0, s93, 0x10000\n"
"ds_read_b64_tr_b16 v[80:81], v122 offset:30848\n"
"ds_read_b64_tr_b16 v[82:83], v122 offset:32128\n"
"ds_read_b64_tr_b16 v[84:85], v122 offset:30912\n"
"ds_read_b64_tr_b16 v[86:87], v122 offset:32192\n"
"buffer_load_dwordx4 v112, s[40:43], s73 offen lds\n"
"s_waitcnt lgkmcnt(9)\n"
"v_mfma_f32_32x32x16_bf16 v[176:191], v[96:99], a[84:87], v[176:191]\n"
"ds_read_b128 v[48:51], v127 offset:64\n"
"ds_read_b64_tr_b16 v[88:89], v122 offset:30976\n"
"ds_read_b64_tr_b16 v[90:91], v122 offset:32256\n"
"v_mfma_f32_32x32x16_bf16 v[192:207], v[100:103], a[84:87], v[192:207]\n"
"ds_read_b128 a[92:95], v35 offset:32768\n"
"ds_read_b64_tr_b16 v[92:93], v122 offset:35840\n"
"ds_read_b64_tr_b16 v[94:95], v122 offset:37120\n"
"buffer_load_dwordx4 v113, s[40:43], s73 offen offset:1024 lds\n"
"v_mfma_f32_16x16x32_bf16 v[44:47], v[52:55], a[84:87], v[44:47]\n"
"s_waitcnt lgkmcnt(10)\n"
"v_mfma_f32_32x32x16_bf16 v[128:143], v[104:107], a[88:91], v[128:143]\n"
"ds_read_b64_tr_b16 v[96:97], v122 offset:35904\n"
"ds_read_b64_tr_b16 v[98:99], v122 offset:37184\n"
"v_mfma_f32_32x32x16_bf16 v[144:159], v[108:111], a[88:91], v[144:159]\n"
"ds_read_b64_tr_b16 v[100:101], v122 offset:35968\n"
"ds_read_b64_tr_b16 v[102:103], v122 offset:37248\n"
"buffer_load_dwordx4 v114, s[40:43], s73 offen offset:2048 lds\n"
"s_waitcnt lgkmcnt(10)\n"
"v_mfma_f32_32x32x16_bf16 v[160:175], v[80:83], a[88:91], v[160:175]\n"
"ds_read_b64_tr_b16 v[104:105], v122 offset:36032\n"
"ds_read_b64_tr_b16 v[106:107], v122 offset:37312\n"
"v_mfma_f32_32x32x16_bf16 v[176:191], v[84:87], a[88:91], v[176:191]\n"
"ds_read_b128 v[52:55], v127 offset:96\n"
"ds_read_b64_tr_b16 v[108:109], v122 offset:36096\n"
"ds_read_b64_tr_b16 v[110:111], v122 offset:37376\n"
"buffer_load_dwordx4 v115, s[40:43], s73 offen offset:3072 lds\n"
"s_waitcnt lgkmcnt(9)\n"
"v_mfma_f32_32x32x16_bf16 v[192:207], v[88:91], a[88:91], v[192:207]\n"
"v_mfma_f32_16x16x32_bf16 v[44:47], v[48:51], a[88:91], v[44:47]\n"
"v_mfma_f32_32x32x16_bf16 v[128:143], v[92:95], a[92:95], v[128:143]\n"
"s_add_u32 m0, s93, 0x10c00\n"
"s_nop 0\n"
"buffer_load_dwordx4 v116, s[40:43], s73 offen offset:1024 lds\n"
"s_waitcnt lgkmcnt(5)\n"
"v_mfma_f32_32x32x16_bf16 v[144:159], v[96:99], a[92:95], v[144:159]\n"
"s_waitcnt vmcnt(9) lgkmcnt(0)\n"
"s_barrier\n"
"ds_read_b128 a[96:99], v32 offset:0\n"
"ds_read_b64_tr_b16 v[80:81], v122 offset:40960\n"
"ds_read_b64_tr_b16 v[82:83], v122 offset:42240\n"
"v_mfma_f32_32x32x16_bf16 v[160:175], v[100:103], a[92:95], v[160:175]\n"
"ds_read_b64_tr_b16 v[84:85], v122 offset:41024\n"
"ds_read_b64_tr_b16 v[86:87], v122 offset:42304\n"
"v_mfma_f32_32x32x16_bf16 v[176:191], v[104:107], a[92:95], v[176:191]\n"
"v_add_u32 v127, s62, v126\n"
"ds_read_b64_tr_b16 v[88:89], v122 offset:41088\n"
"ds_read_b64_tr_b16 v[90:91], v122 offset:42368\n"
"v_mfma_f32_32x32x16_bf16 v[192:207], v[108:111], a[92:95], v[192:207]\n"
"s_add_u32 m0, s81, 0x8000\n"
"ds_read_b64_tr_b16 v[92:93], v122 offset:41152\n"
"ds_read_b64_tr_b16 v[94:95], v122 offset:42432\n"
"buffer_load_dwordx4 v28, s[36:39], s64 offen lds\n"
"v_mfma_f32_16x16x32_bf16 v[44:47], v[52:55], a[92:95], v[44:47]\n"
"s_waitcnt lgkmcnt(4)\n"
"v_mfma_f32_32x32x16_bf16 v[128:143], v[80:83], a[96:99], v[128:143]\n"
"ds_read_b128 v[48:51], v127 offset:0\n"
"ds_read_b64_tr_b16 v[96:97], v122 offset:41216\n"
"ds_read_b64_tr_b16 v[98:99], v122 offset:42496\n"
"v_mfma_f32_32x32x16_bf16 v[144:159], v[84:87], a[96:99], v[144:159]\n"
"ds_read_b128 a[100:103], v33 offset:0\n"
"ds_read_b64_tr_b16 v[100:101], v122 offset:46080\n"
"ds_read_b64_tr_b16 v[102:103], v122 offset:47360\n"
"ds_read_b64_tr_b16 v[104:105], v122 offset:46144\n"
"ds_read_b64_tr_b16 v[106:107], v122 offset:47424\n"
"buffer_load_dwordx4 v29, s[36:39], s64 offen offset:1024 lds\n"
"s_waitcnt lgkmcnt(8)\n"
"v_mfma_f32_32x32x16_bf16 v[160:175], v[88:91], a[96:99], v[160:175]\n"
"ds_read_b64_tr_b16 v[108:109], v122 offset:46208\n"
"ds_read_b64_tr_b16 v[110:111], v122 offset:47488\n"
"v_mfma_f32_32x32x16_bf16 v[176:191], v[92:95], a[96:99], v[176:191]\n"
"ds_read_b64_tr_b16 v[80:81], v122 offset:46272\n"
"ds_read_b64_tr_b16 v[82:83], v122 offset:47552\n"
"buffer_load_dwordx4 v30, s[36:39], s64 offen offset:2048 lds\n"
"s_waitcnt lgkmcnt(6)\n"
"v_mfma_f32_32x32x16_bf16 v[192:207], v[96:99], a[96:99], v[192:207]\n"
"ds_read_b128 v[52:55], v127 offset:32\n"
"ds_read_b64_tr_b16 v[84:85], v122 offset:46336\n"
"ds_read_b64_tr_b16 v[86:87], v122 offset:47616\n"
"v_mfma_f32_16x16x32_bf16 v[44:47], v[48:51], a[96:99], v[44:47]\n"
"v_mfma_f32_32x32x16_bf16 v[128:143], v[100:103], a[100:103], v[128:143]\n"
"ds_read_b128 a[104:107], v34 offset:0\n"
"ds_read_b64_tr_b16 v[88:89], v122 offset:51200\n"
"ds_read_b64_tr_b16 v[90:91], v122 offset:52480\n"
"buffer_load_dwordx4 v31, s[36:39], s64 offen offset:3072 lds\n"
"s_waitcnt lgkmcnt(8)\n"
"v_mfma_f32_32x32x16_bf16 v[144:159], v[104:107], a[100:103], v[144:159]\n"
"ds_read_b64_tr_b16 v[92:93], v122 offset:51264\n"
"ds_read_b64_tr_b16 v[94:95], v122 offset:52544\n"
"v_mfma_f32_32x32x16_bf16 v[160:175], v[108:111], a[100:103], v[160:175]\n"
"s_add_u32 m0, s93, 0x15000\n"
"ds_read_b64_tr_b16 v[96:97], v122 offset:51328\n"
"ds_read_b64_tr_b16 v[98:99], v122 offset:52608\n"
"ds_read_b64_tr_b16 v[100:101], v122 offset:51392\n"
"ds_read_b64_tr_b16 v[102:103], v122 offset:52672\n"
"buffer_load_dwordx4 v112, s[40:43], s74 offen lds\n"
"s_waitcnt lgkmcnt(9)\n"
"v_mfma_f32_32x32x16_bf16 v[176:191], v[80:83], a[100:103], v[176:191]\n"
"ds_read_b128 v[48:51], v127 offset:64\n"
"ds_read_b64_tr_b16 v[104:105], v122 offset:51456\n"
"ds_read_b64_tr_b16 v[106:107], v122 offset:52736\n"
"v_mfma_f32_32x32x16_bf16 v[192:207], v[84:87], a[100:103], v[192:207]\n"
"ds_read_b128 a[108:111], v35 offset:0\n"
"ds_read_b64_tr_b16 v[108:109], v122 offset:56320\n"
"ds_read_b64_tr_b16 v[110:111], v122 offset:57600\n"
"buffer_load_dwordx4 v113, s[40:43], s74 offen offset:1024 lds\n"
"v_mfma_f32_16x16x32_bf16 v[44:47], v[52:55], a[100:103], v[44:47]\n"
"s_waitcnt lgkmcnt(10)\n"
"v_mfma_f32_32x32x16_bf16 v[128:143], v[88:91], a[104:107], v[128:143]\n"
"ds_read_b64_tr_b16 v[80:81], v122 offset:56384\n"
"ds_read_b64_tr_b16 v[82:83], v122 offset:57664\n"
"v_mfma_f32_32x32x16_bf16 v[144:159], v[92:95], a[104:107], v[144:159]\n"
"ds_read_b64_tr_b16 v[84:85], v122 offset:56448\n"
"ds_read_b64_tr_b16 v[86:87], v122 offset:57728\n"
"buffer_load_dwordx4 v114, s[40:43], s74 offen offset:2048 lds\n"
"s_waitcnt lgkmcnt(10)\n"
"v_mfma_f32_32x32x16_bf16 v[160:175], v[96:99], a[104:107], v[160:175]\n"
"ds_read_b64_tr_b16 v[88:89], v122 offset:56512\n"
"ds_read_b64_tr_b16 v[90:91], v122 offset:57792\n"
"v_mfma_f32_32x32x16_bf16 v[176:191], v[100:103], a[104:107], v[176:191]\n"
"ds_read_b128 v[52:55], v127 offset:96\n"
"ds_read_b64_tr_b16 v[92:93], v122 offset:56576\n"
"ds_read_b64_tr_b16 v[94:95], v122 offset:57856\n"
"buffer_load_dwordx4 v115, s[40:43], s74 offen offset:3072 lds\n"
"s_waitcnt lgkmcnt(9)\n"
"v_mfma_f32_32x32x16_bf16 v[192:207], v[104:107], a[104:107], v[192:207]\n"
"v_mfma_f32_16x16x32_bf16 v[44:47], v[48:51], a[104:107], v[44:47]\n"
"v_mfma_f32_32x32x16_bf16 v[128:143], v[108:111], a[108:111], v[128:143]\n"
"s_add_u32 m0, s93, 0x15c00\n"
"s_nop 0\n"
"buffer_load_dwordx4 v116, s[40:43], s74 offen offset:1024 lds\n"
"s_waitcnt lgkmcnt(5)\n"
"v_mfma_f32_32x32x16_bf16 v[144:159], v[80:83], a[108:111], v[144:159]\n"
"s_waitcnt vmcnt(9) lgkmcnt(0)\n"
"s_barrier\n"
"ds_read_b128 a[112:115], v32 offset:16384\n"
"ds_read_b64_tr_b16 v[96:97], v122 offset:0\n"
"ds_read_b64_tr_b16 v[98:99], v122 offset:1280\n"
"v_mfma_f32_32x32x16_bf16 v[160:175], v[84:87], a[108:111], v[160:175]\n"
"ds_read_b64_tr_b16 v[100:101], v122 offset:64\n"
"ds_read_b64_tr_b16 v[102:103], v122 offset:1344\n"
"v_mfma_f32_32x32x16_bf16 v[176:191], v[88:91], a[108:111], v[176:191]\n"
"v_add_u32 v127, s63, v126\n"
"ds_read_b64_tr_b16 v[104:105], v122 offset:128\n"
"ds_read_b64_tr_b16 v[106:107], v122 offset:1408\n"
"v_mfma_f32_32x32x16_bf16 v[192:207], v[92:95], a[108:111], v[192:207]\n"
"s_add_u32 m0, s81, 0x0\n"
"ds_read_b64_tr_b16 v[108:109], v122 offset:192\n"
"ds_read_b64_tr_b16 v[110:111], v122 offset:1472\n"
"buffer_load_dwordx4 v28, s[36:39], s65 offen lds\n"
"v_mfma_f32_16x16x32_bf16 v[44:47], v[52:55], a[108:111], v[44:47]\n"
"s_waitcnt lgkmcnt(4)\n"
"v_mfma_f32_32x32x16_bf16 v[128:143], v[96:99], a[112:115], v[128:143]\n"
"ds_read_b128 v[48:51], v127 offset:0\n"
"ds_read_b64_tr_b16 v[80:81], v122 offset:256\n"
"ds_read_b64_tr_b16 v[82:83], v122 offset:1536\n"
"v_mfma_f32_32x32x16_bf16 v[144:159], v[100:103], a[112:115], v[144:159]\n"
"ds_read_b128 a[116:119], v33 offset:16384\n"
"ds_read_b64_tr_b16 v[84:85], v122 offset:5120\n"
"ds_read_b64_tr_b16 v[86:87], v122 offset:6400\n"
"ds_read_b64_tr_b16 v[88:89], v122 offset:5184\n"
"ds_read_b64_tr_b16 v[90:91], v122 offset:6464\n"
"buffer_load_dwordx4 v29, s[36:39], s65 offen offset:1024 lds\n"
"s_waitcnt lgkmcnt(8)\n"
"v_mfma_f32_32x32x16_bf16 v[160:175], v[104:107], a[112:115], v[160:175]\n"
"ds_read_b64_tr_b16 v[92:93], v122 offset:5248\n"
"ds_read_b64_tr_b16 v[94:95], v122 offset:6528\n"
"v_mfma_f32_32x32x16_bf16 v[176:191], v[108:111], a[112:115], v[176:191]\n"
"ds_read_b64_tr_b16 v[96:97], v122 offset:5312\n"
"ds_read_b64_tr_b16 v[98:99], v122 offset:6592\n"
"buffer_load_dwordx4 v30, s[36:39], s65 offen offset:2048 lds\n"
"s_waitcnt lgkmcnt(6)\n"
"v_mfma_f32_32x32x16_bf16 v[192:207], v[80:83], a[112:115], v[192:207]\n"
"ds_read_b128 v[52:55], v127 offset:32\n"
"ds_read_b64_tr_b16 v[100:101], v122 offset:5376\n"
"ds_read_b64_tr_b16 v[102:103], v122 offset:6656\n"
"v_mfma_f32_16x16x32_bf16 v[44:47], v[48:51], a[112:115], v[44:47]\n"
"v_mfma_f32_32x32x16_bf16 v[128:143], v[84:87], a[116:119], v[128:143]\n"
"ds_read_b128 a[120:123], v34 offset:16384\n"
"ds_read_b64_tr_b16 v[104:105], v122 offset:10240\n"
"ds_read_b64_tr_b16 v[106:107], v122 offset:11520\n"
"buffer_load_dwordx4 v31, s[36:39], s65 offen offset:3072 lds\n"
"s_waitcnt lgkmcnt(8)\n"
"v_mfma_f32_32x32x16_bf16 v[144:159], v[88:91], a[116:119], v[144:159]\n"
"ds_read_b64_tr_b16 v[108:109], v122 offset:10304\n"
"ds_read_b64_tr_b16 v[110:111], v122 offset:11584\n"
"v_mfma_f32_32x32x16_bf16 v[160:175], v[92:95], a[116:119], v[160:175]\n"
"s_add_u32 m0, s93, 0x1a000\n"
"ds_read_b64_tr_b16 v[80:81], v122 offset:10368\n"
"ds_read_b64_tr_b16 v[82:83], v122 offset:11648\n"
"ds_read_b64_tr_b16 v[84:85], v122 offset:10432\n"
"ds_read_b64_tr_b16 v[86:87], v122 offset:11712\n"
"buffer_load_dwordx4 v112, s[40:43], s75 offen lds\n"
"s_waitcnt lgkmcnt(9)\n"
"v_mfma_f32_32x32x16_bf16 v[176:191], v[96:99], a[116:119], v[176:191]\n"
"ds_read_b128 v[48:51], v127 offset:64\n"
"ds_read_b64_tr_b16 v[88:89], v122 offset:10496\n"
"ds_read_b64_tr_b16 v[90:91], v122 offset:11776\n"
"v_mfma_f32_32x32x16_bf16 v[192:207], v[100:103], a[116:119], v[192:207]\n"
"ds_read_b128 a[124:127], v35 offset:16384\n"
"ds_read_b64_tr_b16 v[92:93], v122 offset:15360\n"
"ds_read_b64_tr_b16 v[94:95], v122 offset:16640\n"
"buffer_load_dwordx4 v113, s[40:43], s75 offen offset:1024 lds\n"
"v_mfma_f32_16x16x32_bf16 v[44:47], v[52:55], a[116:119], v[44:47]\n"
"s_waitcnt lgkmcnt(10)\n"
"v_mfma_f32_32x32x16_bf16 v[128:143], v[104:107], a[120:123], v[128:143]\n"
"ds_read_b64_tr_b16 v[96:97], v122 offset:15424\n"
"ds_read_b64_tr_b16 v[98:99], v122 offset:16704\n"
"v_mfma_f32_32x32x16_bf16 v[144:159], v[108:111], a[120:123], v[144:159]\n"
"ds_read_b64_tr_b16 v[100:101], v122 offset:15488\n"
"ds_read_b64_tr_b16 v[102:103], v122 offset:16768\n"
"buffer_load_dwordx4 v114, s[40:43], s75 offen offset:2048 lds\n"
"s_waitcnt lgkmcnt(10)\n"
"v_mfma_f32_32x32x16_bf16 v[160:175], v[80:83], a[120:123], v[160:175]\n"
"ds_read_b64_tr_b16 v[104:105], v122 offset:15552\n"
"ds_read_b64_tr_b16 v[106:107], v122 offset:16832\n"
"v_mfma_f32_32x32x16_bf16 v[176:191], v[84:87], a[120:123], v[176:191]\n"
"ds_read_b128 v[52:55], v127 offset:96\n"
"ds_read_b64_tr_b16 v[108:109], v122 offset:15616\n"
"ds_read_b64_tr_b16 v[110:111], v122 offset:16896\n"
"buffer_load_dwordx4 v115, s[40:43], s75 offen offset:3072 lds\n"
"s_waitcnt lgkmcnt(9)\n"
"v_mfma_f32_32x32x16_bf16 v[192:207], v[88:91], a[120:123], v[192:207]\n"
"v_mfma_f32_16x16x32_bf16 v[44:47], v[48:51], a[120:123], v[44:47]\n"
"v_mfma_f32_32x32x16_bf16 v[128:143], v[92:95], a[124:127], v[128:143]\n"
"s_add_u32 m0, s93, 0x1ac00\n"
"s_nop 0\n"
"buffer_load_dwordx4 v116, s[40:43], s75 offen offset:1024 lds\n"
"s_waitcnt lgkmcnt(5)\n"
"v_mfma_f32_32x32x16_bf16 v[144:159], v[96:99], a[124:127], v[144:159]\n"
"s_waitcnt vmcnt(9) lgkmcnt(0)\n"
"s_barrier\n"
"ds_read_b128 a[128:131], v32 offset:32768\n"
"ds_read_b64_tr_b16 v[80:81], v122 offset:20480\n"
"ds_read_b64_tr_b16 v[82:83], v122 offset:21760\n"
"v_mfma_f32_32x32x16_bf16 v[160:175], v[100:103], a[124:127], v[160:175]\n"
"ds_read_b64_tr_b16 v[84:85], v122 offset:20544\n"
"ds_read_b64_tr_b16 v[86:87], v122 offset:21824\n"
"s_cmp_ge_u32 s76, 2\n"
"s_cselect_b32 s66, s67, s66\n"
"s_cselect_b32 s67, s69, s68\n"
"s_cselect_b32 s68, s71, s70\n"
"s_cselect_b32 s69, s73, s72\n"
"s_cselect_b32 s70, s75, s74\n"
"v_mfma_f32_32x32x16_bf16 v[176:191], v[104:107], a[124:127], v[176:191]\n"
"v_add_u32 v127, s64, v126\n"
"ds_read_b64_tr_b16 v[88:89], v122 offset:20608\n"
"ds_read_b64_tr_b16 v[90:91], v122 offset:21888\n"
"v_mfma_f32_32x32x16_bf16 v[192:207], v[108:111], a[124:127], v[192:207]\n"
"s_add_u32 m0, s93, 0x10000\n"
"ds_read_b64_tr_b16 v[92:93], v122 offset:20672\n"
"ds_read_b64_tr_b16 v[94:95], v122 offset:21952\n"
"buffer_load_dwordx4 v117, s[40:43], s66 offen lds\n"
"v_mfma_f32_16x16x32_bf16 v[44:47], v[52:55], a[124:127], v[44:47]\n"
"s_waitcnt lgkmcnt(4)\n"
"v_mfma_f32_32x32x16_bf16 v[128:143], v[80:83], a[128:131], v[128:143]\n"
"ds_read_b128 v[48:51], v127 offset:0\n"
"ds_read_b64_tr_b16 v[96:97], v122 offset:20736\n"
"ds_read_b64_tr_b16 v[98:99], v122 offset:22016\n"
"v_mfma_f32_32x32x16_bf16 v[144:159], v[84:87], a[128:131], v[144:159]\n"
"ds_read_b128 a[132:135], v33 offset:32768\n"
"ds_read_b64_tr_b16 v[100:101], v122 offset:25600\n"
"ds_read_b64_tr_b16 v[102:103], v122 offset:26880\n"
"ds_read_b64_tr_b16 v[104:105], v122 offset:25664\n"
"ds_read_b64_tr_b16 v[106:107], v122 offset:26944\n"
"buffer_load_dwordx4 v118, s[40:43], s66 offen offset:1024 lds\n"
"s_waitcnt lgkmcnt(8)\n"
"v_mfma_f32_32x32x16_bf16 v[160:175], v[88:91], a[128:131], v[160:175]\n"
"ds_read_b64_tr_b16 v[108:109], v122 offset:25728\n"
"ds_read_b64_tr_b16 v[110:111], v122 offset:27008\n"
"s_mul_i32 s82, s89, 378880\n"
"s_add_u32 s82, s82, 0x100000\n"
"s_mul_i32 s83, s76, 61440\n"
"v_mfma_f32_32x32x16_bf16 v[176:191], v[92:95], a[128:131], v[176:191]\n"
"ds_read_b64_tr_b16 v[80:81], v122 offset:25792\n"
"ds_read_b64_tr_b16 v[82:83], v122 offset:27072\n"
"s_add_u32 s4, s82, s83\n"
"s_bitcmp1_b32 s0, 1\n"
"s_cselect_b32 s83, 30720, 0\n"
"s_waitcnt lgkmcnt(6)\n"
"v_mfma_f32_32x32x16_bf16 v[192:207], v[96:99], a[128:131], v[192:207]\n"
"ds_read_b128 v[52:55], v127 offset:32\n"
"ds_read_b64_tr_b16 v[84:85], v122 offset:25856\n"
"ds_read_b64_tr_b16 v[86:87], v122 offset:27136\n"
"s_add_u32 s79, s4, 30720\n"
"s_sub_u32 s79, s79, s83\n"
"v_mfma_f32_16x16x32_bf16 v[44:47], v[48:51], a[128:131], v[44:47]\n"
"v_mfma_f32_32x32x16_bf16 v[128:143], v[100:103], a[132:135], v[128:143]\n"
"ds_read_b128 a[136:139], v34 offset:32768\n"
"ds_read_b64_tr_b16 v[88:89], v122 offset:30720\n"
"ds_read_b64_tr_b16 v[90:91], v122 offset:32000\n"
"buffer_load_dwordx4 v119, s[40:43], s66 offen offset:2048 lds\n"
"s_waitcnt lgkmcnt(8)\n"
"v_mfma_f32_32x32x16_bf16 v[144:159], v[104:107], a[132:135], v[144:159]\n"
"ds_read_b64_tr_b16 v[92:93], v122 offset:30784\n"
"ds_read_b64_tr_b16 v[94:95], v122 offset:32064\n"
"s_add_u32 s4, s4, s83\n"
"s_mul_i32 s83, s76, 10240\n"
"s_add_u32 s83, s83, s82\n"
"v_mfma_f32_32x32x16_bf16 v[160:175], v[108:111], a[132:135], v[160:175]\n"
"ds_read_b64_tr_b16 v[96:97], v122 offset:30848\n"
"ds_read_b64_tr_b16 v[98:99], v122 offset:32128\n"
"ds_read_b64_tr_b16 v[100:101], v122 offset:30912\n"
"ds_read_b64_tr_b16 v[102:103], v122 offset:32192\n"
"s_add_u32 s5, s83, 245760\n"
"s_waitcnt lgkmcnt(9)\n"
"v_mfma_f32_32x32x16_bf16 v[176:191], v[80:83], a[132:135], v[176:191]\n"
"ds_read_b128 v[48:51], v127 offset:64\n"
"ds_read_b64_tr_b16 v[104:105], v122 offset:30976\n"
"ds_read_b64_tr_b16 v[106:107], v122 offset:32256\n"
"s_mul_i32 s83, s76, 25600\n"
"s_add_u32 s83, s83, s82\n"
"v_mfma_f32_32x32x16_bf16 v[192:207], v[84:87], a[132:135], v[192:207]\n"
"ds_read_b128 a[140:143], v35 offset:32768\n"
"ds_read_b64_tr_b16 v[108:109], v122 offset:35840\n"
"ds_read_b64_tr_b16 v[110:111], v122 offset:37120\n"
"buffer_load_dwordx4 v120, s[40:43], s66 offen offset:3072 lds\n"
"v_mfma_f32_16x16x32_bf16 v[44:47], v[52:55], a[132:135], v[44:47]\n"
"s_waitcnt lgkmcnt(10)\n"
"v_mfma_f32_32x32x16_bf16 v[128:143], v[88:91], a[136:139], v[128:143]\n"
"ds_read_b64_tr_b16 v[80:81], v122 offset:35904\n"
"ds_read_b64_tr_b16 v[82:83], v122 offset:37184\n"
"s_add_u32 s6, s83, 276480\n"
"s_bitcmp1_b32 s0, 0\n"
"s_cselect_b32 s83, 5120, 0\n"
"v_mfma_f32_32x32x16_bf16 v[144:159], v[92:95], a[136:139], v[144:159]\n"
"ds_read_b64_tr_b16 v[84:85], v122 offset:35968\n"
"ds_read_b64_tr_b16 v[86:87], v122 offset:37248\n"
"s_lshl_b32 s1, s83, 1\n"
"s_sub_u32 s1, 5120, s1\n"
"s_add_u32 s4, s4, s83\n"
"s_waitcnt lgkmcnt(10)\n"
"v_mfma_f32_32x32x16_bf16 v[160:175], v[96:99], a[136:139], v[160:175]\n"
"ds_read_b64_tr_b16 v[88:89], v122 offset:36032\n"
"ds_read_b64_tr_b16 v[90:91], v122 offset:37312\n"
"s_add_u32 s79, s79, s83\n"
"s_add_u32 s5, s5, s83\n"
"s_add_u32 s12, s6, 25600\n"
"v_mfma_f32_32x32x16_bf16 v[176:191], v[100:103], a[136:139], v[176:191]\n"
"s_add_u32 m0, s93, 0x10c00\n"
"ds_read_b128 v[52:55], v127 offset:96\n"
"ds_read_b64_tr_b16 v[92:93], v122 offset:36096\n"
"ds_read_b64_tr_b16 v[94:95], v122 offset:37376\n"
"buffer_load_dwordx4 v121, s[40:43], s66 offen offset:1024 lds\n"
"s_waitcnt lgkmcnt(9)\n"
"v_mfma_f32_32x32x16_bf16 v[192:207], v[104:107], a[136:139], v[192:207]\n"
"s_mul_i32 s83, s0, 5120\n"
"s_add_u32 s6, s6, s83\n"
"s_cmp_eq_u32 s76, 3\n"
"s_cselect_b32 s5, 0x40000000, s5\n"
"s_mul_i32 s96, s76, 10240\n"
"v_mfma_f32_16x16x32_bf16 v[44:47], v[48:51], a[136:139], v[44:47]\n"
"v_mfma_f32_32x32x16_bf16 v[128:143], v[108:111], a[140:143], v[128:143]\n"
"s_add_u32 s96, s96, 106496\n"
"s_mul_i32 s99, s76, 13312\n"
"s_add_u32 s99, s99, 10240\n"
"s_mul_i32 s13, s76, 10240\n"
"s_add_u32 s13, s13, 65536\n"
"s_lshr_b32 s82, s89, 2\n"
"s_waitcnt lgkmcnt(5)\n"
"v_mfma_f32_32x32x16_bf16 v[144:159], v[80:83], a[140:143], v[144:159]\n"
"s_waitcnt vmcnt(5) lgkmcnt(0)\n"
"s_barrier\n"
"ds_read_b128 a[144:147], v32 offset:0\n"
"ds_read_b64_tr_b16 v[96:97], v122 offset:40960\n"
"ds_read_b64_tr_b16 v[98:99], v122 offset:42240\n"
"v_mfma_f32_32x32x16_bf16 v[160:175], v[84:87], a[140:143], v[160:175]\n"
"ds_read_b64_tr_b16 v[100:101], v122 offset:41024\n"
"ds_read_b64_tr_b16 v[102:103], v122 offset:42304\n"
"s_lshl_b32 s82, s82, 10\n"
"s_add_u32 s2, s82, 10240\n"
"s_cmp_eq_u32 s76, 0\n"
"s_cselect_b32 s94, -1, 0\n"
"s_cselect_b32 s95, 0x1f, 0\n"
"s_cselect_b32 s2, s2, 0x40000000\n"
"v_mfma_f32_32x32x16_bf16 v[176:191], v[88:91], a[140:143], v[176:191]\n"
"v_add_u32 v127, s65, v126\n"
"ds_read_b64_tr_b16 v[104:105], v122 offset:41088\n"
"ds_read_b64_tr_b16 v[106:107], v122 offset:42368\n"
"v_mfma_f32_32x32x16_bf16 v[192:207], v[92:95], a[140:143], v[192:207]\n"
"s_add_u32 m0, s93, 0x15000\n"
"ds_read_b64_tr_b16 v[108:109], v122 offset:41152\n"
"ds_read_b64_tr_b16 v[110:111], v122 offset:42432\n"
"buffer_load_dwordx4 v117, s[40:43], s67 offen lds\n"
"v_mfma_f32_16x16x32_bf16 v[44:47], v[52:55], a[140:143], v[44:47]\n"
"s_waitcnt lgkmcnt(4)\n"
"v_mfma_f32_32x32x16_bf16 v[128:143], v[96:99], a[144:147], v[128:143]\n"
"ds_read_b128 v[48:51], v127 offset:0\n"
"ds_read_b64_tr_b16 v[80:81], v122 offset:41216\n"
"ds_read_b64_tr_b16 v[82:83], v122 offset:42496\n"
"v_mfma_f32_32x32x16_bf16 v[144:159], v[100:103], a[144:147], v[144:159]\n"
"ds_read_b128 a[148:151], v33 offset:0\n"
"ds_read_b64_tr_b16 v[84:85], v122 offset:46080\n"
"ds_read_b64_tr_b16 v[86:87], v122 offset:47360\n"
"ds_read_b64_tr_b16 v[88:89], v122 offset:46144\n"
"ds_read_b64_tr_b16 v[90:91], v122 offset:47424\n"
"buffer_load_dwordx4 v118, s[40:43], s67 offen offset:1024 lds\n"
"s_waitcnt lgkmcnt(8)\n"
"v_mfma_f32_32x32x16_bf16 v[160:175], v[104:107], a[144:147], v[160:175]\n"
"ds_read_b64_tr_b16 v[92:93], v122 offset:46208\n"
"ds_read_b64_tr_b16 v[94:95], v122 offset:47488\n"
"v_mfma_f32_32x32x16_bf16 v[176:191], v[108:111], a[144:147], v[176:191]\n"
"ds_read_b64_tr_b16 v[96:97], v122 offset:46272\n"
"ds_read_b64_tr_b16 v[98:99], v122 offset:47552\n"
"s_waitcnt lgkmcnt(6)\n"
"v_mfma_f32_32x32x16_bf16 v[192:207], v[80:83], a[144:147], v[192:207]\n"
"ds_read_b128 v[52:55], v127 offset:32\n"
"ds_read_b64_tr_b16 v[100:101], v122 offset:46336\n"
"ds_read_b64_tr_b16 v[102:103], v122 offset:47616\n"
"v_mfma_f32_16x16x32_bf16 v[44:47], v[48:51], a[144:147], v[44:47]\n"
"v_mfma_f32_32x32x16_bf16 v[128:143], v[84:87], a[148:151], v[128:143]\n"
"ds_read_b128 a[152:155], v34 offset:0\n"
"ds_read_b64_tr_b16 v[104:105], v122 offset:51200\n"
"ds_read_b64_tr_b16 v[106:107], v122 offset:52480\n"
"buffer_load_dwordx4 v119, s[40:43], s67 offen offset:2048 lds\n"
"s_waitcnt lgkmcnt(8)\n"
"v_mfma_f32_32x32x16_bf16 v[144:159], v[88:91], a[148:151], v[144:159]\n"
"ds_read_b64_tr_b16 v[108:109], v122 offset:51264\n"
"ds_read_b64_tr_b16 v[110:111], v122 offset:52544\n"
"v_mfma_f32_32x32x16_bf16 v[160:175], v[92:95], a[148:151], v[160:175]\n"
"ds_read_b64_tr_b16 v[80:81], v122 offset:51328\n"
"ds_read_b64_tr_b16 v[82:83], v122 offset:52608\n"
"ds_read_b64_tr_b16 v[84:85], v122 offset:51392\n"
"ds_read_b64_tr_b16 v[86:87], v122 offset:52672\n"
"s_waitcnt lgkmcnt(9)\n"
"v_mfma_f32_32x32x16_bf16 v[176:191], v[96:99], a[148:151], v[176:191]\n"
"ds_read_b128 v[48:51], v127 offset:64\n"
"ds_read_b64_tr_b16 v[88:89], v122 offset:51456\n"
"ds_read_b64_tr_b16 v[90:91], v122 offset:52736\n"
"v_mfma_f32_32x32x16_bf16 v[192:207], v[100:103], a[148:151], v[192:207]\n"
"ds_read_b128 a[156:159], v35 offset:0\n"
"ds_read_b64_tr_b16 v[92:93], v122 offset:56320\n"
"ds_read_b64_tr_b16 v[94:95], v122 offset:57600\n"
"buffer_load_dwordx4 v120, s[40:43], s67 offen offset:3072 lds\n"
"v_mfma_f32_16x16x32_bf16 v[44:47], v[52:55], a[148:151], v[44:47]\n"
"s_waitcnt lgkmcnt(10)\n"
"v_mfma_f32_32x32x16_bf16 v[128:143], v[104:107], a[152:155], v[128:143]\n"
"ds_read_b64_tr_b16 v[96:97], v122 offset:56384\n"
"ds_read_b64_tr_b16 v[98:99], v122 offset:57664\n"
"v_mfma_f32_32x32x16_bf16 v[144:159], v[108:111], a[152:155], v[144:159]\n"
"ds_read_b64_tr_b16 v[100:101], v122 offset:56448\n"
"ds_read_b64_tr_b16 v[102:103], v122 offset:57728\n"
"s_waitcnt lgkmcnt(10)\n"
"v_mfma_f32_32x32x16_bf16 v[160:175], v[80:83], a[152:155], v[160:175]\n"
"ds_read_b64_tr_b16 v[104:105], v122 offset:56512\n"
"ds_read_b64_tr_b16 v[106:107], v122 offset:57792\n"
"v_mfma_f32_32x32x16_bf16 v[176:191], v[84:87], a[152:155], v[176:191]\n"
"s_add_u32 m0, s93, 0x15c00\n"
"ds_read_b128 v[52:55], v127 offset:96\n"
"ds_read_b64_tr_b16 v[108:109], v122 offset:56576\n"
"ds_read_b64_tr_b16 v[110:111], v122 offset:57856\n"
"buffer_load_dwordx4 v121, s[40:43], s67 offen offset:1024 lds\n"
"s_waitcnt lgkmcnt(9)\n"
"v_mfma_f32_32x32x16_bf16 v[192:207], v[88:91], a[152:155], v[192:207]\n"
"v_mfma_f32_16x16x32_bf16 v[44:47], v[48:51], a[152:155], v[44:47]\n"
"v_mfma_f32_32x32x16_bf16 v[128:143], v[92:95], a[156:159], v[128:143]\n"
"s_waitcnt lgkmcnt(5)\n"
"v_mfma_f32_32x32x16_bf16 v[144:159], v[96:99], a[156:159], v[144:159]\n"
"s_waitcnt vmcnt(5) lgkmcnt(0)\n"
"s_barrier\n"
"ds_read_b64_tr_b16 v[80:81], v124 offset:0\n"
"ds_read_b64_tr_b16 v[82:83], v124 offset:160\n"
"v_mfma_f32_32x32x16_bf16 v[160:175], v[100:103], a[156:159], v[160:175]\n"
"ds_read_b64_tr_b16 v[84:85], v124 offset:64\n"
"ds_read_b64_tr_b16 v[86:87], v124 offset:224\n"
"v_mfma_f32_32x32x16_bf16 v[176:191], v[104:107], a[156:159], v[176:191]\n"
"ds_read_b64_tr_b16 v[88:89], v124 offset:128\n"
"ds_read_b64_tr_b16 v[90:91], v124 offset:288\n"
"v_mfma_f32_32x32x16_bf16 v[192:207], v[108:111], a[156:159], v[192:207]\n"
"ds_read_b64_tr_b16 v[92:93], v124 offset:2560\n"
"ds_read_b64_tr_b16 v[94:95], v124 offset:2720\n"
"v_mfma_f32_16x16x32_bf16 v[44:47], v[52:55], a[156:159], v[44:47]\n"
"s_waitcnt lgkmcnt(4)\n"
"v_mfma_f32_32x32x16_bf16 v[208:223], v[80:83], a[0:3], 0\n"
"ds_read_b64_tr_b16 v[96:97], v124 offset:2624\n"
"ds_read_b64_tr_b16 v[98:99], v124 offset:2784\n"
"v_mfma_f32_32x32x16_bf16 v[224:239], v[84:87], a[0:3], 0\n"
"ds_read_b64_tr_b16 v[100:101], v124 offset:2688\n"
"ds_read_b64_tr_b16 v[102:103], v124 offset:2848\n"
"v_and_b32 v48, 15, v1\n"
"v_lshlrev_b32 v48, 2, v48\n"
"ds_bpermute_b32 v49, v48, v44\n"
"ds_bpermute_b32 v50, v48, v45\n"
"v_cvt_pk_bf16_f32 v56, v128, v129\n"
"v_cvt_pk_bf16_f32 v57, v130, v131\n"
"v_cvt_pk_bf16_f32 v58, v132, v133\n"
"v_cvt_pk_bf16_f32 v59, v134, v135\n"
"v_cvt_pk_bf16_f32 v60, v136, v137\n"
"s_waitcnt lgkmcnt(6)\n"
"v_mfma_f32_32x32x16_bf16 v[240:255], v[88:91], a[0:3], 0\n"
"ds_read_b64_tr_b16 v[104:105], v124 offset:5120\n"
"ds_read_b64_tr_b16 v[106:107], v124 offset:5280\n"
"v_cvt_pk_bf16_f32 v61, v138, v139\n"
"v_cvt_pk_bf16_f32 v62, v140, v141\n"
"v_cvt_pk_bf16_f32 v63, v142, v143\n"
"v_cvt_pk_bf16_f32 v64, v144, v145\n"
"v_cvt_pk_bf16_f32 v65, v146, v147\n"
"v_cvt_pk_bf16_f32 v66, v148, v149\n"
"v_mfma_f32_32x32x16_bf16 v[208:223], v[92:95], a[4:7], v[208:223]\n"
"ds_read_b64_tr_b16 v[108:109], v124 offset:5184\n"
"ds_read_b64_tr_b16 v[110:111], v124 offset:5344\n"
"ds_read_b64_tr_b16 v[80:81], v124 offset:5248\n"
"ds_read_b64_tr_b16 v[82:83], v124 offset:5408\n"
"v_cvt_pk_bf16_f32 v67, v150, v151\n"
"v_cvt_pk_bf16_f32 v68, v152, v153\n"
"v_cvt_pk_bf16_f32 v69, v154, v155\n"
"s_waitcnt lgkmcnt(8)\n"
"v_mfma_f32_32x32x16_bf16 v[224:239], v[96:99], a[4:7], v[224:239]\n"
"ds_read_b64_tr_b16 v[84:85], v124 offset:7680\n"
"ds_read_b64_tr_b16 v[86:87], v124 offset:7840\n"
"v_cvt_pk_bf16_f32 v70, v156, v157\n"
"v_cvt_pk_bf16_f32 v71, v158, v159\n"
"v_cvt_pk_bf16_f32 v72, v160, v161\n"
"v_cvt_pk_bf16_f32 v73, v162, v163\n"
"v_cvt_pk_bf16_f32 v74, v164, v165\n"
"v_cvt_pk_bf16_f32 v75, v166, v167\n"
"v_mfma_f32_32x32x16_bf16 v[240:255], v[100:103], a[4:7], v[240:255]\n"
"ds_read_b64_tr_b16 v[88:89], v124 offset:7744\n"
"ds_read_b64_tr_b16 v[90:91], v124 offset:7904\n"
"v_mfma_f32_32x32x16_bf16 v[128:143], a[160:163], v[56:59], 0\n"
"v_cvt_pk_bf16_f32 v168, v168, v169\n"
"v_cvt_pk_bf16_f32 v169, v170, v171\n"
"ds_write_b64 v40, v[168:169] offset:0\n"
"v_mfma_f32_32x32x16_bf16 v[128:143], a[164:167], v[60:63], v[128:143]\n"
"v_cvt_pk_bf16_f32 v172, v172, v173\n"
"v_cvt_pk_bf16_f32 v173, v174, v175\n"
"ds_write_b64 v40, v[172:173] offset:16\n"
"v_mfma_f32_32x32x16_bf16 v[128:143], a[168:171], v[64:67], v[128:143]\n"
"v_cvt_pk_bf16_f32 v176, v176, v177\n"
"v_cvt_pk_bf16_f32 v177, v178, v179\n"
"ds_write_b64 v40, v[176:177] offset:32\n"
"s_waitcnt lgkmcnt(9)\n"
"v_mfma_f32_32x32x16_bf16 v[208:223], v[104:107], a[8:11], v[208:223]\n"
"ds_read_b64_tr_b16 v[92:93], v124 offset:7808\n"
"ds_read_b64_tr_b16 v[94:95], v124 offset:7968\n"
"v_mfma_f32_32x32x16_bf16 v[144:159], a[172:175], v[56:59], 0\n"
"v_cvt_pk_bf16_f32 v180, v180, v181\n"
"v_cvt_pk_bf16_f32 v181, v182, v183\n"
"ds_write_b64 v40, v[180:181] offset:48\n"
"v_mfma_f32_32x32x16_bf16 v[144:159], a[176:179], v[60:63], v[144:159]\n"
"v_cvt_pk_bf16_f32 v184, v184, v185\n"
"v_cvt_pk_bf16_f32 v185, v186, v187\n"
"v_mfma_f32_32x32x16_bf16 v[224:239], v[108:111], a[8:11], v[224:239]\n"
"ds_read_b64_tr_b16 v[96:97], v124 offset:10240\n"
"ds_read_b64_tr_b16 v[98:99], v124 offset:10400\n"
"ds_write_b64 v40, v[184:185] offset:64\n"
"v_mfma_f32_32x32x16_bf16 v[144:159], a[180:183], v[64:67], v[144:159]\n"
"v_cvt_pk_bf16_f32 v188, v188, v189\n"
"v_cvt_pk_bf16_f32 v189, v190, v191\n"
"s_waitcnt lgkmcnt(13)\n"
"ds_write_b64 v40, v[188:189] offset:80\n"
"v_mfma_f32_32x32x16_bf16 v[144:159], a[184:187], v[68:71], v[144:159]\n"
"v_cvt_pk_bf16_f32 v192, v192, v193\n"
"v_cvt_pk_bf16_f32 v193, v194, v195\n"
"ds_write_b64 v40, v[192:193] offset:96\n"
"v_mfma_f32_32x32x16_bf16 v[144:159], a[188:191], v[72:75], v[144:159]\n"
"v_cvt_pk_bf16_f32 v196, v196, v197\n"
"v_cvt_pk_bf16_f32 v197, v198, v199\n"
"s_waitcnt lgkmcnt(13)\n"
"v_mfma_f32_32x32x16_bf16 v[240:255], v[80:83], a[8:11], v[240:255]\n"
"ds_read_b64_tr_b16 v[100:101], v124 offset:10304\n"
"ds_read_b64_tr_b16 v[102:103], v124 offset:10464\n"
"s_waitcnt lgkmcnt(13)\n"
"ds_write_b64 v40, v[196:197] offset:112\n"
"v_mfma_f32_32x32x16_bf16 v[160:175], a[192:195], v[64:67], 0\n"
"v_cvt_pk_bf16_f32 v200, v200, v201\n"
"v_cvt_pk_bf16_f32 v201, v202, v203\n"
"ds_write_b64 v40, v[200:201] offset:128\n"
"v_mfma_f32_32x32x16_bf16 v[160:175], a[196:199], v[68:71], v[160:175]\n"
"v_cvt_pk_bf16_f32 v204, v204, v205\n"
"v_cvt_pk_bf16_f32 v205, v206, v207\n"
"v_mfma_f32_32x32x16_bf16 v[208:223], v[84:87], a[12:15], v[208:223]\n"
"s_waitcnt lgkmcnt(13)\n"
"ds_read_b64_tr_b16 v[104:105], v124 offset:10368\n"
"ds_read_b64_tr_b16 v[106:107], v124 offset:10528\n"
"s_waitcnt lgkmcnt(13)\n"
"ds_write_b64 v40, v[204:205] offset:144\n"
"v_mfma_f32_32x32x16_bf16 v[160:175], a[200:203], v[72:75], v[160:175]\n"
"v_mov_b32 v52, 0x3f80\n"
"v_mov_b32 v53, 0\n"
"ds_write_b64 v40, v[52:53] offset:160\n"
"v_and_b32 v48, 16, v1\n"
"v_cmp_eq_u32 vcc, 0, v48\n"
"s_nop 1\n"
"v_cndmask_b32 v49, v50, v49, vcc\n"
"v_mul_f32 v49, 0xbfb8aa3b, v49\n"
"s_waitcnt lgkmcnt(14)\n"
"v_mfma_f32_32x32x16_bf16 v[224:239], v[88:91], a[12:15], v[224:239]\n"
"ds_read_b64_tr_b16 v[108:109], v124 offset:12800\n"
"s_waitcnt lgkmcnt(13)\n"
"ds_read_b64_tr_b16 v[110:111], v124 offset:12960\n"
"v_exp_f32 v49, v49\n"
"s_nop 0\n"
"v_add_f32 v49, 1.0, v49\n"
"v_rcp_f32 v42, v49\n"
"v_lshlrev_b32 v48, 2, v1\n"
"v_mfma_f32_32x32x16_bf16 v[240:255], v[92:95], a[12:15], v[240:255]\n"
"ds_read_b64_tr_b16 v[80:81], v124 offset:12864\n"
"s_waitcnt lgkmcnt(13)\n"
"ds_read_b64_tr_b16 v[82:83], v124 offset:13024\n"
"ds_read_b64_tr_b16 v[84:85], v124 offset:12928\n"
"s_waitcnt lgkmcnt(13)\n"
"ds_read_b64_tr_b16 v[86:87], v124 offset:13088\n"
"v_cmp_gt_u32 vcc, 32, v1\n"
"v_mov_b32 v50, 0x40000000\n"
"s_nop 0\n"
"v_cndmask_b32 v48, v50, v48, vcc\n"
"v_sub_f32 v49, 1.0, v42\n"
"s_waitcnt lgkmcnt(12)\n"
"v_mfma_f32_32x32x16_bf16 v[208:223], v[96:99], a[16:19], v[208:223]\n"
"ds_read_b64_tr_b16 v[88:89], v124 offset:15360\n"
"ds_read_b64_tr_b16 v[90:91], v124 offset:15520\n"
"buffer_store_dword v49, v48, s[48:51], s15 offen sc1\n"
"v_max3_f32 v8, v128, v129, v130\n"
"v_min3_f32 v9, v128, v129, v130\n"
"v_max3_f32 v10, v131, v132, v133\n"
"v_min3_f32 v12, v131, v132, v133\n"
"v_mfma_f32_32x32x16_bf16 v[224:239], v[100:103], a[16:19], v[224:239]\n"
"ds_read_b64_tr_b16 v[92:93], v124 offset:15424\n"
"s_waitcnt lgkmcnt(13)\n"
"ds_read_b64_tr_b16 v[94:95], v124 offset:15584\n"
"v_max3_f32 v8, v8, v134, v135\n"
"v_min3_f32 v9, v9, v134, v135\n"
"v_max3_f32 v10, v10, v136, v137\n"
"v_min3_f32 v12, v12, v136, v137\n"
"v_max3_f32 v8, v8, v138, v139\n"
"v_min3_f32 v9, v9, v138, v139\n"
"v_max3_f32 v10, v10, v140, v141\n"
"v_min3_f32 v12, v12, v140, v141\n"
"v_max3_f32 v8, v8, v142, v143\n"
"s_waitcnt lgkmcnt(8)\n"
"v_mfma_f32_32x32x16_bf16 v[240:255], v[104:107], a[16:19], v[240:255]\n"
"ds_read_b64_tr_b16 v[96:97], v124 offset:15488\n"
"ds_read_b64_tr_b16 v[98:99], v124 offset:15648\n"
"v_min3_f32 v9, v9, v142, v143\n"
"v_max3_f32 v10, v10, v144, v145\n"
"v_min3_f32 v12, v12, v144, v145\n"
"v_max3_f32 v8, v8, v146, v147\n"
"v_min3_f32 v9, v9, v146, v147\n"
"v_max_f32 v8, v8, v10\n"
"v_mfma_f32_32x32x16_bf16 v[208:223], v[108:111], a[20:23], v[208:223]\n"
"ds_read_b64_tr_b16 v[100:101], v124 offset:17920\n"
"ds_read_b64_tr_b16 v[102:103], v124 offset:18080\n"
"v_min_f32 v9, v9, v12\n"
"v_mul_f32 v8, s98, v8\n"
"v_mul_f32 v9, s98, v9\n"
"v_pk_fma_f32 v[56:57], v[128:129], s[98:99], v[8:9] op_sel:[0,0,1] op_sel_hi:[1,0,1] neg_lo:[1,0,0] neg_hi:[1,0,0]\n"
"v_pk_fma_f32 v[128:129], v[128:129], s[98:99], v[8:9] op_sel_hi:[1,0,0] neg_lo:[0,0,1] neg_hi:[0,0,1]\n"
"v_pk_fma_f32 v[58:59], v[130:131], s[98:99], v[8:9] op_sel:[0,0,1] op_sel_hi:[1,0,1] neg_lo:[1,0,0] neg_hi:[1,0,0]\n"
"v_pk_fma_f32 v[130:131], v[130:131], s[98:99], v[8:9] op_sel_hi:[1,0,0] neg_lo:[0,0,1] neg_hi:[0,0,1]\n"
"v_pk_fma_f32 v[60:61], v[132:133], s[98:99], v[8:9] op_sel:[0,0,1] op_sel_hi:[1,0,1] neg_lo:[1,0,0] neg_hi:[1,0,0]\n"
"v_pk_fma_f32 v[132:133], v[132:133], s[98:99], v[8:9] op_sel_hi:[1,0,0] neg_lo:[0,0,1] neg_hi:[0,0,1]\n"
"v_pk_fma_f32 v[62:63], v[134:135], s[98:99], v[8:9] op_sel:[0,0,1] op_sel_hi:[1,0,1] neg_lo:[1,0,0] neg_hi:[1,0,0]\n"
"v_pk_fma_f32 v[134:135], v[134:135], s[98:99], v[8:9] op_sel_hi:[1,0,0] neg_lo:[0,0,1] neg_hi:[0,0,1]\n"
"v_pk_fma_f32 v[64:65], v[136:137], s[98:99], v[8:9] op_sel:[0,0,1] op_sel_hi:[1,0,1] neg_lo:[1,0,0] neg_hi:[1,0,0]\n"
"v_pk_fma_f32 v[136:137], v[136:137], s[98:99], v[8:9] op_sel_hi:[1,0,0] neg_lo:[0,0,1] neg_hi:[0,0,1]\n"
"v_pk_fma_f32 v[66:67], v[138:139], s[98:99], v[8:9] op_sel:[0,0,1] op_sel_hi:[1,0,1] neg_lo:[1,0,0] neg_hi:[1,0,0]\n"
"v_pk_fma_f32 v[138:139], v[138:139], s[98:99], v[8:9] op_sel_hi:[1,0,0] neg_lo:[0,0,1] neg_hi:[0,0,1]\n"
"v_pk_fma_f32 v[68:69], v[140:141], s[98:99], v[8:9] op_sel:[0,0,1] op_sel_hi:[1,0,1] neg_lo:[1,0,0] neg_hi:[1,0,0]\n"
"v_pk_fma_f32 v[140:141], v[140:141], s[98:99], v[8:9] op_sel_hi:[1,0,0] neg_lo:[0,0,1] neg_hi:[0,0,1]\n"
"v_pk_fma_f32 v[70:71], v[142:143], s[98:99], v[8:9] op_sel:[0,0,1] op_sel_hi:[1,0,1] neg_lo:[1,0,0] neg_hi:[1,0,0]\n"
"v_pk_fma_f32 v[142:143], v[142:143], s[98:99], v[8:9] op_sel_hi:[1,0,0] neg_lo:[0,0,1] neg_hi:[0,0,1]\n"
"v_pk_fma_f32 v[72:73], v[144:145], s[98:99], v[8:9] op_sel:[0,0,1] op_sel_hi:[1,0,1] neg_lo:[1,0,0] neg_hi:[1,0,0]\n"
"v_pk_fma_f32 v[144:145], v[144:145], s[98:99], v[8:9] op_sel_hi:[1,0,0] neg_lo:[0,0,1] neg_hi:[0,0,1]\n"
"v_pk_fma_f32 v[74:75], v[146:147], s[98:99], v[8:9] op_sel:[0,0,1] op_sel_hi:[1,0,1] neg_lo:[1,0,0] neg_hi:[1,0,0]\n"
"v_pk_fma_f32 v[146:147], v[146:147], s[98:99], v[8:9] op_sel_hi:[1,0,0] neg_lo:[0,0,1] neg_hi:[0,0,1]\n"
"s_waitcnt lgkmcnt(8)\n"
"v_mfma_f32_32x32x16_bf16 v[224:239], v[80:83], a[20:23], v[224:239]\n"
"ds_read_b64_tr_b16 v[104:105], v124 offset:17984\n"
"ds_read_b64_tr_b16 v[106:107], v124 offset:18144\n"
"v_exp_f32 v56, v56\n"
"v_exp_f32 v128, v128\n"
"v_exp_f32 v57, v57\n"
"v_mfma_f32_32x32x16_bf16 v[240:255], v[84:87], a[20:23], v[240:255]\n"
"ds_read_b64_tr_b16 v[108:109], v124 offset:18048\n"
"ds_read_b64_tr_b16 v[110:111], v124 offset:18208\n"
"v_exp_f32 v129, v129\n"
"v_exp_f32 v58, v58\n"
"v_exp_f32 v130, v130\n"
"v_exp_f32 v59, v59\n"
"s_waitcnt lgkmcnt(8)\n"
"v_mfma_f32_32x32x16_bf16 v[208:223], v[88:91], a[24:27], v[208:223]\n"
"v_exp_f32 v131, v131\n"
"v_exp_f32 v60, v60\n"
"v_exp_f32 v132, v132\n"
"v_exp_f32 v61, v61\n"
"v_exp_f32 v133, v133\n"
"v_exp_f32 v62, v62\n"
"v_mfma_f32_32x32x16_bf16 v[224:239], v[92:95], a[24:27], v[224:239]\n"
"v_exp_f32 v134, v134\n"
"v_exp_f32 v63, v63\n"
"v_exp_f32 v135, v135\n"
"v_exp_f32 v64, v64\n"
"v_exp_f32 v136, v136\n"
"v_exp_f32 v65, v65\n"
"v_exp_f32 v137, v137\n"
"s_waitcnt lgkmcnt(4)\n"
"v_mfma_f32_32x32x16_bf16 v[240:255], v[96:99], a[24:27], v[240:255]\n"
"s_waitcnt vmcnt(1) lgkmcnt(0)\n"
"s_barrier\n"
"ds_read_b64_tr_b16 v[80:81], v124 offset:20480\n"
"ds_read_b64_tr_b16 v[82:83], v124 offset:20640\n"
"v_mfma_f32_32x32x16_bf16 v[208:223], v[100:103], a[28:31], v[208:223]\n"
"ds_read_b64_tr_b16 v[84:85], v124 offset:20544\n"
"ds_read_b64_tr_b16 v[86:87], v124 offset:20704\n"
"v_exp_f32 v66, v66\n"
"v_exp_f32 v138, v138\n"
"v_exp_f32 v67, v67\n"
"v_exp_f32 v139, v139\n"
"v_exp_f32 v68, v68\n"
"v_mfma_f32_32x32x16_bf16 v[224:239], v[104:107], a[28:31], v[224:239]\n"
"ds_read_b64_tr_b16 v[88:89], v124 offset:20608\n"
"ds_read_b64_tr_b16 v[90:91], v124 offset:20768\n"
"v_exp_f32 v140, v140\n"
"v_exp_f32 v69, v69\n"
"v_exp_f32 v141, v141\n"
"v_mfma_f32_32x32x16_bf16 v[240:255], v[108:111], a[28:31], v[240:255]\n"
"s_add_u32 m0, s93, 0x10000\n"
"ds_read_b64_tr_b16 v[92:93], v124 offset:23040\n"
"ds_read_b64_tr_b16 v[94:95], v124 offset:23200\n"
"buffer_load_dwordx4 v117, s[40:43], s68 offen lds\n"
"s_waitcnt lgkmcnt(4)\n"
"v_mfma_f32_32x32x16_bf16 v[208:223], v[80:83], a[32:35], v[208:223]\n"
"ds_read_b64_tr_b16 v[96:97], v124 offset:23104\n"
"ds_read_b64_tr_b16 v[98:99], v124 offset:23264\n"
"buffer_load_dwordx4 v[16:19], v6, s[48:51], s2 offen sc1\n"
"v_mfma_f32_32x32x16_bf16 v[224:239], v[84:87], a[32:35], v[224:239]\n"
"ds_read_b64_tr_b16 v[100:101], v124 offset:23168\n"
"ds_read_b64_tr_b16 v[102:103], v124 offset:23328\n"
"buffer_load_dwordx4 v118, s[40:43], s68 offen offset:1024 lds\n"
"v_exp_f32 v70, v70\n"
"v_exp_f32 v142, v142\n"
"s_waitcnt lgkmcnt(4)\n"
"v_mfma_f32_32x32x16_bf16 v[240:255], v[88:91], a[32:35], v[240:255]\n"
"ds_read_b64_tr_b16 v[104:105], v124 offset:25600\n"
"ds_read_b64_tr_b16 v[106:107], v124 offset:25760\n"
"v_exp_f32 v71, v71\n"
"v_exp_f32 v143, v143\n"
"v_exp_f32 v72, v72\n"
"v_mfma_f32_32x32x16_bf16 v[208:223], v[92:95], a[36:39], v[208:223]\n"
"ds_read_b64_tr_b16 v[108:109], v124 offset:25664\n"
"ds_read_b64_tr_b16 v[110:111], v124 offset:25824\n"
"ds_read_b64_tr_b16 v[80:81], v124 offset:25728\n"
"ds_read_b64_tr_b16 v[82:83], v124 offset:25888\n"
"buffer_load_dwordx4 v119, s[40:43], s68 offen offset:2048 lds\n"
"s_waitcnt lgkmcnt(6)\n"
"v_mfma_f32_32x32x16_bf16 v[224:239], v[96:99], a[36:39], v[224:239]\n"
"ds_read_b64_tr_b16 v[84:85], v124 offset:28160\n"
"ds_read_b64_tr_b16 v[86:87], v124 offset:28320\n"
"v_exp_f32 v144, v144\n"
"v_exp_f32 v73, v73\n"
"v_exp_f32 v145, v145\n"
"v_mfma_f32_32x32x16_bf16 v[240:255], v[100:103], a[36:39], v[240:255]\n"
"ds_read_b64_tr_b16 v[88:89], v124 offset:28224\n"
"ds_read_b64_tr_b16 v[90:91], v124 offset:28384\n"
"buffer_load_dwordx4 v120, s[40:43], s68 offen offset:3072 lds\n"
"v_exp_f32 v74, v74\n"
"v_exp_f32 v146, v146\n"
"s_waitcnt lgkmcnt(6)\n"
"v_mfma_f32_32x32x16_bf16 v[208:223], v[104:107], a[40:43], v[208:223]\n"
"ds_read_b64_tr_b16 v[92:93], v124 offset:28288\n"
"ds_read_b64_tr_b16 v[94:95], v124 offset:28448\n"
"v_exp_f32 v75, v75\n"
"v_exp_f32 v147, v147\n"
"v_pk_add_f32 v[12:13], v[56:57], v[58:59]\n"
"v_pk_add_f32 v[10:11], v[128:129], v[130:131]\n"
"v_pk_add_f32 v[12:13], v[12:13], v[60:61]\n"
"v_pk_add_f32 v[10:11], v[10:11], v[132:133]\n"
"v_pk_add_f32 v[12:13], v[12:13], v[62:63]\n"
"v_pk_add_f32 v[10:11], v[10:11], v[134:135]\n"
"v_pk_add_f32 v[12:13], v[12:13], v[64:65]\n"
"v_pk_add_f32 v[10:11], v[10:11], v[136:137]\n"
"v_pk_add_f32 v[12:13], v[12:13], v[66:67]\n"
"v_pk_add_f32 v[10:11], v[10:11], v[138:139]\n"
"v_pk_add_f32 v[12:13], v[12:13], v[68:69]\n"
"v_pk_add_f32 v[10:11], v[10:11], v[140:141]\n"
"v_pk_add_f32 v[12:13], v[12:13], v[70:71]\n"
"v_pk_add_f32 v[10:11], v[10:11], v[142:143]\n"
"v_pk_add_f32 v[12:13], v[12:13], v[72:73]\n"
"v_pk_add_f32 v[10:11], v[10:11], v[144:145]\n"
"v_pk_add_f32 v[12:13], v[12:13], v[74:75]\n"
"v_pk_add_f32 v[10:11], v[10:11], v[146:147]\n"
"v_mfma_f32_32x32x16_bf16 v[224:239], v[108:111], a[40:43], v[224:239]\n"
"s_add_u32 m0, s93, 0x10c00\n"
"ds_read_b64_tr_b16 v[96:97], v124 offset:30720\n"
"ds_read_b64_tr_b16 v[98:99], v124 offset:30880\n"
"buffer_load_dwordx4 v121, s[40:43], s68 offen offset:1024 lds\n"
"v_add_f32 v12, v12, v13\n"
"s_waitcnt lgkmcnt(6)\n"
"v_mfma_f32_32x32x16_bf16 v[240:255], v[80:83], a[40:43], v[240:255]\n"
"ds_read_b64_tr_b16 v[100:101], v124 offset:30784\n"
"ds_read_b64_tr_b16 v[102:103], v124 offset:30944\n"
"v_add_f32 v10, v10, v11\n"
"v_rcp_f32 v12, v12\n"
"v_rcp_f32 v10, v10\n"
"s_nop 0\n"
"v_pk_mul_f32 v[128:129], v[128:129], v[10:11] op_sel_hi:[1,0]\n"
"v_pk_mul_f32 v[56:57], v[56:57], v[12:13] op_sel_hi:[1,0]\n"
"v_pk_mul_f32 v[130:131], v[130:131], v[10:11] op_sel_hi:[1,0]\n"
"v_pk_mul_f32 v[58:59], v[58:59], v[12:13] op_sel_hi:[1,0]\n"
"v_pk_mul_f32 v[132:133], v[132:133], v[10:11] op_sel_hi:[1,0]\n"
"v_pk_mul_f32 v[60:61], v[60:61], v[12:13] op_sel_hi:[1,0]\n"
"v_pk_mul_f32 v[134:135], v[134:135], v[10:11] op_sel_hi:[1,0]\n"
"v_pk_mul_f32 v[62:63], v[62:63], v[12:13] op_sel_hi:[1,0]\n"
"v_pk_mul_f32 v[136:137], v[136:137], v[10:11] op_sel_hi:[1,0]\n"
"v_pk_mul_f32 v[64:65], v[64:65], v[12:13] op_sel_hi:[1,0]\n"
"v_pk_mul_f32 v[138:139], v[138:139], v[10:11] op_sel_hi:[1,0]\n"
"v_pk_mul_f32 v[66:67], v[66:67], v[12:13] op_sel_hi:[1,0]\n"
"v_pk_mul_f32 v[140:141], v[140:141], v[10:11] op_sel_hi:[1,0]\n"
"v_pk_mul_f32 v[68:69], v[68:69], v[12:13] op_sel_hi:[1,0]\n"
"v_pk_mul_f32 v[142:143], v[142:143], v[10:11] op_sel_hi:[1,0]\n"
"v_pk_mul_f32 v[70:71], v[70:71], v[12:13] op_sel_hi:[1,0]\n"
"v_pk_mul_f32 v[144:145], v[144:145], v[10:11] op_sel_hi:[1,0]\n"
"v_pk_mul_f32 v[72:73], v[72:73], v[12:13] op_sel_hi:[1,0]\n"
"v_pk_mul_f32 v[146:147], v[146:147], v[10:11] op_sel_hi:[1,0]\n"
"v_pk_mul_f32 v[74:75], v[74:75], v[12:13] op_sel_hi:[1,0]\n"
"v_mfma_f32_32x32x16_bf16 v[208:223], v[84:87], a[44:47], v[208:223]\n"
"ds_read_b64_tr_b16 v[104:105], v124 offset:30848\n"
"ds_read_b64_tr_b16 v[106:107], v124 offset:31008\n"
"v_cvt_pk_bf16_f32 v128, v128, v129\n"
"v_cvt_pk_bf16_f32 v129, v130, v131\n"
"v_cvt_pk_bf16_f32 v130, v132, v133\n"
"v_cvt_pk_bf16_f32 v131, v134, v135\n"
"v_cvt_pk_bf16_f32 v132, v136, v137\n"
"v_cvt_pk_bf16_f32 v133, v138, v139\n"
"v_cvt_pk_bf16_f32 v134, v140, v141\n"
"v_cvt_pk_bf16_f32 v135, v142, v143\n"
"v_cvt_pk_bf16_f32 v136, v144, v145\n"
"s_waitcnt lgkmcnt(6)\n"
"v_mfma_f32_32x32x16_bf16 v[224:239], v[88:91], a[44:47], v[224:239]\n"
"ds_read_b64_tr_b16 v[108:109], v124 offset:33280\n"
"ds_read_b64_tr_b16 v[110:111], v124 offset:33440\n"
"v_cvt_pk_bf16_f32 v137, v146, v147\n"
"v_cvt_pk_bf16_f32 v138, v56, v57\n"
"v_cvt_pk_bf16_f32 v139, v58, v59\n"
"v_cvt_pk_bf16_f32 v140, v60, v61\n"
"v_cvt_pk_bf16_f32 v141, v62, v63\n"
"v_cvt_pk_bf16_f32 v142, v64, v65\n"
"v_mfma_f32_32x32x16_bf16 v[240:255], v[92:95], a[44:47], v[240:255]\n"
"ds_read_b64_tr_b16 v[80:81], v124 offset:33344\n"
"ds_read_b64_tr_b16 v[82:83], v124 offset:33504\n"
"ds_read_b64_tr_b16 v[84:85], v124 offset:33408\n"
"ds_read_b64_tr_b16 v[86:87], v124 offset:33568\n"
"v_cvt_pk_bf16_f32 v143, v66, v67\n"
"v_cvt_pk_bf16_f32 v144, v68, v69\n"
"v_cvt_pk_bf16_f32 v145, v70, v71\n"
"s_waitcnt lgkmcnt(8)\n"
"v_mfma_f32_32x32x16_bf16 v[208:223], v[96:99], a[48:51], v[208:223]\n"
"ds_read_b64_tr_b16 v[88:89], v124 offset:35840\n"
"ds_read_b64_tr_b16 v[90:91], v124 offset:36000\n"
"v_cvt_pk_bf16_f32 v146, v72, v73\n"
"v_cvt_pk_bf16_f32 v147, v74, v75\n"
"ds_write_b128 v41, v[128:131] offset:0\n"
"ds_write_b128 v41, v[132:135] offset:16\n"
"ds_write_b128 v41, v[136:139] offset:32\n"
"ds_write_b128 v41, v[140:143] offset:48\n"
"v_mfma_f32_32x32x16_bf16 v[224:239], v[100:103], a[48:51], v[224:239]\n"
"ds_read_b64_tr_b16 v[92:93], v124 offset:35904\n"
"s_waitcnt lgkmcnt(13)\n"
"ds_read_b64_tr_b16 v[94:95], v124 offset:36064\n"
"ds_write_b128 v41, v[144:147] offset:64\n"
"v_max3_f32 v8, v148, v149, v150\n"
"v_min3_f32 v9, v148, v149, v150\n"
"v_max3_f32 v10, v151, v152, v153\n"
"v_min3_f32 v12, v151, v152, v153\n"
"v_max3_f32 v8, v8, v154, v155\n"
"v_min3_f32 v9, v9, v154, v155\n"
"v_max3_f32 v10, v10, v156, v157\n"
"v_min3_f32 v12, v12, v156, v157\n"
"s_waitcnt lgkmcnt(13)\n"
"v_mfma_f32_32x32x16_bf16 v[240:255], v[104:107], a[48:51], v[240:255]\n"
"ds_read_b64_tr_b16 v[96:97], v124 offset:35968\n"
"ds_read_b64_tr_b16 v[98:99], v124 offset:36128\n"
"v_max3_f32 v8, v8, v158, v159\n"
"v_min3_f32 v9, v9, v158, v159\n"
"v_max3_f32 v10, v10, v160, v161\n"
"v_min3_f32 v12, v12, v160, v161\n"
"v_max3_f32 v8, v8, v162, v163\n"
"v_mfma_f32_32x32x16_bf16 v[208:223], v[108:111], a[52:55], v[208:223]\n"
"s_waitcnt lgkmcnt(13)\n"
"ds_read_b64_tr_b16 v[100:101], v124 offset:38400\n"
"ds_read_b64_tr_b16 v[102:103], v124 offset:38560\n"
"v_min3_f32 v9, v9, v162, v163\n"
"v_max3_f32 v10, v10, v164, v165\n"
"v_min3_f32 v12, v12, v164, v165\n"
"v_max3_f32 v8, v8, v166, v167\n"
"v_min3_f32 v9, v9, v166, v167\n"
"v_max_f32 v8, v8, v10\n"
"v_min_f32 v9, v9, v12\n"
"v_mul_f32 v8, s98, v8\n"
"v_mul_f32 v9, s98, v9\n"
"v_pk_fma_f32 v[56:57], v[148:149], s[98:99], v[8:9] op_sel:[0,0,1] op_sel_hi:[1,0,1] neg_lo:[1,0,0] neg_hi:[1,0,0]\n"
"v_pk_fma_f32 v[148:149], v[148:149], s[98:99], v[8:9] op_sel_hi:[1,0,0] neg_lo:[0,0,1] neg_hi:[0,0,1]\n"
"v_pk_fma_f32 v[58:59], v[150:151], s[98:99], v[8:9] op_sel:[0,0,1] op_sel_hi:[1,0,1] neg_lo:[1,0,0] neg_hi:[1,0,0]\n"
"v_pk_fma_f32 v[150:151], v[150:151], s[98:99], v[8:9] op_sel_hi:[1,0,0] neg_lo:[0,0,1] neg_hi:[0,0,1]\n"
"v_pk_fma_f32 v[60:61], v[152:153], s[98:99], v[8:9] op_sel:[0,0,1] op_sel_hi:[1,0,1] neg_lo:[1,0,0] neg_hi:[1,0,0]\n"
"v_pk_fma_f32 v[152:153], v[152:153], s[98:99], v[8:9] op_sel_hi:[1,0,0] neg_lo:[0,0,1] neg_hi:[0,0,1]\n"
"v_pk_fma_f32 v[62:63], v[154:155], s[98:99], v[8:9] op_sel:[0,0,1] op_sel_hi:[1,0,1] neg_lo:[1,0,0] neg_hi:[1,0,0]\n"
"v_pk_fma_f32 v[154:155], v[154:155], s[98:99], v[8:9] op_sel_hi:[1,0,0] neg_lo:[0,0,1] neg_hi:[0,0,1]\n"
"v_pk_fma_f32 v[64:65], v[156:157], s[98:99], v[8:9] op_sel:[0,0,1] op_sel_hi:[1,0,1] neg_lo:[1,0,0] neg_hi:[1,0,0]\n"
"v_pk_fma_f32 v[156:157], v[156:157], s[98:99], v[8:9] op_sel_hi:[1,0,0] neg_lo:[0,0,1] neg_hi:[0,0,1]\n"
"v_pk_fma_f32 v[66:67], v[158:159], s[98:99], v[8:9] op_sel:[0,0,1] op_sel_hi:[1,0,1] neg_lo:[1,0,0] neg_hi:[1,0,0]\n"
"v_pk_fma_f32 v[158:159], v[158:159], s[98:99], v[8:9] op_sel_hi:[1,0,0] neg_lo:[0,0,1] neg_hi:[0,0,1]\n"
"v_pk_fma_f32 v[68:69], v[160:161], s[98:99], v[8:9] op_sel:[0,0,1] op_sel_hi:[1,0,1] neg_lo:[1,0,0] neg_hi:[1,0,0]\n"
"v_pk_fma_f32 v[160:161], v[160:161], s[98:99], v[8:9] op_sel_hi:[1,0,0] neg_lo:[0,0,1] neg_hi:[0,0,1]\n"
"v_pk_fma_f32 v[70:71], v[162:163], s[98:99], v[8:9] op_sel:[0,0,1] op_sel_hi:[1,0,1] neg_lo:[1,0,0] neg_hi:[1,0,0]\n"
"v_pk_fma_f32 v[162:163], v[162:163], s[98:99], v[8:9] op_sel_hi:[1,0,0] neg_lo:[0,0,1] neg_hi:[0,0,1]\n"
"v_pk_fma_f32 v[72:73], v[164:165], s[98:99], v[8:9] op_sel:[0,0,1] op_sel_hi:[1,0,1] neg_lo:[1,0,0] neg_hi:[1,0,0]\n"
"v_pk_fma_f32 v[164:165], v[164:165], s[98:99], v[8:9] op_sel_hi:[1,0,0] neg_lo:[0,0,1] neg_hi:[0,0,1]\n"
"v_pk_fma_f32 v[74:75], v[166:167], s[98:99], v[8:9] op_sel:[0,0,1] op_sel_hi:[1,0,1] neg_lo:[1,0,0] neg_hi:[1,0,0]\n"
"v_pk_fma_f32 v[166:167], v[166:167], s[98:99], v[8:9] op_sel_hi:[1,0,0] neg_lo:[0,0,1] neg_hi:[0,0,1]\n"
"s_waitcnt lgkmcnt(13)\n"
"v_mfma_f32_32x32x16_bf16 v[224:239], v[80:83], a[52:55], v[224:239]\n"
"ds_read_b64_tr_b16 v[104:105], v124 offset:38464\n"
"ds_read_b64_tr_b16 v[106:107], v124 offset:38624\n"
"v_exp_f32 v56, v56\n"
"v_exp_f32 v148, v148\n"
"v_exp_f32 v57, v57\n"
"v_mfma_f32_32x32x16_bf16 v[240:255], v[84:87], a[52:55], v[240:255]\n"
"s_waitcnt lgkmcnt(13)\n"
"ds_read_b64_tr_b16 v[108:109], v124 offset:38528\n"
"ds_read_b64_tr_b16 v[110:111], v124 offset:38688\n"
"v_exp_f32 v149, v149\n"
"v_exp_f32 v58, v58\n"
"v_exp_f32 v150, v150\n"
"v_exp_f32 v59, v59\n"
"s_waitcnt lgkmcnt(9)\n"
"v_mfma_f32_32x32x16_bf16 v[208:223], v[88:91], a[56:59], v[208:223]\n"
"v_exp_f32 v151, v151\n"
"v_exp_f32 v60, v60\n"
"v_exp_f32 v152, v152\n"
"v_exp_f32 v61, v61\n"
"v_exp_f32 v153, v153\n"
"v_exp_f32 v62, v62\n"
"v_mfma_f32_32x32x16_bf16 v[224:239], v[92:95], a[56:59], v[224:239]\n"
"s_waitcnt vmcnt(4)\n"
"v_cmp_eq_u64 vcc, s[84:85], v[16:17]\n"
"v_cmp_eq_u64 s[10:11], s[84:85], v[18:19]\n"
"s_and_b64 vcc, vcc, s[10:11]\n"
"s_and_b64 s[10:11], vcc, s[94:95]\n"
"s_cmp_eq_u64 s[10:11], s[94:95]\n"
"s_cbranch_scc0 L_att_b_slow\n"
"L_att_b_ok:\n"
"s_waitcnt lgkmcnt(4)\n"
"v_mfma_f32_32x32x16_bf16 v[240:255], v[96:99], a[56:59], v[240:255]\n"
"s_waitcnt vmcnt(0) lgkmcnt(0)\n"
"s_barrier\n"
"ds_read_b64_tr_b16 v[80:81], v124 offset:0\n"
"ds_read_b64_tr_b16 v[82:83], v124 offset:160\n"
"v_exp_f32 v154, v154\n"
"v_mfma_f32_32x32x16_bf16 v[208:223], v[100:103], a[60:63], v[208:223]\n"
"ds_read_b64_tr_b16 v[84:85], v124 offset:64\n"
"ds_read_b64_tr_b16 v[86:87], v124 offset:224\n"
"v_exp_f32 v63, v63\n"
"v_exp_f32 v155, v155\n"
"v_exp_f32 v64, v64\n"
"v_exp_f32 v156, v156\n"
"v_mfma_f32_32x32x16_bf16 v[224:239], v[104:107], a[60:63], v[224:239]\n"
"s_add_u32 s7, s4, 0\n"
"ds_read_b64_tr_b16 v[88:89], v124 offset:128\n"
"ds_read_b64_tr_b16 v[90:91], v124 offset:288\n"
"buffer_load_dwordx4 a[204:207], v6, s[48:51], s7 offen offset:0 sc1\n"
"v_mfma_f32_32x32x16_bf16 v[240:255], v[108:111], a[60:63], v[240:255]\n"
"s_add_u32 m0, s93, 0x15000\n"
"ds_read_b64_tr_b16 v[92:93], v124 offset:2560\n"
"ds_read_b64_tr_b16 v[94:95], v124 offset:2720\n"
"buffer_load_dwordx4 v117, s[40:43], s69 offen lds\n"
"v_exp_f32 v65, v65\n"
"s_waitcnt lgkmcnt(4)\n"
"v_mfma_f32_32x32x16_bf16 v[208:223], v[80:83], a[64:67], v[208:223]\n"
"ds_read_b64_tr_b16 v[96:97], v124 offset:2624\n"
"ds_read_b64_tr_b16 v[98:99], v124 offset:2784\n"
"buffer_load_dwordx4 a[208:211], v6, s[48:51], s7 offen offset:1024 sc1\n"
"v_mfma_f32_32x32x16_bf16 v[224:239], v[84:87], a[64:67], v[224:239]\n"
"ds_read_b64_tr_b16 v[100:101], v124 offset:2688\n"
"ds_read_b64_tr_b16 v[102:103], v124 offset:2848\n"
"buffer_load_dwordx4 v118, s[40:43], s69 offen offset:1024 lds\n"
"v_exp_f32 v157, v157\n"
"v_exp_f32 v66, v66\n"
"s_waitcnt lgkmcnt(4)\n"
"v_mfma_f32_32x32x16_bf16 v[240:255], v[88:91], a[64:67], v[240:255]\n"
"ds_read_b64_tr_b16 v[104:105], v124 offset:5120\n"
"ds_read_b64_tr_b16 v[106:107], v124 offset:5280\n"
"buffer_load_dwordx4 a[212:215], v6, s[48:51], s7 offen offset:2048 sc1\n"
"v_mfma_f32_32x32x16_bf16 v[208:223], v[92:95], a[68:71], v[208:223]\n"
"ds_read_b64_tr_b16 v[108:109], v124 offset:5184\n"
"ds_read_b64_tr_b16 v[110:111], v124 offset:5344\n"
"ds_read_b64_tr_b16 v[80:81], v124 offset:5248\n"
"ds_read_b64_tr_b16 v[82:83], v124 offset:5408\n"
"buffer_load_dwordx4 v119, s[40:43], s69 offen offset:2048 lds\n"
"s_waitcnt lgkmcnt(6)\n"
"v_mfma_f32_32x32x16_bf16 v[224:239], v[96:99], a[68:71], v[224:239]\n"
"ds_read_b64_tr_b16 v[84:85], v124 offset:7680\n"
"ds_read_b64_tr_b16 v[86:87], v124 offset:7840\n"
"buffer_load_dwordx4 a[216:219], v6, s[48:51], s7 offen offset:3072 sc1\n"
"v_exp_f32 v158, v158\n"
"v_mfma_f32_32x32x16_bf16 v[240:255], v[100:103], a[68:71], v[240:255]\n"
"ds_read_b64_tr_b16 v[88:89], v124 offset:7744\n"
"ds_read_b64_tr_b16 v[90:91], v124 offset:7904\n"
"buffer_load_dwordx4 v120, s[40:43], s69 offen offset:3072 lds\n"
"v_exp_f32 v67, v67\n"
"s_waitcnt lgkmcnt(6)\n"
"v_mfma_f32_32x32x16_bf16 v[208:223], v[104:107], a[72:75], v[208:223]\n"
"s_add_u32 s7, s7, 1024\n"
"ds_read_b64_tr_b16 v[92:93], v124 offset:7808\n"
"ds_read_b64_tr_b16 v[94:95], v124 offset:7968\n"
"buffer_load_dwordx4 a[220:223], v6, s[48:51], s7 offen offset:3072 sc1\n"
"v_exp_f32 v159, v159\n"
"v_mfma_f32_32x32x16_bf16 v[224:239], v[108:111], a[72:75], v[224:239]\n"
"s_add_u32 m0, s93, 0x15c00\n"
"ds_read_b64_tr_b16 v[96:97], v124 offset:10240\n"
"ds_read_b64_tr_b16 v[98:99], v124 offset:10400\n"
"buffer_load_dwordx4 v121, s[40:43], s69 offen offset:1024 lds\n"
"s_waitcnt lgkmcnt(6)\n"
"v_mfma_f32_32x32x16_bf16 v[240:255], v[80:83], a[72:75], v[240:255]\n"
"ds_read_b64_tr_b16 v[100:101], v124 offset:10304\n"
"ds_read_b64_tr_b16 v[102:103], v124 offset:10464\n"
"v_exp_f32 v68, v68\n"
"v_exp_f32 v160, v160\n"
"v_exp_f32 v69, v69\n"
"v_mfma_f32_32x32x16_bf16 v[208:223], v[84:87], a[76:79], v[208:223]\n"
"s_add_u32 s7, s4, 0\n"
"s_add_u32 s7, s7, s1\n"
"ds_read_b64_tr_b16 v[104:105], v124 offset:10368\n"
"ds_read_b64_tr_b16 v[106:107], v124 offset:10528\n"
"buffer_load_dwordx4 a[224:227], v6, s[48:51], s7 offen offset:0 sc1\n"
"s_waitcnt lgkmcnt(6)\n"
"v_mfma_f32_32x32x16_bf16 v[224:239], v[88:91], a[76:79], v[224:239]\n"
"ds_read_b64_tr_b16 v[108:109], v124 offset:12800\n"
"ds_read_b64_tr_b16 v[110:111], v124 offset:12960\n"
"v_exp_f32 v161, v161\n"
"v_exp_f32 v70, v70\n"
"v_exp_f32 v162, v162\n"
"v_mfma_f32_32x32x16_bf16 v[240:255], v[92:95], a[76:79], v[240:255]\n"
"ds_read_b64_tr_b16 v[80:81], v124 offset:12864\n"
"ds_read_b64_tr_b16 v[82:83], v124 offset:13024\n"
"ds_read_b64_tr_b16 v[84:85], v124 offset:12928\n"
"ds_read_b64_tr_b16 v[86:87], v124 offset:13088\n"
"buffer_load_dwordx4 a[228:231], v6, s[48:51], s7 offen offset:1024 sc1\n"
"v_exp_f32 v71, v71\n"
"s_waitcnt lgkmcnt(8)\n"
"v_mfma_f32_32x32x16_bf16 v[208:223], v[96:99], a[80:83], v[208:223]\n"
"ds_read_b64_tr_b16 v[88:89], v124 offset:15360\n"
"ds_read_b64_tr_b16 v[90:91], v124 offset:15520\n"
"v_exp_f32 v163, v163\n"
"v_exp_f32 v72, v72\n"
"v_exp_f32 v164, v164\n"
"v_mfma_f32_32x32x16_bf16 v[224:239], v[100:103], a[80:83], v[224:239]\n"
"ds_read_b64_tr_b16 v[92:93], v124 offset:15424\n"
"ds_read_b64_tr_b16 v[94:95], v124 offset:15584\n"
"buffer_load_dwordx4 a[232:235], v6, s[48:51], s7 offen offset:2048 sc1\n"
"v_exp_f32 v73, v73\n"
"s_waitcnt lgkmcnt(8)\n"
"v_mfma_f32_32x32x16_bf16 v[240:255], v[104:107], a[80:83], v[240:255]\n"
"ds_read_b64_tr_b16 v[96:97], v124 offset:15488\n"
"ds_read_b64_tr_b16 v[98:99], v124 offset:15648\n"
"v_exp_f32 v165, v165\n"
"v_exp_f32 v74, v74\n"
"v_exp_f32 v166, v166\n"
"v_mfma_f32_32x32x16_bf16 v[208:223], v[108:111], a[84:87], v[208:223]\n"
"ds_read_b64_tr_b16 v[100:101], v124 offset:17920\n"
"ds_read_b64_tr_b16 v[102:103], v124 offset:18080\n"
"buffer_load_dwordx4 a[236:239], v6, s[48:51], s7 offen offset:3072 sc1\n"
"v_exp_f32 v75, v75\n"
"v_exp_f32 v167, v167\n"
"s_waitcnt lgkmcnt(8)\n"
"v_mfma_f32_32x32x16_bf16 v[224:239], v[80:83], a[84:87], v[224:239]\n"
"ds_read_b64_tr_b16 v[104:105], v124 offset:17984\n"
"ds_read_b64_tr_b16 v[106:107], v124 offset:18144\n"
"v_pk_add_f32 v[12:13], v[56:57], v[58:59]\n"
"v_pk_add_f32 v[10:11], v[148:149], v[150:151]\n"
"v_pk_add_f32 v[12:13], v[12:13], v[60:61]\n"
"v_pk_add_f32 v[10:11], v[10:11], v[152:153]\n"
"v_pk_add_f32 v[12:13], v[12:13], v[62:63]\n"
"v_pk_add_f32 v[10:11], v[10:11], v[154:155]\n"
"v_pk_add_f32 v[12:13], v[12:13], v[64:65]\n"
"v_pk_add_f32 v[10:11], v[10:11], v[156:157]\n"
"v_pk_add_f32 v[12:13], v[12:13], v[66:67]\n"
"v_pk_add_f32 v[10:11], v[10:11], v[158:159]\n"
"v_pk_add_f32 v[12:13], v[12:13], v[68:69]\n"
"v_pk_add_f32 v[10:11], v[10:11], v[160:161]\n"
"v_pk_add_f32 v[12:13], v[12:13], v[70:71]\n"
"v_pk_add_f32 v[10:11], v[10:11], v[162:163]\n"
"v_pk_add_f32 v[12:13], v[12:13], v[72:73]\n"
"v_pk_add_f32 v[10:11], v[10:11], v[164:165]\n"
"v_pk_add_f32 v[12:13], v[12:13], v[74:75]\n"
"v_pk_add_f32 v[10:11], v[10:11], v[166:167]\n"
"v_mfma_f32_32x32x16_bf16 v[240:255], v[84:87], a[84:87], v[240:255]\n"
"s_add_u32 s7, s7, 1024\n"
"ds_read_b64_tr_b16 v[108:109], v124 offset:18048\n"
"ds_read_b64_tr_b16 v[110:111], v124 offset:18208\n"
"buffer_load_dwordx4 a[240:243], v6, s[48:51], s7 offen offset:3072 sc1\n"
"v_add_f32 v12, v12, v13\n"
"s_waitcnt lgkmcnt(8)\n"
"v_mfma_f32_32x32x16_bf16 v[208:223], v[88:91], a[88:91], v[208:223]\n"
"v_add_f32 v10, v10, v11\n"
"v_rcp_f32 v12, v12\n"
"v_rcp_f32 v10, v10\n"
"s_nop 0\n"
"v_pk_mul_f32 v[148:149], v[148:149], v[10:11] op_sel_hi:[1,0]\n"
"v_pk_mul_f32 v[56:57], v[56:57], v[12:13] op_sel_hi:[1,0]\n"
"v_pk_mul_f32 v[150:151], v[150:151], v[10:11] op_sel_hi:[1,0]\n"
"v_pk_mul_f32 v[58:59], v[58:59], v[12:13] op_sel_hi:[1,0]\n"
"v_pk_mul_f32 v[152:153], v[152:153], v[10:11] op_sel_hi:[1,0]\n"
"v_pk_mul_f32 v[60:61], v[60:61], v[12:13] op_sel_hi:[1,0]\n"
"v_pk_mul_f32 v[154:155], v[154:155], v[10:11] op_sel_hi:[1,0]\n"
"v_pk_mul_f32 v[62:63], v[62:63], v[12:13] op_sel_hi:[1,0]\n"
"v_pk_mul_f32 v[156:157], v[156:157], v[10:11] op_sel_hi:[1,0]\n"
"v_pk_mul_f32 v[64:65], v[64:65], v[12:13] op_sel_hi:[1,0]\n"
"v_pk_mul_f32 v[158:159], v[158:159], v[10:11] op_sel_hi:[1,0]\n"
"v_pk_mul_f32 v[66:67], v[66:67], v[12:13] op_sel_hi:[1,0]\n"
"v_pk_mul_f32 v[160:161], v[160:161], v[10:11] op_sel_hi:[1,0]\n"
"v_pk_mul_f32 v[68:69], v[68:69], v[12:13] op_sel_hi:[1,0]\n"
"v_pk_mul_f32 v[162:163], v[162:163], v[10:11] op_sel_hi:[1,0]\n"
"v_pk_mul_f32 v[70:71], v[70:71], v[12:13] op_sel_hi:[1,0]\n"
"v_pk_mul_f32 v[164:165], v[164:165], v[10:11] op_sel_hi:[1,0]\n"
"v_pk_mul_f32 v[72:73], v[72:73], v[12:13] op_sel_hi:[1,0]\n"
"v_pk_mul_f32 v[166:167], v[166:167], v[10:11] op_sel_hi:[1,0]\n"
"v_pk_mul_f32 v[74:75], v[74:75], v[12:13] op_sel_hi:[1,0]\n"
"v_mfma_f32_32x32x16_bf16 v[224:239], v[92:95], a[88:91], v[224:239]\n"
"s_add_u32 s7, s79, 0\n"
"buffer_load_dwordx4 a[244:247], v6, s[48:51], s7 offen offset:0 sc1\n"
"v_cvt_pk_bf16_f32 v148, v148, v149\n"
"v_cvt_pk_bf16_f32 v149, v150, v151\n"
"v_cvt_pk_bf16_f32 v150, v152, v153\n"
"v_cvt_pk_bf16_f32 v151, v154, v155\n"
"v_cvt_pk_bf16_f32 v152, v156, v157\n"
"v_cvt_pk_bf16_f32 v153, v158, v159\n"
"v_cvt_pk_bf16_f32 v154, v160, v161\n"
"v_cvt_pk_bf16_f32 v155, v162, v163\n"
"v_cvt_pk_bf16_f32 v156, v164, v165\n"
"v_cvt_pk_bf16_f32 v157, v166, v167\n"
"v_cvt_pk_bf16_f32 v158, v56, v57\n"
"v_cvt_pk_bf16_f32 v159, v58, v59\n"
"v_cvt_pk_bf16_f32 v160, v60, v61\n"
"v_cvt_pk_bf16_f32 v161, v62, v63\n"
"v_cvt_pk_bf16_f32 v162, v64, v65\n"
"v_cvt_pk_bf16_f32 v163, v66, v67\n"
"v_cvt_pk_bf16_f32 v164, v68, v69\n"
"v_cvt_pk_bf16_f32 v165, v70, v71\n"
"v_cvt_pk_bf16_f32 v166, v72, v73\n"
"v_cvt_pk_bf16_f32 v167, v74, v75\n"
"ds_write_b128 v41, v[148:151] offset:160\n"
"ds_write_b128 v41, v[152:155] offset:176\n"
"ds_write_b128 v41, v[156:159] offset:192\n"
"ds_write_b128 v41, v[160:163] offset:208\n"
"ds_write_b128 v41, v[164:167] offset:224\n"
"s_waitcnt lgkmcnt(9)\n"
"v_mfma_f32_32x32x16_bf16 v[240:255], v[96:99], a[88:91], v[240:255]\n"
"s_waitcnt vmcnt(6) lgkmcnt(0)\n"
"s_barrier\n"
"ds_read_b64_tr_b16 v[80:81], v124 offset:20480\n"
"ds_read_b64_tr_b16 v[82:83], v124 offset:20640\n"
"v_mfma_f32_32x32x16_bf16 v[208:223], v[100:103], a[92:95], v[208:223]\n"
"ds_read_b64_tr_b16 v[84:85], v124 offset:20544\n"
"ds_read_b64_tr_b16 v[86:87], v124 offset:20704\n"
"buffer_load_dwordx4 a[248:251], v6, s[48:51], s7 offen offset:1024 sc1\n"
"v_mfma_f32_32x32x16_bf16 v[224:239], v[104:107], a[92:95], v[224:239]\n"
"ds_read_b64_tr_b16 v[88:89], v124 offset:20608\n"
"ds_read_b64_tr_b16 v[90:91], v124 offset:20768\n"
"buffer_load_dwordx4 a[252:255], v6, s[48:51], s7 offen offset:2048 sc1\n"
"ds_read_b64_tr_b16 v[56:57], v25 offset:0\n"
"v_mfma_f32_32x32x16_bf16 v[240:255], v[108:111], a[92:95], v[240:255]\n"
"s_add_u32 m0, s93, 0x10000\n"
"ds_read_b64_tr_b16 v[92:93], v124 offset:23040\n"
"ds_read_b64_tr_b16 v[94:95], v124 offset:23200\n"
"buffer_load_dwordx4 v117, s[40:43], s70 offen lds\n"
"ds_read_b64_tr_b16 v[58:59], v25 offset:176\n"
"s_waitcnt lgkmcnt(6)\n"
"v_mfma_f32_32x32x16_bf16 v[208:223], v[80:83], a[96:99], v[208:223]\n"
"ds_read_b64_tr_b16 v[96:97], v124 offset:23104\n"
"ds_read_b64_tr_b16 v[98:99], v124 offset:23264\n"
"buffer_load_dwordx4 v[192:195], v6, s[48:51], s7 offen offset:3072 sc1\n"
"ds_read_b64_tr_b16 v[60:61], v24 offset:0\n"
"v_mfma_f32_32x32x16_bf16 v[224:239], v[84:87], a[96:99], v[224:239]\n"
"ds_read_b64_tr_b16 v[100:101], v124 offset:23168\n"
"ds_read_b64_tr_b16 v[102:103], v124 offset:23328\n"
"buffer_load_dwordx4 v118, s[40:43], s70 offen offset:1024 lds\n"
"ds_read_b64_tr_b16 v[62:63], v24 offset:336\n"
"ds_read_b64_tr_b16 v[64:65], v24 offset:64\n"
"ds_read_b64_tr_b16 v[66:67], v24 offset:400\n"
"ds_read_b64_tr_b16 v[68:69], v25 offset:2816\n"
"s_waitcnt lgkmcnt(13)\n"
"ds_read_b64_tr_b16 v[70:71], v25 offset:2992\n"
"s_waitcnt lgkmcnt(11)\n"
"v_mfma_f32_32x32x16_bf16 v[240:255], v[88:91], a[96:99], v[240:255]\n"
"s_add_u32 s7, s7, 1024\n"
"ds_read_b64_tr_b16 v[104:105], v124 offset:25600\n"
"ds_read_b64_tr_b16 v[106:107], v124 offset:25760\n"
"buffer_load_dwordx4 v[196:199], v6, s[48:51], s7 offen offset:3072 sc1\n"
"ds_read_b64_tr_b16 v[72:73], v24 offset:5376\n"
"v_mfma_f32_32x32x16_bf16 v[208:223], v[92:95], a[100:103], v[208:223]\n"
"ds_read_b64_tr_b16 v[108:109], v124 offset:25664\n"
"s_waitcnt lgkmcnt(13)\n"
"ds_read_b64_tr_b16 v[110:111], v124 offset:25824\n"
"ds_read_b64_tr_b16 v[80:81], v124 offset:25728\n"
"s_waitcnt lgkmcnt(13)\n"
"ds_read_b64_tr_b16 v[82:83], v124 offset:25888\n"
"buffer_load_dwordx4 v119, s[40:43], s70 offen offset:2048 lds\n"
"ds_read_b64_tr_b16 v[74:75], v24 offset:5712\n"
"s_waitcnt lgkmcnt(13)\n"
"v_mfma_f32_32x32x16_bf16 v[224:239], v[96:99], a[100:103], v[224:239]\n"
"s_add_u32 s7, s79, 0\n"
"s_add_u32 s7, s7, s1\n"
"ds_read_b64_tr_b16 v[84:85], v124 offset:28160\n"
"ds_read_b64_tr_b16 v[86:87], v124 offset:28320\n"
"buffer_load_dwordx4 v[200:203], v6, s[48:51], s7 offen offset:0 sc1\n"
"s_waitcnt lgkmcnt(13)\n"
"ds_read_b64_tr_b16 v[76:77], v24 offset:5440\n"
"v_mfma_f32_32x32x16_bf16 v[240:255], v[100:103], a[100:103], v[240:255]\n"
"ds_read_b64_tr_b16 v[88:89], v124 offset:28224\n"
"s_waitcnt lgkmcnt(13)\n"
"ds_read_b64_tr_b16 v[90:91], v124 offset:28384\n"
"buffer_load_dwordx4 v120, s[40:43], s70 offen offset:3072 lds\n"
"ds_read_b64_tr_b16 v[78:79], v24 offset:5776\n"
"v_mfma_f32_32x32x16_bf16 v[128:143], v[60:63], v[56:59], 0\n"
"v_mfma_f32_32x32x16_bf16 v[144:159], v[64:67], v[56:59], 0\n"
"s_waitcnt lgkmcnt(13)\n"
"ds_read_b64_tr_b16 v[56:57], v25 offset:5632\n"
"s_waitcnt lgkmcnt(10)\n"
"v_mfma_f32_32x32x16_bf16 v[208:223], v[104:107], a[104:107], v[208:223]\n"
"ds_read_b64_tr_b16 v[92:93], v124 offset:28288\n"
"ds_read_b64_tr_b16 v[94:95], v124 offset:28448\n"
"buffer_load_dwordx4 v[204:207], v6, s[48:51], s7 offen offset:1024 sc1\n"
"ds_read_b64_tr_b16 v[58:59], v25 offset:5808\n"
"v_mfma_f32_32x32x16_bf16 v[224:239], v[108:111], a[104:107], v[224:239]\n"
"s_add_u32 m0, s93, 0x10c00\n"
"ds_read_b64_tr_b16 v[96:97], v124 offset:30720\n"
"ds_read_b64_tr_b16 v[98:99], v124 offset:30880\n"
"buffer_load_dwordx4 v121, s[40:43], s70 offen offset:1024 lds\n"
"s_waitcnt lgkmcnt(13)\n"
"ds_read_b64_tr_b16 v[60:61], v24 offset:10752\n"
"s_waitcnt lgkmcnt(11)\n"
"v_mfma_f32_32x32x16_bf16 v[240:255], v[80:83], a[104:107], v[240:255]\n"
"ds_read_b64_tr_b16 v[100:101], v124 offset:30784\n"
"ds_read_b64_tr_b16 v[102:103], v124 offset:30944\n"
"ds_read_b64_tr_b16 v[62:63], v24 offset:11088\n"
"ds_read_b64_tr_b16 v[64:65], v24 offset:10816\n"
"s_waitcnt lgkmcnt(13)\n"
"ds_read_b64_tr_b16 v[66:67], v24 offset:11152\n"
"v_mfma_f32_32x32x16_bf16 v[128:143], v[72:75], v[68:71], v[128:143]\n"
"s_waitcnt lgkmcnt(12)\n"
"v_mfma_f32_32x32x16_bf16 v[144:159], v[76:79], v[68:71], v[144:159]\n"
"ds_read_b64_tr_b16 v[68:69], v25 offset:8448\n"
"ds_read_b64_tr_b16 v[70:71], v25 offset:8624\n"
"v_mfma_f32_32x32x16_bf16 v[208:223], v[84:87], a[108:111], v[208:223]\n"
"s_add_u32 s7, s4, 10240\n"
"s_add_u32 s7, s7, s1\n"
"s_add_u32 m0, s96, 0xfffff400\n"
"ds_read_b64_tr_b16 v[104:105], v124 offset:30848\n"
"s_waitcnt lgkmcnt(13)\n"
"ds_read_b64_tr_b16 v[106:107], v124 offset:31008\n"
"buffer_load_dwordx4 v6, s[48:51], s7 offen offset:3072 sc1 lds\n"
"ds_read_b64_tr_b16 v[72:73], v24 offset:16128\n"
"s_waitcnt lgkmcnt(14)\n"
"v_mfma_f32_32x32x16_bf16 v[224:239], v[88:91], a[108:111], v[224:239]\n"
"ds_read_b64_tr_b16 v[108:109], v124 offset:33280\n"
"s_waitcnt lgkmcnt(13)\n"
"ds_read_b64_tr_b16 v[110:111], v124 offset:33440\n"
"ds_read_b64_tr_b16 v[74:75], v24 offset:16464\n"
"s_waitcnt lgkmcnt(13)\n"
"ds_read_b64_tr_b16 v[76:77], v24 offset:16192\n"
"ds_read_b64_tr_b16 v[78:79], v24 offset:16528\n"
"s_waitcnt lgkmcnt(12)\n"
"v_mfma_f32_32x32x16_bf16 v[128:143], v[60:63], v[56:59], v[128:143]\n"
"s_waitcnt lgkmcnt(10)\n"
"v_mfma_f32_32x32x16_bf16 v[144:159], v[64:67], v[56:59], v[144:159]\n"
"ds_read_b64_tr_b16 v[56:57], v25 offset:11264\n"
"ds_read_b64_tr_b16 v[58:59], v25 offset:11440\n"
"ds_read_b64_tr_b16 v[60:61], v24 offset:21504\n"
"v_mfma_f32_32x32x16_bf16 v[240:255], v[92:95], a[108:111], v[240:255]\n"
"s_add_u32 s7, s7, 1024\n"
"s_add_u32 m0, s96, 0xfffff800\n"
"ds_read_b64_tr_b16 v[80:81], v124 offset:33344\n"
"ds_read_b64_tr_b16 v[82:83], v124 offset:33504\n"
"s_waitcnt lgkmcnt(13)\n"
"ds_read_b64_tr_b16 v[84:85], v124 offset:33408\n"
"ds_read_b64_tr_b16 v[86:87], v124 offset:33568\n"
"buffer_load_dwordx4 v6, s[48:51], s7 offen offset:3072 sc1 lds\n"
"s_waitcnt lgkmcnt(13)\n"
"ds_read_b64_tr_b16 v[62:63], v24 offset:21840\n"
"v_mfma_f32_32x32x16_bf16 v[208:223], v[96:99], a[112:115], v[208:223]\n"
"ds_read_b64_tr_b16 v[88:89], v124 offset:35840\n"
"s_waitcnt lgkmcnt(13)\n"
"ds_read_b64_tr_b16 v[90:91], v124 offset:36000\n"
"ds_read_b64_tr_b16 v[64:65], v24 offset:21568\n"
"s_waitcnt lgkmcnt(13)\n"
"ds_read_b64_tr_b16 v[66:67], v24 offset:21904\n"
"v_mfma_f32_32x32x16_bf16 v[128:143], v[72:75], v[68:71], v[128:143]\n"
"s_waitcnt lgkmcnt(12)\n"
"v_mfma_f32_32x32x16_bf16 v[144:159], v[76:79], v[68:71], v[144:159]\n"
"ds_read_b64_tr_b16 v[68:69], v25 offset:14080\n"
"ds_read_b64_tr_b16 v[70:71], v25 offset:14256\n"
"ds_read_b64_tr_b16 v[72:73], v24 offset:26880\n"
"v_mfma_f32_32x32x16_bf16 v[224:239], v[100:103], a[112:115], v[224:239]\n"
"s_add_u32 s7, s79, 10240\n"
"s_add_u32 m0, s96, 0x800\n"
"s_waitcnt lgkmcnt(13)\n"
"ds_read_b64_tr_b16 v[92:93], v124 offset:35904\n"
"ds_read_b64_tr_b16 v[94:95], v124 offset:36064\n"
"buffer_load_dwordx4 v6, s[48:51], s7 offen sc1 lds\n"
"s_waitcnt lgkmcnt(13)\n"
"ds_read_b64_tr_b16 v[74:75], v24 offset:27216\n"
"v_mfma_f32_32x32x16_bf16 v[240:255], v[104:107], a[112:115], v[240:255]\n"
"ds_read_b64_tr_b16 v[96:97], v124 offset:35968\n"
"s_waitcnt lgkmcnt(13)\n"
"ds_read_b64_tr_b16 v[98:99], v124 offset:36128\n"
"ds_read_b64_tr_b16 v[76:77], v24 offset:26944\n"
"s_waitcnt lgkmcnt(13)\n"
"ds_read_b64_tr_b16 v[78:79], v24 offset:27280\n"
"v_mfma_f32_32x32x16_bf16 v[128:143], v[60:63], v[56:59], v[128:143]\n"
"s_waitcnt lgkmcnt(10)\n"
"v_mfma_f32_32x32x16_bf16 v[144:159], v[64:67], v[56:59], v[144:159]\n"
"ds_read_b64_tr_b16 v[56:57], v25 offset:16896\n"
"ds_read_b64_tr_b16 v[58:59], v25 offset:17072\n"
"ds_read_b64_tr_b16 v[60:61], v24 offset:32256\n"
"v_mfma_f32_32x32x16_bf16 v[208:223], v[108:111], a[116:119], v[208:223]\n"
"ds_read_b64_tr_b16 v[100:101], v124 offset:38400\n"
"ds_read_b64_tr_b16 v[102:103], v124 offset:38560\n"
"buffer_load_dwordx4 v6, s[48:51], s7 offen offset:1024 sc1 lds\n"
"s_waitcnt lgkmcnt(13)\n"
"ds_read_b64_tr_b16 v[62:63], v24 offset:32592\n"
"v_mfma_f32_32x32x16_bf16 v[224:239], v[80:83], a[116:119], v[224:239]\n"
"ds_read_b64_tr_b16 v[104:105], v124 offset:38464\n"
"s_waitcnt lgkmcnt(13)\n"
"ds_read_b64_tr_b16 v[106:107], v124 offset:38624\n"
"ds_read_b64_tr_b16 v[64:65], v24 offset:32320\n"
"s_waitcnt lgkmcnt(13)\n"
"ds_read_b64_tr_b16 v[66:67], v24 offset:32656\n"
"v_mfma_f32_32x32x16_bf16 v[128:143], v[72:75], v[68:71], v[128:143]\n"
"s_waitcnt lgkmcnt(10)\n"
"v_mfma_f32_32x32x16_bf16 v[144:159], v[76:79], v[68:71], v[144:159]\n"
"ds_read_b64_tr_b16 v[68:69], v25 offset:19712\n"
"ds_read_b64_tr_b16 v[70:71], v25 offset:19888\n"
"ds_read_b64_tr_b16 v[72:73], v24 offset:37632\n"
"ds_read_b64_tr_b16 v[74:75], v24 offset:37968\n"
"v_mfma_f32_32x32x16_bf16 v[240:255], v[84:87], a[116:119], v[240:255]\n"
"ds_read_b64_tr_b16 v[108:109], v124 offset:38528\n"
"s_waitcnt lgkmcnt(13)\n"
"ds_read_b64_tr_b16 v[110:111], v124 offset:38688\n"
"buffer_load_dwordx4 v6, s[48:51], s7 offen offset:2048 sc1 lds\n"
"ds_read_b64_tr_b16 v[76:77], v24 offset:37696\n"
"v_mfma_f32_32x32x16_bf16 v[208:223], v[88:91], a[120:123], v[208:223]\n"
"s_waitcnt lgkmcnt(13)\n"
"ds_read_b64_tr_b16 v[78:79], v24 offset:38032\n"
"s_waitcnt lgkmcnt(12)\n"
"v_mfma_f32_32x32x16_bf16 v[128:143], v[60:63], v[56:59], v[128:143]\n"
"s_waitcnt lgkmcnt(8)\n"
"v_mfma_f32_32x32x16_bf16 v[144:159], v[64:67], v[56:59], v[144:159]\n"
"s_waitcnt lgkmcnt(4)\n"
"v_mfma_f32_32x32x16_bf16 v[128:143], v[72:75], v[68:71], v[128:143]\n"
"s_waitcnt lgkmcnt(0)\n"
"v_mfma_f32_32x32x16_bf16 v[144:159], v[76:79], v[68:71], v[144:159]\n"
"s_nop 9\n"
"v_cvt_pk_bf16_f32 v160, v128, v129\n"
"v_cvt_pk_bf16_f32 v161, v130, v131\n"
"v_cvt_pk_bf16_f32 v162, v132, v133\n"
"v_cvt_pk_bf16_f32 v163, v134, v135\n"
"buffer_store_dwordx4 v[160:163], v26, s[48:51], s14 offen offset:0 sc1\n"
"v_cvt_pk_bf16_f32 v164, v136, v137\n"
"v_cvt_pk_bf16_f32 v165, v138, v139\n"
"v_cvt_pk_bf16_f32 v166, v140, v141\n"
"v_mfma_f32_32x32x16_bf16 v[224:239], v[92:95], a[120:123], v[224:239]\n"
"buffer_load_dwordx4 v6, s[48:51], s7 offen offset:3072 sc1 lds\n"
"v_cvt_pk_bf16_f32 v167, v142, v143\n"
"buffer_store_dwordx4 v[164:167], v26, s[48:51], s14 offen offset:672 sc1\n"
"v_cvt_pk_bf16_f32 v168, v144, v145\n"
"v_cvt_pk_bf16_f32 v169, v146, v147\n"
"v_cvt_pk_bf16_f32 v170, v148, v149\n"
"v_cvt_pk_bf16_f32 v171, v150, v151\n"
"buffer_store_dwordx4 v[168:171], v27, s[48:51], s14 offen offset:1344 sc1\n"
"v_mfma_f32_32x32x16_bf16 v[240:255], v[96:99], a[120:123], v[240:255]\n"
"s_waitcnt vmcnt(9)\n"
"s_barrier\n"
"ds_read_b64_tr_b16 v[80:81], v124 offset:0\n"
"ds_read_b64_tr_b16 v[82:83], v124 offset:160\n"
"v_mfma_f32_32x32x16_bf16 v[208:223], v[100:103], a[124:127], v[208:223]\n"
"s_add_u32 s7, s7, 1024\n"
"s_add_u32 m0, s96, 0xc00\n"
"ds_read_b64_tr_b16 v[84:85], v124 offset:64\n"
"ds_read_b64_tr_b16 v[86:87], v124 offset:224\n"
"buffer_load_dwordx4 v6, s[48:51], s7 offen offset:3072 sc1 lds\n"
"v_mfma_f32_32x32x16_bf16 v[224:239], v[104:107], a[124:127], v[224:239]\n"
"ds_read_b64_tr_b16 v[88:89], v124 offset:128\n"
"ds_read_b64_tr_b16 v[90:91], v124 offset:288\n"
"v_mfma_f32_32x32x16_bf16 v[240:255], v[108:111], a[124:127], v[240:255]\n"
"s_add_u32 s7, s79, 10240\n"
"s_add_u32 s7, s7, s1\n"
"s_add_u32 m0, s96, 0x1c00\n"
"ds_read_b64_tr_b16 v[92:93], v124 offset:2560\n"
"ds_read_b64_tr_b16 v[94:95], v124 offset:2720\n"
"buffer_load_dwordx4 v6, s[48:51], s7 offen sc1 lds\n"
"s_waitcnt lgkmcnt(4)\n"
"v_mfma_f32_32x32x16_bf16 v[208:223], v[80:83], a[128:131], v[208:223]\n"
"ds_read_b64_tr_b16 v[96:97], v124 offset:2624\n"
"ds_read_b64_tr_b16 v[98:99], v124 offset:2784\n"
"v_mfma_f32_32x32x16_bf16 v[224:239], v[84:87], a[128:131], v[224:239]\n"
"ds_read_b64_tr_b16 v[100:101], v124 offset:2688\n"
"ds_read_b64_tr_b16 v[102:103], v124 offset:2848\n"
"buffer_load_dwordx4 v6, s[48:51], s7 offen offset:1024 sc1 lds\n"
"s_waitcnt lgkmcnt(4)\n"
"v_mfma_f32_32x32x16_bf16 v[240:255], v[88:91], a[128:131], v[240:255]\n"
"ds_read_b64_tr_b16 v[104:105], v124 offset:5120\n"
"ds_read_b64_tr_b16 v[106:107], v124 offset:5280\n"
"v_mfma_f32_32x32x16_bf16 v[208:223], v[92:95], a[132:135], v[208:223]\n"
"ds_read_b64_tr_b16 v[108:109], v124 offset:5184\n"
"ds_read_b64_tr_b16 v[110:111], v124 offset:5344\n"
"ds_read_b64_tr_b16 v[80:81], v124 offset:5248\n"
"ds_read_b64_tr_b16 v[82:83], v124 offset:5408\n"
"buffer_load_dwordx4 v6, s[48:51], s7 offen offset:2048 sc1 lds\n"
"s_waitcnt lgkmcnt(6)\n"
"v_mfma_f32_32x32x16_bf16 v[224:239], v[96:99], a[132:135], v[224:239]\n"
"ds_read_b64_tr_b16 v[84:85], v124 offset:7680\n"
"ds_read_b64_tr_b16 v[86:87], v124 offset:7840\n"
"v_mfma_f32_32x32x16_bf16 v[240:255], v[100:103], a[132:135], v[240:255]\n"
"ds_read_b64_tr_b16 v[88:89], v124 offset:7744\n"
"ds_read_b64_tr_b16 v[90:91], v124 offset:7904\n"
"s_waitcnt lgkmcnt(6)\n"
"v_mfma_f32_32x32x16_bf16 v[208:223], v[104:107], a[136:139], v[208:223]\n"
"ds_read_b64_tr_b16 v[92:93], v124 offset:7808\n"
"ds_read_b64_tr_b16 v[94:95], v124 offset:7968\n"
"v_mfma_f32_32x32x16_bf16 v[224:239], v[108:111], a[136:139], v[224:239]\n"
"ds_read_b64_tr_b16 v[96:97], v124 offset:10240\n"
"ds_read_b64_tr_b16 v[98:99], v124 offset:10400\n"
"s_waitcnt lgkmcnt(6)\n"
"v_mfma_f32_32x32x16_bf16 v[240:255], v[80:83], a[136:139], v[240:255]\n"
"ds_read_b64_tr_b16 v[100:101], v124 offset:10304\n"
"ds_read_b64_tr_b16 v[102:103], v124 offset:10464\n"
"v_mfma_f32_32x32x16_bf16 v[208:223], v[84:87], a[140:143], v[208:223]\n"
"ds_read_b64_tr_b16 v[104:105], v124 offset:10368\n"
"ds_read_b64_tr_b16 v[106:107], v124 offset:10528\n"
"s_waitcnt lgkmcnt(6)\n"
"v_mfma_f32_32x32x16_bf16 v[224:239], v[88:91], a[140:143], v[224:239]\n"
"ds_read_b64_tr_b16 v[108:109], v124 offset:12800\n"
"ds_read_b64_tr_b16 v[110:111], v124 offset:12960\n"
"v_mfma_f32_32x32x16_bf16 v[240:255], v[92:95], a[140:143], v[240:255]\n"
"ds_read_b64_tr_b16 v[80:81], v124 offset:12864\n"
"ds_read_b64_tr_b16 v[82:83], v124 offset:13024\n"
"ds_read_b64_tr_b16 v[84:85], v124 offset:12928\n"
"ds_read_b64_tr_b16 v[86:87], v124 offset:13088\n"
"s_waitcnt lgkmcnt(8)\n"
"v_mfma_f32_32x32x16_bf16 v[208:223], v[96:99], a[144:147], v[208:223]\n"
"ds_read_b64_tr_b16 v[88:89], v124 offset:15360\n"
"ds_read_b64_tr_b16 v[90:91], v124 offset:15520\n"
"v_mfma_f32_32x32x16_bf16 v[224:239], v[100:103], a[144:147], v[224:239]\n"
"ds_read_b64_tr_b16 v[92:93], v124 offset:15424\n"
"ds_read_b64_tr_b16 v[94:95], v124 offset:15584\n"
"s_waitcnt lgkmcnt(8)\n"
"v_mfma_f32_32x32x16_bf16 v[240:255], v[104:107], a[144:147], v[240:255]\n"
"ds_read_b64_tr_b16 v[96:97], v124 offset:15488\n"
"ds_read_b64_tr_b16 v[98:99], v124 offset:15648\n"
"v_mfma_f32_32x32x16_bf16 v[208:223], v[108:111], a[148:151], v[208:223]\n"
"ds_read_b64_tr_b16 v[100:101], v124 offset:17920\n"
"ds_read_b64_tr_b16 v[102:103], v124 offset:18080\n"
"s_waitcnt lgkmcnt(8)\n"
"v_mfma_f32_32x32x16_bf16 v[224:239], v[80:83], a[148:151], v[224:239]\n"
"ds_read_b64_tr_b16 v[104:105], v124 offset:17984\n"
"ds_read_b64_tr_b16 v[106:107], v124 offset:18144\n"
"v_mfma_f32_32x32x16_bf16 v[240:255], v[84:87], a[148:151], v[240:255]\n"
"ds_read_b64_tr_b16 v[108:109], v124 offset:18048\n"
"ds_read_b64_tr_b16 v[110:111], v124 offset:18208\n"
"s_waitcnt lgkmcnt(8)\n"
"v_mfma_f32_32x32x16_bf16 v[208:223], v[88:91], a[152:155], v[208:223]\n"
"v_mfma_f32_32x32x16_bf16 v[224:239], v[92:95], a[152:155], v[224:239]\n"
"s_waitcnt lgkmcnt(4)\n"
"v_mfma_f32_32x32x16_bf16 v[240:255], v[96:99], a[152:155], v[240:255]\n"
"v_mfma_f32_32x32x16_bf16 v[208:223], v[100:103], a[156:159], v[208:223]\n"
"s_waitcnt lgkmcnt(0)\n"
"v_mfma_f32_32x32x16_bf16 v[224:239], v[104:107], a[156:159], v[224:239]\n"
"v_mfma_f32_32x32x16_bf16 v[240:255], v[108:111], a[156:159], v[240:255]\n"
"s_nop 8\n"
"v_cvt_pk_bf16_f32 v56, v208, v209\n"
"v_cvt_pk_bf16_f32 v57, v210, v211\n"
"v_cvt_pk_bf16_f32 v58, v212, v213\n"
"v_cvt_pk_bf16_f32 v59, v214, v215\n"
"v_cvt_pk_bf16_f32 v60, v216, v217\n"
"v_cvt_pk_bf16_f32 v61, v218, v219\n"
"v_cvt_pk_bf16_f32 v62, v220, v221\n"
"v_cvt_pk_bf16_f32 v63, v222, v223\n"
"v_cvt_pk_bf16_f32 v64, v224, v225\n"
"v_cvt_pk_bf16_f32 v65, v226, v227\n"
"v_cvt_pk_bf16_f32 v66, v228, v229\n"
"v_cvt_pk_bf16_f32 v67, v230, v231\n"
"v_cvt_pk_bf16_f32 v68, v232, v233\n"
"v_cvt_pk_bf16_f32 v69, v234, v235\n"
"v_cvt_pk_bf16_f32 v70, v236, v237\n"
"v_cvt_pk_bf16_f32 v71, v238, v239\n"
"v_cvt_pk_bf16_f32 v72, v240, v241\n"
"v_cvt_pk_bf16_f32 v73, v242, v243\n"
"v_cvt_pk_bf16_f32 v74, v244, v245\n"
"v_cvt_pk_bf16_f32 v75, v246, v247\n"
"v_mfma_f32_32x32x16_bf16 v[208:223], a[160:163], v[56:59], 0\n"
"v_mfma_f32_32x32x16_bf16 v[208:223], a[164:167], v[60:63], v[208:223]\n"
"v_mfma_f32_32x32x16_bf16 v[208:223], a[168:171], v[64:67], v[208:223]\n"
"v_mfma_f32_32x32x16_bf16 v[224:239], a[172:175], v[56:59], 0\n"
"v_mfma_f32_32x32x16_bf16 v[224:239], a[176:179], v[60:63], v[224:239]\n"
"v_mfma_f32_32x32x16_bf16 v[224:239], a[180:183], v[64:67], v[224:239]\n"
"v_mfma_f32_32x32x16_bf16 v[224:239], a[184:187], v[68:71], v[224:239]\n"
"v_mfma_f32_32x32x16_bf16 v[224:239], a[188:191], v[72:75], v[224:239]\n"
"v_mfma_f32_32x32x16_bf16 v[240:255], a[192:195], v[64:67], 0\n"
"v_mfma_f32_32x32x16_bf16 v[240:255], a[196:199], v[68:71], v[240:255]\n"
"v_mfma_f32_32x32x16_bf16 v[240:255], a[200:203], v[72:75], v[240:255]\n"
"s_lshl_b32 s82, s78, 7\n"
"s_add_u32 s2, s82, 9216\n"
"s_mov_b32 s94, 0xffff\n"
"s_mov_b32 s95, 0\n"
"v_lshlrev_b32 v43, 3, v1\n"
"s_waitcnt vmcnt(4)\n"
"s_barrier\n"
"buffer_store_dwordx2 v[2:3], v4, s[48:51], s92 offen offset:0 sc1\n"
"buffer_store_dwordx2 v[2:3], v4, s[48:51], s87 offen offset:16 sc1\n"
"v_max3_f32 v8, v208, v209, v210\n"
"s_add_u32 s7, s79, 0\n"
"s_add_u32 s7, s7, s1\n"
"buffer_load_dwordx4 a[160:163], v6, s[48:51], s7 offen offset:2048 sc1\n"
"v_min3_f32 v9, v208, v209, v210\n"
"v_max3_f32 v10, v211, v212, v213\n"
"v_min3_f32 v12, v211, v212, v213\n"
"v_max3_f32 v8, v8, v214, v215\n"
"v_min3_f32 v9, v9, v214, v215\n"
"v_max3_f32 v10, v10, v216, v217\n"
"v_min3_f32 v12, v12, v216, v217\n"
"v_max3_f32 v8, v8, v218, v219\n"
"v_min3_f32 v9, v9, v218, v219\n"
"v_max3_f32 v10, v10, v220, v221\n"
"buffer_load_dwordx4 a[164:167], v6, s[48:51], s7 offen offset:3072 sc1\n"
"v_min3_f32 v12, v12, v220, v221\n"
"v_max3_f32 v8, v8, v222, v223\n"
"v_min3_f32 v9, v9, v222, v223\n"
"v_max3_f32 v10, v10, v224, v225\n"
"v_min3_f32 v12, v12, v224, v225\n"
"v_max3_f32 v8, v8, v226, v227\n"
"v_min3_f32 v9, v9, v226, v227\n"
"v_max_f32 v8, v8, v10\n"
"v_min_f32 v9, v9, v12\n"
"v_mul_f32 v8, s98, v8\n"
"v_mul_f32 v9, s98, v9\n"
"s_add_u32 s7, s7, 1024\n"
"buffer_load_dwordx4 a[168:171], v6, s[48:51], s7 offen offset:3072 sc1\n"
"v_pk_fma_f32 v[56:57], v[208:209], s[98:99], v[8:9] op_sel:[0,0,1] op_sel_hi:[1,0,1] neg_lo:[1,0,0] neg_hi:[1,0,0]\n"
"v_pk_fma_f32 v[208:209], v[208:209], s[98:99], v[8:9] op_sel_hi:[1,0,0] neg_lo:[0,0,1] neg_hi:[0,0,1]\n"
"v_pk_fma_f32 v[58:59], v[210:211], s[98:99], v[8:9] op_sel:[0,0,1] op_sel_hi:[1,0,1] neg_lo:[1,0,0] neg_hi:[1,0,0]\n"
"v_pk_fma_f32 v[210:211], v[210:211], s[98:99], v[8:9] op_sel_hi:[1,0,0] neg_lo:[0,0,1] neg_hi:[0,0,1]\n"
"v_exp_f32 v56, v56\n"
"v_exp_f32 v208, v208\n"
"v_exp_f32 v57, v57\n"
"v_exp_f32 v209, v209\n"
"v_pk_fma_f32 v[60:61], v[212:213], s[98:99], v[8:9] op_sel:[0,0,1] op_sel_hi:[1,0,1] neg_lo:[1,0,0] neg_hi:[1,0,0]\n"
"v_pk_fma_f32 v[212:213], v[212:213], s[98:99], v[8:9] op_sel_hi:[1,0,0] neg_lo:[0,0,1] neg_hi:[0,0,1]\n"
"v_exp_f32 v58, v58\n"
"s_add_u32 s7, s4, 10240\n"
"buffer_load_dwordx4 a[172:175], v6, s[48:51], s7 offen offset:0 sc1\n"
"v_exp_f32 v210, v210\n"
"v_exp_f32 v59, v59\n"
"v_exp_f32 v211, v211\n"
"v_pk_fma_f32 v[62:63], v[214:215], s[98:99], v[8:9] op_sel:[0,0,1] op_sel_hi:[1,0,1] neg_lo:[1,0,0] neg_hi:[1,0,0]\n"
"v_pk_fma_f32 v[214:215], v[214:215], s[98:99], v[8:9] op_sel_hi:[1,0,0] neg_lo:[0,0,1] neg_hi:[0,0,1]\n"
"v_exp_f32 v60, v60\n"
"v_exp_f32 v212, v212\n"
"v_pk_add_f32 v[12:13], v[56:57], v[58:59]\n"
"v_exp_f32 v61, v61\n"
"v_exp_f32 v213, v213\n"
"v_pk_add_f32 v[10:11], v[208:209], v[210:211]\n"
"buffer_load_dwordx4 a[176:179], v6, s[48:51], s7 offen offset:1024 sc1\n"
"v_pk_fma_f32 v[64:65], v[216:217], s[98:99], v[8:9] op_sel:[0,0,1] op_sel_hi:[1,0,1] neg_lo:[1,0,0] neg_hi:[1,0,0]\n"
"v_pk_fma_f32 v[216:217], v[216:217], s[98:99], v[8:9] op_sel_hi:[1,0,0] neg_lo:[0,0,1] neg_hi:[0,0,1]\n"
"v_exp_f32 v62, v62\n"
"v_exp_f32 v214, v214\n"
"v_pk_add_f32 v[12:13], v[12:13], v[60:61]\n"
"v_exp_f32 v63, v63\n"
"v_exp_f32 v215, v215\n"
"v_pk_add_f32 v[10:11], v[10:11], v[212:213]\n"
"v_pk_fma_f32 v[66:67], v[218:219], s[98:99], v[8:9] op_sel:[0,0,1] op_sel_hi:[1,0,1] neg_lo:[1,0,0] neg_hi:[1,0,0]\n"
"v_pk_fma_f32 v[218:219], v[218:219], s[98:99], v[8:9] op_sel_hi:[1,0,0] neg_lo:[0,0,1] neg_hi:[0,0,1]\n"
"v_exp_f32 v64, v64\n"
"buffer_load_dwordx4 a[180:183], v6, s[48:51], s7 offen offset:2048 sc1\n"
"v_exp_f32 v216, v216\n"
"v_pk_add_f32 v[12:13], v[12:13], v[62:63]\n"
"v_exp_f32 v65, v65\n"
"v_exp_f32 v217, v217\n"
"v_pk_add_f32 v[10:11], v[10:11], v[214:215]\n"
"v_pk_fma_f32 v[68:69], v[220:221], s[98:99], v[8:9] op_sel:[0,0,1] op_sel_hi:[1,0,1] neg_lo:[1,0,0] neg_hi:[1,0,0]\n"
"v_pk_fma_f32 v[220:221], v[220:221], s[98:99], v[8:9] op_sel_hi:[1,0,0] neg_lo:[0,0,1] neg_hi:[0,0,1]\n"
"v_exp_f32 v66, v66\n"
"v_exp_f32 v218, v218\n"
"v_pk_add_f32 v[12:13], v[12:13], v[64:65]\n"
"buffer_load_dwordx4 a[184:187], v6, s[48:51], s7 offen offset:3072 sc1\n"
"v_exp_f32 v67, v67\n"
"v_exp_f32 v219, v219\n"
"v_pk_add_f32 v[10:11], v[10:11], v[216:217]\n"
"v_pk_fma_f32 v[70:71], v[222:223], s[98:99], v[8:9] op_sel:[0,0,1] op_sel_hi:[1,0,1] neg_lo:[1,0,0] neg_hi:[1,0,0]\n"
"v_pk_fma_f32 v[222:223], v[222:223], s[98:99], v[8:9] op_sel_hi:[1,0,0] neg_lo:[0,0,1] neg_hi:[0,0,1]\n"
"v_exp_f32 v68, v68\n"
"v_exp_f32 v220, v220\n"
"v_pk_add_f32 v[12:13], v[12:13], v[66:67]\n"
"v_exp_f32 v69, v69\n"
"v_exp_f32 v221, v221\n"
"v_pk_add_f32 v[10:11], v[10:11], v[218:219]\n"
"s_add_u32 s7, s7, 1024\n"
"buffer_load_dwordx4 a[188:191], v6, s[48:51], s7 offen offset:3072 sc1\n"
"v_pk_fma_f32 v[72:73], v[224:225], s[98:99], v[8:9] op_sel:[0,0,1] op_sel_hi:[1,0,1] neg_lo:[1,0,0] neg_hi:[1,0,0]\n"
"v_pk_fma_f32 v[224:225], v[224:225], s[98:99], v[8:9] op_sel_hi:[1,0,0] neg_lo:[0,0,1] neg_hi:[0,0,1]\n"
"v_exp_f32 v70, v70\n"
"v_exp_f32 v222, v222\n"
"v_pk_add_f32 v[12:13], v[12:13], v[68:69]\n"
"v_exp_f32 v71, v71\n"
"v_exp_f32 v223, v223\n"
"v_pk_add_f32 v[10:11], v[10:11], v[220:221]\n"
"v_pk_fma_f32 v[74:75], v[226:227], s[98:99], v[8:9] op_sel:[0,0,1] op_sel_hi:[1,0,1] neg_lo:[1,0,0] neg_hi:[1,0,0]\n"
"v_pk_fma_f32 v[226:227], v[226:227], s[98:99], v[8:9] op_sel_hi:[1,0,0] neg_lo:[0,0,1] neg_hi:[0,0,1]\n"
"v_exp_f32 v72, v72\n"
"s_add_u32 s7, s4, 10240\n"
"s_add_u32 s7, s7, s1\n"
"buffer_load_dwordx4 a[192:195], v6, s[48:51], s7 offen offset:0 sc1\n"
"v_exp_f32 v224, v224\n"
"v_pk_add_f32 v[12:13], v[12:13], v[70:71]\n"
"v_exp_f32 v73, v73\n"
"v_exp_f32 v225, v225\n"
"v_pk_add_f32 v[10:11], v[10:11], v[222:223]\n"
"v_exp_f32 v74, v74\n"
"v_exp_f32 v226, v226\n"
"v_pk_add_f32 v[12:13], v[12:13], v[72:73]\n"
"v_exp_f32 v75, v75\n"
"v_exp_f32 v227, v227\n"
"v_pk_add_f32 v[10:11], v[10:11], v[224:225]\n"
"buffer_load_dwordx4 a[196:199], v6, s[48:51], s7 offen offset:1024 sc1\n"
"v_pk_add_f32 v[12:13], v[12:13], v[74:75]\n"
"v_pk_add_f32 v[10:11], v[10:11], v[226:227]\n"
"v_add_f32 v12, v12, v13\n"
"v_add_f32 v10, v10, v11\n"
"v_rcp_f32 v12, v12\n"
"v_rcp_f32 v10, v10\n"
"s_nop 0\n"
"v_pk_mul_f32 v[208:209], v[208:209], v[10:11] op_sel_hi:[1,0]\n"
"v_pk_mul_f32 v[210:211], v[210:211], v[10:11] op_sel_hi:[1,0]\n"
"v_cvt_pk_bf16_f32 v208, v208, v209\n"
"v_pk_mul_f32 v[212:213], v[212:213], v[10:11] op_sel_hi:[1,0]\n"
"v_cvt_pk_bf16_f32 v209, v210, v211\n"
"buffer_load_dwordx4 a[200:203], v6, s[48:51], s7 offen offset:2048 sc1\n"
"v_pk_mul_f32 v[214:215], v[214:215], v[10:11] op_sel_hi:[1,0]\n"
"v_cvt_pk_bf16_f32 v210, v212, v213\n"
"v_pk_mul_f32 v[216:217], v[216:217], v[10:11] op_sel_hi:[1,0]\n"
"v_cvt_pk_bf16_f32 v211, v214, v215\n"
"v_pk_mul_f32 v[218:219], v[218:219], v[10:11] op_sel_hi:[1,0]\n"
"v_cvt_pk_bf16_f32 v212, v216, v217\n"
"v_pk_mul_f32 v[220:221], v[220:221], v[10:11] op_sel_hi:[1,0]\n"
"v_cvt_pk_bf16_f32 v213, v218, v219\n"
"v_pk_mul_f32 v[222:223], v[222:223], v[10:11] op_sel_hi:[1,0]\n"
"v_cvt_pk_bf16_f32 v214, v220, v221\n"
"s_add_u32 s7, s79, 10240\n"
"s_add_u32 s7, s7, s1\n"
"s_add_u32 m0, s99, 0xfffff400\n"
"s_nop 0\n"
"buffer_load_dwordx4 v6, s[48:51], s7 offen offset:3072 sc1 lds\n"
"v_pk_mul_f32 v[224:225], v[224:225], v[10:11] op_sel_hi:[1,0]\n"
"v_cvt_pk_bf16_f32 v215, v222, v223\n"
"v_pk_mul_f32 v[226:227], v[226:227], v[10:11] op_sel_hi:[1,0]\n"
"v_cvt_pk_bf16_f32 v216, v224, v225\n"
"v_cvt_pk_bf16_f32 v217, v226, v227\n"
"v_pk_mul_f32 v[56:57], v[56:57], v[12:13] op_sel_hi:[1,0]\n"
"v_pk_mul_f32 v[58:59], v[58:59], v[12:13] op_sel_hi:[1,0]\n"
"v_cvt_pk_bf16_f32 v218, v56, v57\n"
"v_pk_mul_f32 v[60:61], v[60:61], v[12:13] op_sel_hi:[1,0]\n"
"v_cvt_pk_bf16_f32 v219, v58, v59\n"
"v_pk_mul_f32 v[62:63], v[62:63], v[12:13] op_sel_hi:[1,0]\n"
"s_add_u32 s7, s7, 1024\n"
"s_add_u32 m0, s99, 0xfffff800\n"
"s_nop 0\n"
"buffer_load_dwordx4 v6, s[48:51], s7 offen offset:3072 sc1 lds\n"
"v_cvt_pk_bf16_f32 v220, v60, v61\n"
"v_pk_mul_f32 v[64:65], v[64:65], v[12:13] op_sel_hi:[1,0]\n"
"v_cvt_pk_bf16_f32 v221, v62, v63\n"
"v_pk_mul_f32 v[66:67], v[66:67], v[12:13] op_sel_hi:[1,0]\n"
"v_cvt_pk_bf16_f32 v222, v64, v65\n"
"v_pk_mul_f32 v[68:69], v[68:69], v[12:13] op_sel_hi:[1,0]\n"
"v_cvt_pk_bf16_f32 v223, v66, v67\n"
"v_pk_mul_f32 v[70:71], v[70:71], v[12:13] op_sel_hi:[1,0]\n"
"v_cvt_pk_bf16_f32 v224, v68, v69\n"
"v_pk_mul_f32 v[72:73], v[72:73], v[12:13] op_sel_hi:[1,0]\n"
"v_cvt_pk_bf16_f32 v225, v70, v71\n"
"s_add_u32 s7, s5, 0\n"
"s_add_u32 m0, s99, 0x800\n"
"s_nop 0\n"
"buffer_load_dwordx4 v6, s[48:51], s7 offen sc1 lds\n"
"v_pk_mul_f32 v[74:75], v[74:75], v[12:13] op_sel_hi:[1,0]\n"
"v_cvt_pk_bf16_f32 v226, v72, v73\n"
"v_cvt_pk_bf16_f32 v227, v74, v75\n"
"v_max3_f32 v8, v228, v229, v230\n"
"v_min3_f32 v9, v228, v229, v230\n"
"v_max3_f32 v10, v231, v232, v233\n"
"v_min3_f32 v12, v231, v232, v233\n"
"v_max3_f32 v8, v8, v234, v235\n"
"v_min3_f32 v9, v9, v234, v235\n"
"v_max3_f32 v10, v10, v236, v237\n"
"v_min3_f32 v12, v12, v236, v237\n"
"buffer_load_dwordx4 v6, s[48:51], s7 offen offset:1024 sc1 lds\n"
"v_max3_f32 v8, v8, v238, v239\n"
"v_min3_f32 v9, v9, v238, v239\n"
"v_max3_f32 v10, v10, v240, v241\n"
"v_min3_f32 v12, v12, v240, v241\n"
"v_max3_f32 v8, v8, v242, v243\n"
"v_min3_f32 v9, v9, v242, v243\n"
"v_max3_f32 v10, v10, v244, v245\n"
"v_min3_f32 v12, v12, v244, v245\n"
"v_max3_f32 v8, v8, v246, v247\n"
"v_min3_f32 v9, v9, v246, v247\n"
"v_max_f32 v8, v8, v10\n"
"buffer_load_dwordx4 v6, s[48:51], s7 offen offset:2048 sc1 lds\n"
"v_min_f32 v9, v9, v12\n"
"v_mul_f32 v8, s98, v8\n"
"v_mul_f32 v9, s98, v9\n"
"v_pk_fma_f32 v[56:57], v[228:229], s[98:99], v[8:9] op_sel:[0,0,1] op_sel_hi:[1,0,1] neg_lo:[1,0,0] neg_hi:[1,0,0]\n"
"v_pk_fma_f32 v[228:229], v[228:229], s[98:99], v[8:9] op_sel_hi:[1,0,0] neg_lo:[0,0,1] neg_hi:[0,0,1]\n"
"v_pk_fma_f32 v[58:59], v[230:231], s[98:99], v[8:9] op_sel:[0,0,1] op_sel_hi:[1,0,1] neg_lo:[1,0,0] neg_hi:[1,0,0]\n"
"v_pk_fma_f32 v[230:231], v[230:231], s[98:99], v[8:9] op_sel_hi:[1,0,0] neg_lo:[0,0,1] neg_hi:[0,0,1]\n"
"v_exp_f32 v56, v56\n"
"v_exp_f32 v228, v228\n"
"v_exp_f32 v57, v57\n"
"buffer_load_dwordx4 v6, s[48:51], s7 offen offset:3072 sc1 lds\n"
"v_exp_f32 v229, v229\n"
"v_pk_fma_f32 v[60:61], v[232:233], s[98:99], v[8:9] op_sel:[0,0,1] op_sel_hi:[1,0,1] neg_lo:[1,0,0] neg_hi:[1,0,0]\n"
"v_pk_fma_f32 v[232:233], v[232:233], s[98:99], v[8:9] op_sel_hi:[1,0,0] neg_lo:[0,0,1] neg_hi:[0,0,1]\n"
"v_exp_f32 v58, v58\n"
"v_exp_f32 v230, v230\n"
"v_exp_f32 v59, v59\n"
"v_exp_f32 v231, v231\n"
"v_pk_fma_f32 v[62:63], v[234:235], s[98:99], v[8:9] op_sel:[0,0,1] op_sel_hi:[1,0,1] neg_lo:[1,0,0] neg_hi:[1,0,0]\n"
"v_pk_fma_f32 v[234:235], v[234:235], s[98:99], v[8:9] op_sel_hi:[1,0,0] neg_lo:[0,0,1] neg_hi:[0,0,1]\n"
"v_exp_f32 v60, v60\n"
"v_exp_f32 v232, v232\n"
"s_add_u32 s7, s7, 1024\n"
"s_add_u32 m0, s99, 0xc00\n"
"s_nop 0\n"
"buffer_load_dwordx4 v6, s[48:51], s7 offen offset:3072 sc1 lds\n"
"v_pk_add_f32 v[12:13], v[56:57], v[58:59]\n"
"v_exp_f32 v61, v61\n"
"v_exp_f32 v233, v233\n"
"v_pk_add_f32 v[10:11], v[228:229], v[230:231]\n"
"v_pk_fma_f32 v[64:65], v[236:237], s[98:99], v[8:9] op_sel:[0,0,1] op_sel_hi:[1,0,1] neg_lo:[1,0,0] neg_hi:[1,0,0]\n"
"v_pk_fma_f32 v[236:237], v[236:237], s[98:99], v[8:9] op_sel_hi:[1,0,0] neg_lo:[0,0,1] neg_hi:[0,0,1]\n"
"v_exp_f32 v62, v62\n"
"v_exp_f32 v234, v234\n"
"v_pk_add_f32 v[12:13], v[12:13], v[60:61]\n"
"v_exp_f32 v63, v63\n"
"v_exp_f32 v235, v235\n"
"s_add_u32 s7, s5, 0\n"
"s_add_u32 s7, s7, s1\n"
"s_add_u32 m0, s99, 0x1c00\n"
"s_nop 0\n"
"buffer_load_dwordx4 v6, s[48:51], s7 offen sc1 lds\n"
"v_pk_add_f32 v[10:11], v[10:11], v[232:233]\n"
"v_pk_fma_f32 v[66:67], v[238:239], s[98:99], v[8:9] op_sel:[0,0,1] op_sel_hi:[1,0,1] neg_lo:[1,0,0] neg_hi:[1,0,0]\n"
"v_pk_fma_f32 v[238:239], v[238:239], s[98:99], v[8:9] op_sel_hi:[1,0,0] neg_lo:[0,0,1] neg_hi:[0,0,1]\n"
"v_exp_f32 v64, v64\n"
"v_exp_f32 v236, v236\n"
"v_pk_add_f32 v[12:13], v[12:13], v[62:63]\n"
"v_exp_f32 v65, v65\n"
"v_exp_f32 v237, v237\n"
"v_pk_add_f32 v[10:11], v[10:11], v[234:235]\n"
"v_pk_fma_f32 v[68:69], v[240:241], s[98:99], v[8:9] op_sel:[0,0,1] op_sel_hi:[1,0,1] neg_lo:[1,0,0] neg_hi:[1,0,0]\n"
"v_pk_fma_f32 v[240:241], v[240:241], s[98:99], v[8:9] op_sel_hi:[1,0,0] neg_lo:[0,0,1] neg_hi:[0,0,1]\n"
"buffer_load_dwordx4 v6, s[48:51], s7 offen offset:1024 sc1 lds\n"
"v_exp_f32 v66, v66\n"
"v_exp_f32 v238, v238\n"
"v_pk_add_f32 v[12:13], v[12:13], v[64:65]\n"
"v_exp_f32 v67, v67\n"
"v_exp_f32 v239, v239\n"
"v_pk_add_f32 v[10:11], v[10:11], v[236:237]\n"
"v_pk_fma_f32 v[70:71], v[242:243], s[98:99], v[8:9] op_sel:[0,0,1] op_sel_hi:[1,0,1] neg_lo:[1,0,0] neg_hi:[1,0,0]\n"
"v_pk_fma_f32 v[242:243], v[242:243], s[98:99], v[8:9] op_sel_hi:[1,0,0] neg_lo:[0,0,1] neg_hi:[0,0,1]\n"
"v_exp_f32 v68, v68\n"
"v_exp_f32 v240, v240\n"
"v_pk_add_f32 v[12:13], v[12:13], v[66:67]\n"
"buffer_load_dwordx4 v6, s[48:51], s7 offen offset:2048 sc1 lds\n"
"v_exp_f32 v69, v69\n"
"v_exp_f32 v241, v241\n"
"v_pk_add_f32 v[10:11], v[10:11], v[238:239]\n"
"v_pk_fma_f32 v[72:73], v[244:245], s[98:99], v[8:9] op_sel:[0,0,1] op_sel_hi:[1,0,1] neg_lo:[1,0,0] neg_hi:[1,0,0]\n"
"v_pk_fma_f32 v[244:245], v[244:245], s[98:99], v[8:9] op_sel_hi:[1,0,0] neg_lo:[0,0,1] neg_hi:[0,0,1]\n"
"v_exp_f32 v70, v70\n"
"v_exp_f32 v242, v242\n"
"v_pk_add_f32 v[12:13], v[12:13], v[68:69]\n"
"v_exp_f32 v71, v71\n"
"v_exp_f32 v243, v243\n"
"buffer_load_dwordx4 v6, s[48:51], s7 offen offset:3072 sc1 lds\n"
"v_pk_add_f32 v[10:11], v[10:11], v[240:241]\n"
"v_pk_fma_f32 v[74:75], v[246:247], s[98:99], v[8:9] op_sel:[0,0,1] op_sel_hi:[1,0,1] neg_lo:[1,0,0] neg_hi:[1,0,0]\n"
"v_pk_fma_f32 v[246:247], v[246:247], s[98:99], v[8:9] op_sel_hi:[1,0,0] neg_lo:[0,0,1] neg_hi:[0,0,1]\n"
"v_exp_f32 v72, v72\n"
"v_exp_f32 v244, v244\n"
"v_pk_add_f32 v[12:13], v[12:13], v[70:71]\n"
"v_exp_f32 v73, v73\n"
"v_exp_f32 v245, v245\n"
"v_pk_add_f32 v[10:11], v[10:11], v[242:243]\n"
"v_exp_f32 v74, v74\n"
"v_exp_f32 v246, v246\n"
"s_add_u32 s7, s7, 1024\n"
"s_add_u32 m0, s99, 0x2000\n"
"s_nop 0\n"
"buffer_load_dwordx4 v6, s[48:51], s7 offen offset:3072 sc1 lds\n"
"v_pk_add_f32 v[12:13], v[12:13], v[72:73]\n"
"v_exp_f32 v75, v75\n"
"v_exp_f32 v247, v247\n"
"v_pk_add_f32 v[10:11], v[10:11], v[244:245]\n"
"v_pk_add_f32 v[12:13], v[12:13], v[74:75]\n"
"v_pk_add_f32 v[10:11], v[10:11], v[246:247]\n"
"v_add_f32 v12, v12, v13\n"
"v_add_f32 v10, v10, v11\n"
"v_rcp_f32 v12, v12\n"
"v_rcp_f32 v10, v10\n"
"s_nop 0\n"
"v_pk_mul_f32 v[228:229], v[228:229], v[10:11] op_sel_hi:[1,0]\n"
"s_add_u32 s7, s4, 20480\n"
"s_add_u32 m0, s99, 0x3000\n"
"s_nop 0\n"
"buffer_load_dwordx4 v6, s[48:51], s7 offen sc1 lds\n"
"v_pk_mul_f32 v[230:231], v[230:231], v[10:11] op_sel_hi:[1,0]\n"
"v_cvt_pk_bf16_f32 v228, v228, v229\n"
"v_pk_mul_f32 v[232:233], v[232:233], v[10:11] op_sel_hi:[1,0]\n"
"v_cvt_pk_bf16_f32 v229, v230, v231\n"
"v_pk_mul_f32 v[234:235], v[234:235], v[10:11] op_sel_hi:[1,0]\n"
"v_cvt_pk_bf16_f32 v230, v232, v233\n"
"v_pk_mul_f32 v[236:237], v[236:237], v[10:11] op_sel_hi:[1,0]\n"
"v_cvt_pk_bf16_f32 v231, v234, v235\n"
"v_pk_mul_f32 v[238:239], v[238:239], v[10:11] op_sel_hi:[1,0]\n"
"v_cvt_pk_bf16_f32 v232, v236, v237\n"
"v_pk_mul_f32 v[240:241], v[240:241], v[10:11] op_sel_hi:[1,0]\n"
"v_cvt_pk_bf16_f32 v233, v238, v239\n"
"v_pk_mul_f32 v[242:243], v[242:243], v[10:11] op_sel_hi:[1,0]\n"
"v_cvt_pk_bf16_f32 v234, v240, v241\n"
"v_pk_mul_f32 v[244:245], v[244:245], v[10:11] op_sel_hi:[1,0]\n"
"v_cvt_pk_bf16_f32 v235, v242, v243\n"
"v_pk_mul_f32 v[246:247], v[246:247], v[10:11] op_sel_hi:[1,0]\n"
"v_cvt_pk_bf16_f32 v236, v244, v245\n"
"v_cvt_pk_bf16_f32 v237, v246, v247\n"
"v_pk_mul_f32 v[56:57], v[56:57], v[12:13] op_sel_hi:[1,0]\n"
"v_pk_mul_f32 v[58:59], v[58:59], v[12:13] op_sel_hi:[1,0]\n"
"v_cvt_pk_bf16_f32 v238, v56, v57\n"
"v_pk_mul_f32 v[60:61], v[60:61], v[12:13] op_sel_hi:[1,0]\n"
"v_cvt_pk_bf16_f32 v239, v58, v59\n"
"v_pk_mul_f32 v[62:63], v[62:63], v[12:13] op_sel_hi:[1,0]\n"
"v_cvt_pk_bf16_f32 v240, v60, v61\n"
"v_pk_mul_f32 v[64:65], v[64:65], v[12:13] op_sel_hi:[1,0]\n"
"v_cvt_pk_bf16_f32 v241, v62, v63\n"
"v_pk_mul_f32 v[66:67], v[66:67], v[12:13] op_sel_hi:[1,0]\n"
"v_cvt_pk_bf16_f32 v242, v64, v65\n"
"v_pk_mul_f32 v[68:69], v[68:69], v[12:13] op_sel_hi:[1,0]\n"
"v_cvt_pk_bf16_f32 v243, v66, v67\n"
"v_pk_mul_f32 v[70:71], v[70:71], v[12:13] op_sel_hi:[1,0]\n"
"v_cvt_pk_bf16_f32 v244, v68, v69\n"
"v_pk_mul_f32 v[72:73], v[72:73], v[12:13] op_sel_hi:[1,0]\n"
"v_cvt_pk_bf16_f32 v245, v70, v71\n"
"v_pk_mul_f32 v[74:75], v[74:75], v[12:13] op_sel_hi:[1,0]\n"
"v_cvt_pk_bf16_f32 v246, v72, v73\n"
"v_cvt_pk_bf16_f32 v247, v74, v75\n"
"buffer_load_dwordx2 v[8:9], v43, s[48:51], s2 offen sc1\n"
"v_and_b32 v12, 15, v1\n"
"v_and_b32 v13, 1, v1\n"
"v_lshlrev_b32 v13, 4, v13\n"
"v_mov_b32 v44, 0x3f80\n"
"v_lshlrev_b32 v13, v13, v44\n"
"v_lshrrev_b32 v12, 1, v12\n"
"v_cmp_eq_u32 vcc, 0, v12\n"
"s_nop 1\n"
"v_cndmask_b32 v76, 0, v13, vcc\n"
"v_cmp_eq_u32 vcc, 1, v12\n"
"s_nop 1\n"
"v_cndmask_b32 v77, 0, v13, vcc\n"
"v_cmp_eq_u32 vcc, 2, v12\n"
"s_nop 1\n"
"v_cndmask_b32 v78, 0, v13, vcc\n"
"v_cmp_eq_u32 vcc, 3, v12\n"
"s_nop 1\n"
"v_cndmask_b32 v79, 0, v13, vcc\n"
"v_mov_b32 v44, 0\n"
"v_mov_b32 v45, 0\n"
"v_mov_b32 v46, 0\n"
"v_mov_b32 v47, 0\n"
"v_add_u32 v12, s80, v6\n"
"ds_write_b128 v12, v[44:47] offset:0\n"
"ds_write_b128 v12, v[44:47] offset:1024\n"
"v_and_b32 v12, 15, v1\n"
"v_bfe_u32 v13, v1, 4, 1\n"
"v_lshlrev_b32 v13, 3, v13\n"
"v_lshl_add_u32 v12, v12, 4, v13\n"
"s_mul_i32 s83, s76, 1680\n"
"s_add_u32 s83, s83, 0x26000\n"
"v_add_u32 v40, s83, v12\n"
"v_add_u32 v12, 0x2000, v12\n"
"v_cmp_gt_u32 vcc, 32, v1\n"
"s_nop 1\n"
"v_cndmask_b32 v40, v12, v40, vcc\n"
"v_and_b32 v13, 47, v1\n"
"v_cmp_gt_u32 vcc, 9, v13\n"
"s_nop 1\n"
"v_cndmask_b32 v41, v12, v40, vcc\n"
"v_bfe_u32 v12, v1, 3, 2\n"
"v_and_b32 v13, 3, v1\n"
"v_lshl_add_u32 v12, v12, 2, v13\n"
"v_lshlrev_b32 v12, 4, v12\n"
"v_lshrrev_b32 v13, 5, v1\n"
"v_mul_u32_u24 v39, 1680, v13\n"
"v_add_u32 v39, v39, v12\n"
"v_add_u32 v39, 0x26000, v39\n"
"v_bfe_u32 v38, v1, 2, 1\n"
"v_cmp_eq_u32 vcc, v38, v13\n"
"v_add_u32 v12, 0, v12\n"
"s_nop 0\n"
"v_cndmask_b32 v39, v12, v39, vcc\n"
"s_add_u32 s82, s76, 0\n"
"s_and_b32 s82, s82, 3\n"
"s_mul_i32 s82, s82, 0x12800\n"
"v_add_u32 v16, s82, v6\n"
"s_add_u32 s82, s76, 1\n"
"s_and_b32 s82, s82, 3\n"
"s_mul_i32 s82, s82, 0x12800\n"
"v_add_u32 v17, s82, v6\n"
"s_add_u32 s82, s76, 2\n"
"s_and_b32 s82, s82, 3\n"
"s_mul_i32 s82, s82, 0x12800\n"
"v_add_u32 v18, s82, v6\n"
"s_add_u32 s82, s76, 3\n"
"s_and_b32 s82, s82, 3\n"
"s_mul_i32 s82, s82, 0x12800\n"
"v_add_u32 v19, s82, v6\n"
"s_mul_i32 s82, s88, 303104\n"
"s_add_u32 s82, s82, 0x400000\n"
"s_mul_i32 s83, s76, 12288\n"
"s_add_u32 s56, s82, s83\n"
"s_bitcmp1_b32 s0, 1\n"
"s_cselect_b32 s83, 6144, 0\n"
"s_add_u32 s62, s56, 6144\n"
"s_sub_u32 s62, s62, s83\n"
"s_add_u32 s56, s56, s83\n"
"s_lshl_b32 s83, s76, 11\n"
"s_add_u32 s83, s83, s82\n"
"s_add_u32 s57, s83, 49152\n"
"s_mul_i32 s83, s76, 5120\n"
"s_add_u32 s83, s83, s82\n"
"s_add_u32 s58, s83, 55296\n"
"s_bitcmp1_b32 s0, 0\n"
"s_cselect_b32 s83, 1024, 0\n"
"s_lshl_b32 s63, s83, 1\n"
"s_sub_u32 s63, 1024, s63\n"
"s_add_u32 s56, s56, s83\n"
"s_add_u32 s62, s62, s83\n"
"s_add_u32 s57, s57, s83\n"
"s_add_u32 s66, s58, 5120\n"
"s_lshl_b32 s83, s0, 10\n"
"s_add_u32 s58, s58, s83\n"
"s_cmp_eq_u32 s76, 3\n"
"s_cselect_b32 s57, 0x40000000, s57\n"
"v_add_u32 v27, s96, v6\n"
"v_add_u32 v28, s99, v6\n"
"v_add_u32 v29, s13, v6\n"
"v_lshlrev_b32 v32, 3, v1\n"
"s_lshr_b32 s82, s88, 1\n"
"s_lshl_b32 s82, s82, 9\n"
"s_add_u32 s60, s82, 1024\n"
"s_lshl_b32 s97, s76, 3\n"
"s_waitcnt vmcnt(0)\n"
"v_cmp_eq_u64 vcc, s[84:85], v[8:9]\n"
"s_and_b64 s[10:11], vcc, s[94:95]\n"
"s_cmp_eq_u64 s[10:11], s[94:95]\n"
"s_cbranch_scc0 L_att_p_slow\n"
"L_att_p_ok:\n"
"v_lshrrev_b32 v44, 4, v1\n"
"v_mul_u32_u24 v44, 1792, v44\n"
"v_and_b32 v45, 15, v1\n"
"v_lshl_add_u32 v44, v45, 4, v44\n"
"v_cmp_gt_u32 vcc, 9, v45\n"
"v_mov_b32 v45, 0x40000000\n"
"s_nop 0\n"
"v_cndmask_b32 v45, v45, v44, vcc\n"
"s_lshl_b32 s14, s78, 2\n"
"s_add_u32 s14, s14, s76\n"
"s_mul_i32 s14, s14, 28672\n"
"s_add_u32 s14, s14, 81920\n"
"buffer_load_dwordx4 a[0:3], v44, s[48:51], s14 offen offset:0 sc1\n"
"buffer_load_dwordx4 a[4:7], v44, s[48:51], s14 offen offset:256 sc1\n"
"buffer_load_dwordx4 a[8:11], v44, s[48:51], s14 offen offset:512 sc1\n"
"buffer_load_dwordx4 a[12:15], v44, s[48:51], s14 offen offset:768 sc1\n"
"buffer_load_dwordx4 a[16:19], v44, s[48:51], s14 offen offset:1024 sc1\n"
"buffer_load_dwordx4 a[20:23], v44, s[48:51], s14 offen offset:1280 sc1\n"
"buffer_load_dwordx4 a[24:27], v45, s[48:51], s14 offen offset:1536 sc1\n"
"s_add_u32 s14, s14, 7168\n"
"buffer_load_dwordx4 a[28:31], v44, s[48:51], s14 offen offset:0 sc1\n"
"buffer_load_dwordx4 a[32:35], v44, s[48:51], s14 offen offset:256 sc1\n"
"buffer_load_dwordx4 a[36:39], v44, s[48:51], s14 offen offset:512 sc1\n"
"buffer_load_dwordx4 a[40:43], v44, s[48:51], s14 offen offset:768 sc1\n"
"buffer_load_dwordx4 a[44:47], v44, s[48:51], s14 offen offset:1024 sc1\n"
"buffer_load_dwordx4 a[48:51], v44, s[48:51], s14 offen offset:1280 sc1\n"
"buffer_load_dwordx4 a[52:55], v45, s[48:51], s14 offen offset:1536 sc1\n"
"s_add_u32 s14, s14, 7168\n"
"buffer_load_dwordx4 a[56:59], v44, s[48:51], s14 offen offset:0 sc1\n"
"buffer_load_dwordx4 a[60:63], v44, s[48:51], s14 offen offset:256 sc1\n"
"buffer_load_dwordx4 a[64:67], v44, s[48:51], s14 offen offset:512 sc1\n"
"buffer_load_dwordx4 a[68:71], v44, s[48:51], s14 offen offset:768 sc1\n"
"buffer_load_dwordx4 a[72:75], v44, s[48:51], s14 offen offset:1024 sc1\n"
"buffer_load_dwordx4 a[76:79], v44, s[48:51], s14 offen offset:1280 sc1\n"
"buffer_load_dwordx4 a[80:83], v45, s[48:51], s14 offen offset:1536 sc1\n"
"s_add_u32 s14, s14, 7168\n"
"buffer_load_dwordx4 a[84:87], v44, s[48:51], s14 offen offset:0 sc1\n"
"buffer_load_dwordx4 a[88:91], v44, s[48:51], s14 offen offset:256 sc1\n"
"buffer_load_dwordx4 a[92:95], v44, s[48:51], s14 offen offset:512 sc1\n"
"buffer_load_dwordx4 v[128:131], v44, s[48:51], s14 offen offset:768 sc1\n"
"buffer_load_dwordx4 v[132:135], v44, s[48:51], s14 offen offset:1024 sc1\n"
"buffer_load_dwordx4 v[136:139], v44, s[48:51], s14 offen offset:1280 sc1\n"
"buffer_load_dwordx4 v[140:143], v45, s[48:51], s14 offen offset:1536 sc1\n"
"buffer_load_dwordx2 v[8:9], v32, s[48:51], s60 offen sc1\n"
"s_add_u32 m0, s13, 0xfffffc00\n"
"s_nop 0\n"
"buffer_load_dwordx4 v6, s[48:51], s7 offen offset:1024 sc1 lds\n"
"buffer_load_dwordx4 v6, s[48:51], s7 offen offset:2048 sc1 lds\n"
"buffer_load_dwordx4 v6, s[48:51], s7 offen offset:3072 sc1 lds\n"
"s_add_u32 s7, s7, 1024\n"
"s_add_u32 m0, s13, 0x0\n"
"s_nop 0\n"
"buffer_load_dwordx4 v6, s[48:51], s7 offen offset:3072 sc1 lds\n"
"s_add_u32 s7, s4, 20480\n"
"s_add_u32 s7, s7, s1\n"
"s_add_u32 m0, s13, 0x1000\n"
"s_nop 0\n"
"buffer_load_dwordx4 v6, s[48:51], s7 offen sc1 lds\n"
"buffer_load_dwordx4 v6, s[48:51], s7 offen offset:1024 sc1 lds\n"
"s_waitcnt vmcnt(34)\n"
"v_mfma_f32_16x16x32_bf16 v[48:51], v[76:79], a[0:3], 0\n"
"s_waitcnt vmcnt(33)\n"
"v_mfma_f32_16x16x32_bf16 v[52:55], v[76:79], a[4:7], 0\n"
"s_waitcnt vmcnt(32)\n"
"v_mfma_f32_16x16x32_bf16 v[56:59], v[76:79], a[8:11], 0\n"
"s_waitcnt vmcnt(31)\n"
"v_mfma_f32_16x16x32_bf16 v[60:63], v[76:79], a[12:15], 0\n"
"s_waitcnt vmcnt(30)\n"
"v_mfma_f32_16x16x32_bf16 v[64:67], v[76:79], a[16:19], 0\n"
"s_waitcnt vmcnt(29)\n"
"v_mfma_f32_16x16x32_bf16 v[68:71], v[76:79], a[20:23], 0\n"
"s_waitcnt vmcnt(28)\n"
"v_mfma_f32_16x16x32_bf16 v[72:75], v[76:79], a[24:27], 0\n"
"s_waitcnt vmcnt(27)\n"
"v_mfma_f32_16x16x32_bf16 v[48:51], v[76:79], a[28:31], v[48:51]\n"
"s_waitcnt vmcnt(26)\n"
"v_mfma_f32_16x16x32_bf16 v[52:55], v[76:79], a[32:35], v[52:55]\n"
"s_waitcnt vmcnt(25)\n"
"v_mfma_f32_16x16x32_bf16 v[56:59], v[76:79], a[36:39], v[56:59]\n"
"s_waitcnt vmcnt(24)\n"
"v_mfma_f32_16x16x32_bf16 v[60:63], v[76:79], a[40:43], v[60:63]\n"
"s_waitcnt vmcnt(23)\n"
"v_mfma_f32_16x16x32_bf16 v[64:67], v[76:79], a[44:47], v[64:67]\n"
"s_waitcnt vmcnt(22)\n"
"v_mfma_f32_16x16x32_bf16 v[68:71], v[76:79], a[48:51], v[68:71]\n"
"s_waitcnt vmcnt(21)\n"
"v_mfma_f32_16x16x32_bf16 v[72:75], v[76:79], a[52:55], v[72:75]\n"
"s_waitcnt vmcnt(20)\n"
"v_mfma_f32_16x16x32_bf16 v[48:51], v[76:79], a[56:59], v[48:51]\n"
"s_waitcnt vmcnt(19)\n"
"v_mfma_f32_16x16x32_bf16 v[52:55], v[76:79], a[60:63], v[52:55]\n"
"s_waitcnt vmcnt(18)\n"
"v_mfma_f32_16x16x32_bf16 v[56:59], v[76:79], a[64:67], v[56:59]\n"
"s_waitcnt vmcnt(17)\n"
"v_mfma_f32_16x16x32_bf16 v[60:63], v[76:79], a[68:71], v[60:63]\n"
"s_waitcnt vmcnt(16)\n"
"v_mfma_f32_16x16x32_bf16 v[64:67], v[76:79], a[72:75], v[64:67]\n"
"s_waitcnt vmcnt(15)\n"
"v_mfma_f32_16x16x32_bf16 v[68:71], v[76:79], a[76:79], v[68:71]\n"
"s_waitcnt vmcnt(14)\n"
"v_mfma_f32_16x16x32_bf16 v[72:75], v[76:79], a[80:83], v[72:75]\n"
"s_waitcnt vmcnt(13)\n"
"v_mfma_f32_16x16x32_bf16 v[48:51], v[76:79], a[84:87], v[48:51]\n"
"s_waitcnt vmcnt(12)\n"
"v_mfma_f32_16x16x32_bf16 v[52:55], v[76:79], a[88:91], v[52:55]\n"
"s_waitcnt vmcnt(11)\n"
"v_mfma_f32_16x16x32_bf16 v[56:59], v[76:79], a[92:95], v[56:59]\n"
"s_waitcnt vmcnt(10)\n"
"v_mfma_f32_16x16x32_bf16 v[60:63], v[76:79], v[128:131], v[60:63]\n"
"s_waitcnt vmcnt(9)\n"
"v_mfma_f32_16x16x32_bf16 v[64:67], v[76:79], v[132:135], v[64:67]\n"
"s_waitcnt vmcnt(8)\n"
"v_mfma_f32_16x16x32_bf16 v[68:71], v[76:79], v[136:139], v[68:71]\n"
"s_waitcnt vmcnt(7)\n"
"v_mfma_f32_16x16x32_bf16 v[72:75], v[76:79], v[140:143], v[72:75]\n"
"v_cvt_pk_bf16_f32 v48, v48, v49\n"
"v_cvt_pk_bf16_f32 v49, v50, v51\n"
"ds_write_b64 v40, v[48:49] offset:0\n"
"v_cvt_pk_bf16_f32 v52, v52, v53\n"
"v_cvt_pk_bf16_f32 v53, v54, v55\n"
"ds_write_b64 v40, v[52:53] offset:256\n"
"v_cvt_pk_bf16_f32 v56, v56, v57\n"
"v_cvt_pk_bf16_f32 v57, v58, v59\n"
"ds_write_b64 v40, v[56:57] offset:512\n"
"v_cvt_pk_bf16_f32 v60, v60, v61\n"
"v_cvt_pk_bf16_f32 v61, v62, v63\n"
"ds_write_b64 v40, v[60:61] offset:768\n"
"v_cvt_pk_bf16_f32 v64, v64, v65\n"
"v_cvt_pk_bf16_f32 v65, v66, v67\n"
"ds_write_b64 v40, v[64:65] offset:1024\n"
"v_cvt_pk_bf16_f32 v68, v68, v69\n"
"v_cvt_pk_bf16_f32 v69, v70, v71\n"
"ds_write_b64 v40, v[68:69] offset:1280\n"
"v_cvt_pk_bf16_f32 v72, v72, v73\n"
"v_cvt_pk_bf16_f32 v73, v74, v75\n"
"ds_write_b64 v41, v[72:73] offset:1536\n"
"s_lshl_b32 s94, 0x11, s97\n"
"s_lshl_b32 s95, 0x4, s97\n"
"s_waitcnt vmcnt(6)\n"
"v_cmp_eq_u64 vcc, s[84:85], v[8:9]\n"
"s_and_b64 s[10:11], vcc, s[94:95]\n"
"s_cmp_eq_u64 s[10:11], s[94:95]\n"
"s_cbranch_scc0 L_att_e_slow\n"
"L_att_e_ok:\n"
"s_waitcnt lgkmcnt(0)\n"
"s_barrier\n"
"ds_read_b128 a[0:3], v39 offset:0\n"
"ds_read_b128 a[4:7], v39 offset:256\n"
"ds_read_b128 a[8:11], v39 offset:336\n"
"ds_read_b128 a[12:15], v39 offset:592\n"
"ds_read_b128 a[16:19], v39 offset:672\n"
"ds_read_b128 a[20:23], v39 offset:928\n"
"s_waitcnt lgkmcnt(5)\n"
"v_mfma_f32_32x32x16_bf16 v[128:143], a[0:3], v[208:211], 0\n"
"ds_read_b128 a[24:27], v39 offset:1008\n"
"s_waitcnt lgkmcnt(5)\n"
"v_mfma_f32_32x32x16_bf16 v[144:159], a[4:7], v[208:211], 0\n"
"ds_read_b128 a[28:31], v39 offset:1264\n"
"s_waitcnt lgkmcnt(5)\n"
"v_mfma_f32_32x32x16_bf16 v[128:143], a[8:11], v[212:215], v[128:143]\n"
"ds_read_b128 a[32:35], v39 offset:1344\n"
"s_add_u32 s59, s56, 0\n"
"buffer_load_dwordx4 a[96:99], v16, s[48:51], s59 offen sc1\n"
"s_waitcnt lgkmcnt(5)\n"
"v_mfma_f32_32x32x16_bf16 v[144:159], a[12:15], v[212:215], v[144:159]\n"
"ds_read_b128 a[36:39], v39 offset:1600\n"
"s_waitcnt lgkmcnt(5)\n"
"v_mfma_f32_32x32x16_bf16 v[128:143], a[16:19], v[216:219], v[128:143]\n"
"ds_read_b128 a[40:43], v39 offset:3360\n"
"buffer_load_dwordx4 a[100:103], v17, s[48:51], s59 offen sc1\n"
"s_waitcnt lgkmcnt(5)\n"
"v_mfma_f32_32x32x16_bf16 v[144:159], a[20:23], v[216:219], v[144:159]\n"
"ds_read_b128 a[44:47], v39 offset:3616\n"
"s_waitcnt lgkmcnt(5)\n"
"v_mfma_f32_32x32x16_bf16 v[128:143], a[24:27], v[220:223], v[128:143]\n"
"ds_read_b128 a[48:51], v39 offset:3696\n"
"s_waitcnt lgkmcnt(5)\n"
"v_mfma_f32_32x32x16_bf16 v[144:159], a[28:31], v[220:223], v[144:159]\n"
"ds_read_b128 a[52:55], v39 offset:3952\n"
"buffer_load_dwordx4 a[104:107], v18, s[48:51], s59 offen sc1\n"
"s_waitcnt lgkmcnt(5)\n"
"v_mfma_f32_32x32x16_bf16 v[128:143], a[32:35], v[224:227], v[128:143]\n"
"ds_read_b128 a[56:59], v39 offset:4032\n"
"s_waitcnt lgkmcnt(5)\n"
"v_mfma_f32_32x32x16_bf16 v[144:159], a[36:39], v[224:227], v[144:159]\n"
"ds_read_b128 a[60:63], v39 offset:4288\n"
"buffer_load_dwordx4 a[108:111], v19, s[48:51], s59 offen sc1\n"
"s_waitcnt lgkmcnt(5)\n"
"v_mfma_f32_32x32x16_bf16 v[160:175], a[40:43], v[228:231], 0\n"
"ds_read_b128 a[64:67], v39 offset:4368\n"
"s_waitcnt lgkmcnt(5)\n"
"v_mfma_f32_32x32x16_bf16 v[176:191], a[44:47], v[228:231], 0\n"
"ds_read_b128 a[68:71], v39 offset:4624\n"
"s_waitcnt lgkmcnt(5)\n"
"v_mfma_f32_32x32x16_bf16 v[160:175], a[48:51], v[232:235], v[160:175]\n"
"ds_read_b128 a[72:75], v39 offset:4704\n"
"s_add_u32 m0, s13, 0x1000\n"
"s_nop 0\n"
"buffer_load_dwordx4 v6, s[48:51], s7 offen offset:2048 sc1 lds\n"
"v_add_f32 v44, 0x358637bd, v148\n"
"v_rcp_f32 v44, v44\n"
"s_nop 0\n"
"v_mul_f32 v44, v44, v42\n"
"v_mul_f32 v128, v44, v128\n"
"v_mul_f32 v129, v44, v129\n"
"s_waitcnt lgkmcnt(5)\n"
"v_mfma_f32_32x32x16_bf16 v[176:191], a[52:55], v[232:235], v[176:191]\n"
"ds_read_b128 a[76:79], v39 offset:4960\n"
"v_cvt_pk_bf16_f32 v48, v128, v129\n"
"v_mul_f32 v130, v44, v130\n"
"v_mul_f32 v131, v44, v131\n"
"v_cvt_pk_bf16_f32 v49, v130, v131\n"
"v_mul_f32 v132, v44, v132\n"
"s_waitcnt lgkmcnt(5)\n"
"v_mfma_f32_32x32x16_bf16 v[160:175], a[56:59], v[236:239], v[160:175]\n"
"buffer_load_dwordx4 v6, s[48:51], s7 offen offset:3072 sc1 lds\n"
"v_mul_f32 v133, v44, v133\n"
"v_cvt_pk_bf16_f32 v50, v132, v133\n"
"v_mul_f32 v134, v44, v134\n"
"v_mul_f32 v135, v44, v135\n"
"v_cvt_pk_bf16_f32 v51, v134, v135\n"
"s_waitcnt lgkmcnt(4)\n"
"v_mfma_f32_32x32x16_bf16 v[176:191], a[60:63], v[236:239], v[176:191]\n"
"buffer_store_dwordx4 v[48:51], v5, s[48:51], s86 offen offset:0 sc1\n"
"v_mul_f32 v136, v44, v136\n"
"v_mul_f32 v137, v44, v137\n"
"v_cvt_pk_bf16_f32 v52, v136, v137\n"
"v_mul_f32 v138, v44, v138\n"
"s_waitcnt lgkmcnt(3)\n"
"v_mfma_f32_32x32x16_bf16 v[160:175], a[64:67], v[240:243], v[160:175]\n"
"v_mul_f32 v139, v44, v139\n"
"v_cvt_pk_bf16_f32 v53, v138, v139\n"
"v_mul_f32 v140, v44, v140\n"
"v_mul_f32 v141, v44, v141\n"
"v_cvt_pk_bf16_f32 v54, v140, v141\n"
"s_waitcnt lgkmcnt(2)\n"
"v_mfma_f32_32x32x16_bf16 v[176:191], a[68:71], v[240:243], v[176:191]\n"
"s_add_u32 s7, s7, 1024\n"
"s_add_u32 m0, s13, 0x1400\n"
"s_nop 0\n"
"buffer_load_dwordx4 v6, s[48:51], s7 offen offset:3072 sc1 lds\n"
"v_mul_f32 v142, v44, v142\n"
"v_mul_f32 v143, v44, v143\n"
"v_cvt_pk_bf16_f32 v55, v142, v143\n"
"buffer_store_dwordx4 v[52:55], v5, s[48:51], s86 offen offset:512 sc1\n"
"v_mul_f32 v144, v44, v144\n"
"s_waitcnt lgkmcnt(1)\n"
"v_mfma_f32_32x32x16_bf16 v[160:175], a[72:75], v[244:247], v[160:175]\n"
"v_mul_f32 v145, v44, v145\n"
"v_cvt_pk_bf16_f32 v56, v144, v145\n"
"v_mul_f32 v146, v44, v146\n"
"v_mul_f32 v147, v44, v147\n"
"v_cvt_pk_bf16_f32 v57, v146, v147\n"
"s_waitcnt lgkmcnt(0)\n"
"v_mfma_f32_32x32x16_bf16 v[176:191], a[76:79], v[244:247], v[176:191]\n"
"s_add_u32 s7, s79, 20480\n"
"s_add_u32 m0, s13, 0x2400\n"
"s_nop 0\n"
"buffer_load_dwordx4 v6, s[48:51], s7 offen sc1 lds\n"
"s_nop 7\n"
"v_add_f32 v44, 0x358637bd, v180\n"
"v_rcp_f32 v44, v44\n"
"s_nop 0\n"
"v_mul_f32 v44, v44, v42\n"
"v_pk_mul_f32 v[160:161], v[160:161], v[44:45] op_sel_hi:[1,0]\n"
"v_cvt_pk_bf16_f32 v58, v160, v161\n"
"v_pk_mul_f32 v[162:163], v[162:163], v[44:45] op_sel_hi:[1,0]\n"
"v_cvt_pk_bf16_f32 v59, v162, v163\n"
"buffer_store_dwordx4 v[56:59], v5, s[48:51], s86 offen offset:1024 sc1\n"
"v_pk_mul_f32 v[164:165], v[164:165], v[44:45] op_sel_hi:[1,0]\n"
"v_cvt_pk_bf16_f32 v60, v164, v165\n"
"v_pk_mul_f32 v[166:167], v[166:167], v[44:45] op_sel_hi:[1,0]\n"
"v_cvt_pk_bf16_f32 v61, v166, v167\n"
"v_pk_mul_f32 v[168:169], v[168:169], v[44:45] op_sel_hi:[1,0]\n"
"v_cvt_pk_bf16_f32 v62, v168, v169\n"
"v_pk_mul_f32 v[170:171], v[170:171], v[44:45] op_sel_hi:[1,0]\n"
"v_cvt_pk_bf16_f32 v63, v170, v171\n"
"buffer_store_dwordx4 v[60:63], v5, s[48:51], s86 offen offset:1536 sc1\n"
"v_pk_mul_f32 v[172:173], v[172:173], v[44:45] op_sel_hi:[1,0]\n"
"v_cvt_pk_bf16_f32 v64, v172, v173\n"
"v_pk_mul_f32 v[174:175], v[174:175], v[44:45] op_sel_hi:[1,0]\n"
"v_cvt_pk_bf16_f32 v65, v174, v175\n"
"v_pk_mul_f32 v[176:177], v[176:177], v[44:45] op_sel_hi:[1,0]\n"
"v_cvt_pk_bf16_f32 v66, v176, v177\n"
"v_pk_mul_f32 v[178:179], v[178:179], v[44:45] op_sel_hi:[1,0]\n"
"v_cvt_pk_bf16_f32 v67, v178, v179\n"
"buffer_store_dwordx4 v[64:67], v5, s[48:51], s86 offen offset:2048 sc1\n"
"s_add_u32 s59, s56, 0\n"
"s_add_u32 s59, s59, s63\n"
"buffer_load_dwordx4 a[112:115], v16, s[48:51], s59 offen sc1\n"
"buffer_load_dwordx4 a[116:119], v17, s[48:51], s59 offen sc1\n"
"buffer_load_dwordx4 a[120:123], v18, s[48:51], s59 offen sc1\n"
"buffer_load_dwordx4 a[124:127], v19, s[48:51], s59 offen sc1\n"
"s_waitcnt vmcnt(16)\n"
"v_mfma_f32_16x16x32_bf16 v[128:131], a[204:207], a[96:99], 0\n"
"buffer_load_dwordx2 v[8:9], v32, s[48:51], s60 offen sc1\n"
"s_add_u32 s59, s62, 0\n"
"buffer_load_dwordx4 a[128:131], v16, s[48:51], s59 offen sc1\n"
"v_mfma_f32_16x16x32_bf16 v[144:147], a[208:211], a[96:99], 0\n"
"v_mfma_f32_16x16x32_bf16 v[160:163], a[212:215], a[96:99], 0\n"
"v_mfma_f32_16x16x32_bf16 v[176:179], a[216:219], a[96:99], 0\n"
"buffer_load_dwordx4 a[132:135], v17, s[48:51], s59 offen sc1\n"
"v_mfma_f32_16x16x32_bf16 v[224:227], a[220:223], a[96:99], 0\n"
"s_waitcnt vmcnt(18)\n"
"v_mfma_f32_16x16x32_bf16 v[132:135], a[204:207], a[100:103], 0\n"
"v_mfma_f32_16x16x32_bf16 v[148:151], a[208:211], a[100:103], 0\n"
"buffer_load_dwordx4 a[136:139], v18, s[48:51], s59 offen sc1\n"
"v_mfma_f32_16x16x32_bf16 v[164:167], a[212:215], a[100:103], 0\n"
"v_mfma_f32_16x16x32_bf16 v[180:183], a[216:219], a[100:103], 0\n"
"v_mfma_f32_16x16x32_bf16 v[228:231], a[220:223], a[100:103], 0\n"
"buffer_load_dwordx4 a[140:143], v19, s[48:51], s59 offen sc1\n"
"s_waitcnt vmcnt(19)\n"
"v_mfma_f32_16x16x32_bf16 v[136:139], a[204:207], a[104:107], 0\n"
"v_mfma_f32_16x16x32_bf16 v[152:155], a[208:211], a[104:107], 0\n"
"v_mfma_f32_16x16x32_bf16 v[168:171], a[212:215], a[104:107], 0\n"
"s_add_u32 s59, s62, 0\n"
"s_add_u32 s59, s59, s63\n"
"buffer_load_dwordx4 a[144:147], v16, s[48:51], s59 offen sc1\n"
"v_mfma_f32_16x16x32_bf16 v[184:187], a[216:219], a[104:107], 0\n"
"v_mfma_f32_16x16x32_bf16 v[232:235], a[220:223], a[104:107], 0\n"
"s_waitcnt vmcnt(19)\n"
"v_mfma_f32_16x16x32_bf16 v[140:143], a[204:207], a[108:111], 0\n"
"v_mfma_f32_16x16x32_bf16 v[156:159], a[208:211], a[108:111], 0\n"
"buffer_load_dwordx4 a[148:151], v17, s[48:51], s59 offen sc1\n"
"s_waitcnt vmcnt(11)\n"
"s_barrier\n"
"buffer_store_dwordx2 v[2:3], v4, s[48:51], s87 offen offset:8 sc1\n"
"v_mfma_f32_16x16x32_bf16 v[172:175], a[212:215], a[108:111], 0\n"
"v_mfma_f32_16x16x32_bf16 v[188:191], a[216:219], a[108:111], 0\n"
"v_mfma_f32_16x16x32_bf16 v[236:239], a[220:223], a[108:111], 0\n"
"buffer_load_dwordx4 a[152:155], v18, s[48:51], s59 offen sc1\n"
"s_waitcnt vmcnt(12)\n"
"v_mfma_f32_16x16x32_bf16 v[128:131], a[224:227], a[112:115], v[128:131]\n"
"v_mfma_f32_16x16x32_bf16 v[144:147], a[228:231], a[112:115], v[144:147]\n"
"v_mfma_f32_16x16x32_bf16 v[160:163], a[232:235], a[112:115], v[160:163]\n"
"buffer_load_dwordx4 a[156:159], v19, s[48:51], s59 offen sc1\n"
"v_mfma_f32_16x16x32_bf16 v[176:179], a[236:239], a[112:115], v[176:179]\n"
"v_mfma_f32_16x16x32_bf16 v[224:227], a[240:243], a[112:115], v[224:227]\n"
"s_waitcnt vmcnt(12)\n"
"v_mfma_f32_16x16x32_bf16 v[132:135], a[224:227], a[116:119], v[132:135]\n"
"buffer_load_dwordx4 v[116:119], v6, s[48:51], s7 offen offset:1024 sc1\n"
"v_mfma_f32_16x16x32_bf16 v[148:151], a[228:231], a[116:119], v[148:151]\n"
"v_mfma_f32_16x16x32_bf16 v[164:167], a[232:235], a[116:119], v[164:167]\n"
"v_mfma_f32_16x16x32_bf16 v[180:183], a[236:239], a[116:119], v[180:183]\n"
"v_mfma_f32_16x16x32_bf16 v[228:231], a[240:243], a[116:119], v[228:231]\n"
"buffer_load_dwordx4 v[120:123], v6, s[48:51], s7 offen offset:2048 sc1\n"
"s_waitcnt vmcnt(13)\n"
"v_mfma_f32_16x16x32_bf16 v[136:139], a[224:227], a[120:123], v[136:139]\n"
"s_lshl_b32 s94, 0xaa, s97\n"
"s_lshl_b32 s95, 0x0, s97\n"
"s_waitcnt vmcnt(11)\n"
"v_cmp_eq_u64 vcc, s[84:85], v[8:9]\n"
"s_and_b64 s[10:11], vcc, s[94:95]\n"
"s_cmp_eq_u64 s[10:11], s[94:95]\n"
"s_cbranch_scc0 L_att_f_slow\n"
"L_att_f_ok:\n"
"v_mfma_f32_16x16x32_bf16 v[152:155], a[228:231], a[120:123], v[152:155]\n"
"v_mfma_f32_16x16x32_bf16 v[168:171], a[232:235], a[120:123], v[168:171]\n"
"s_add_u32 s59, s56, 2048\n"
"buffer_load_dwordx4 v[80:83], v16, s[48:51], s59 offen sc1\n"
"v_mfma_f32_16x16x32_bf16 v[184:187], a[236:239], a[120:123], v[184:187]\n"
"v_mfma_f32_16x16x32_bf16 v[232:235], a[240:243], a[120:123], v[232:235]\n"
"v_mfma_f32_16x16x32_bf16 v[140:143], a[224:227], a[124:127], v[140:143]\n"
"buffer_load_dwordx4 v[84:87], v17, s[48:51], s59 offen sc1\n"
"v_mfma_f32_16x16x32_bf16 v[156:159], a[228:231], a[124:127], v[156:159]\n"
"v_mfma_f32_16x16x32_bf16 v[172:175], a[232:235], a[124:127], v[172:175]\n"
"v_mfma_f32_16x16x32_bf16 v[188:191], a[236:239], a[124:127], v[188:191]\n"
"buffer_load_dwordx4 v[88:91], v18, s[48:51], s59 offen sc1\n"
"v_mfma_f32_16x16x32_bf16 v[236:239], a[240:243], a[124:127], v[236:239]\n"
"s_waitcnt vmcnt(13)\n"
"v_mfma_f32_16x16x32_bf16 v[128:131], a[244:247], a[128:131], v[128:131]\n"
"s_lshl_b32 s82, s76, 14\n"
"v_mfma_f32_16x16x32_bf16 v[144:147], a[248:251], a[128:131], v[144:147]\n"
"s_lshl_b32 s83, s88, 8\n"
"v_mfma_f32_16x16x32_bf16 v[160:163], a[252:255], a[128:131], v[160:163]\n"
"buffer_load_dwordx4 v[92:95], v19, s[48:51], s59 offen sc1\n"
"v_mfma_f32_16x16x32_bf16 v[176:179], v[192:195], a[128:131], v[176:179]\n"
"s_add_u32 s82, s82, s83\n"
"v_mfma_f32_16x16x32_bf16 v[224:227], v[196:199], a[128:131], v[224:227]\n"
"s_add_u32 s82, s82, 16384\n"
"s_waitcnt vmcnt(13)\n"
"v_mfma_f32_16x16x32_bf16 v[132:135], a[244:247], a[132:135], v[132:135]\n"
"s_add_u32 s59, s56, 2048\n"
"s_add_u32 s59, s59, s63\n"
"buffer_load_dwordx4 v[96:99], v16, s[48:51], s59 offen sc1\n"
"v_and_b32 v12, 15, v1\n"
"v_mfma_f32_16x16x32_bf16 v[148:151], a[248:251], a[132:135], v[148:151]\n"
"v_lshlrev_b32 v12, 2, v12\n"
"v_mfma_f32_16x16x32_bf16 v[164:167], a[252:255], a[132:135], v[164:167]\n"
"s_add_u32 s83, s76, 0\n"
"v_mfma_f32_16x16x32_bf16 v[180:183], v[192:195], a[132:135], v[180:183]\n"
"buffer_load_dwordx4 v[100:103], v17, s[48:51], s59 offen sc1\n"
"v_mfma_f32_16x16x32_bf16 v[228:231], v[196:199], a[132:135], v[228:231]\n"
"s_and_b32 s83, s83, 3\n"
"s_waitcnt vmcnt(14)\n"
"v_mfma_f32_16x16x32_bf16 v[136:139], a[244:247], a[136:139], v[136:139]\n"
"s_lshl_b32 s83, s83, 6\n"
"v_mfma_f32_16x16x32_bf16 v[152:155], a[248:251], a[136:139], v[152:155]\n"
"buffer_load_dwordx4 v[104:107], v18, s[48:51], s59 offen sc1\n"
"v_mfma_f32_16x16x32_bf16 v[168:171], a[252:255], a[136:139], v[168:171]\n"
"s_add_u32 s83, s83, s82\n"
"v_mfma_f32_16x16x32_bf16 v[184:187], v[192:195], a[136:139], v[184:187]\n"
"buffer_load_dword v21, v12, s[48:51], s83 offen sc1\n"
"v_mfma_f32_16x16x32_bf16 v[232:235], v[196:199], a[136:139], v[232:235]\n"
"buffer_load_dwordx4 v[108:111], v19, s[48:51], s59 offen sc1\n"
"s_waitcnt vmcnt(16)\n"
"v_mfma_f32_16x16x32_bf16 v[140:143], a[244:247], a[140:143], v[140:143]\n"
"v_mfma_f32_16x16x32_bf16 v[156:159], a[248:251], a[140:143], v[156:159]\n"
"s_add_u32 s83, s76, 1\n"
"v_mfma_f32_16x16x32_bf16 v[172:175], a[252:255], a[140:143], v[172:175]\n"
"s_and_b32 s83, s83, 3\n"
"s_lshl_b32 s83, s83, 6\n"
"v_mfma_f32_16x16x32_bf16 v[188:191], v[192:195], a[140:143], v[188:191]\n"
"s_add_u32 s59, s62, 2048\n"
"buffer_load_dwordx4 a[96:99], v16, s[48:51], s59 offen sc1\n"
"v_mfma_f32_16x16x32_bf16 v[236:239], v[196:199], a[140:143], v[236:239]\n"
"s_add_u32 s83, s83, s82\n"
"s_waitcnt vmcnt(16)\n"
"v_mfma_f32_16x16x32_bf16 v[128:131], v[200:203], a[144:147], v[128:131]\n"
"v_mfma_f32_16x16x32_bf16 v[144:147], v[204:207], a[144:147], v[144:147]\n"
"buffer_load_dwordx4 a[100:103], v17, s[48:51], s59 offen sc1\n"
"v_mfma_f32_16x16x32_bf16 v[160:163], a[160:163], a[144:147], v[160:163]\n"
"buffer_load_dword v22, v12, s[48:51], s83 offen sc1\n"
"v_mfma_f32_16x16x32_bf16 v[176:179], a[164:167], a[144:147], v[176:179]\n"
"s_add_u32 s83, s76, 2\n"
"v_mfma_f32_16x16x32_bf16 v[224:227], a[168:171], a[144:147], v[224:227]\n"
"buffer_load_dwordx4 a[104:107], v18, s[48:51], s59 offen sc1\n"
"s_waitcnt vmcnt(18)\n"
"v_mfma_f32_16x16x32_bf16 v[132:135], v[200:203], a[148:151], v[132:135]\n"
"s_and_b32 s83, s83, 3\n"
"v_mfma_f32_16x16x32_bf16 v[148:151], v[204:207], a[148:151], v[148:151]\n"
"s_lshl_b32 s83, s83, 6\n"
"v_mfma_f32_16x16x32_bf16 v[164:167], a[160:163], a[148:151], v[164:167]\n"
"buffer_load_dwordx4 a[108:111], v19, s[48:51], s59 offen sc1\n"
"v_mfma_f32_16x16x32_bf16 v[180:183], a[164:167], a[148:151], v[180:183]\n"
"s_add_u32 s83, s83, s82\n"
"v_mfma_f32_16x16x32_bf16 v[228:231], a[168:171], a[148:151], v[228:231]\n"
"buffer_load_dword v23, v12, s[48:51], s83 offen sc1\n"
"s_waitcnt vmcnt(18)\n"
"v_mfma_f32_16x16x32_bf16 v[136:139], v[200:203], a[152:155], v[136:139]\n"
"s_add_u32 s83, s76, 3\n"
"v_mfma_f32_16x16x32_bf16 v[152:155], v[204:207], a[152:155], v[152:155]\n"
"s_add_u32 s59, s62, 2048\n"
"s_add_u32 s59, s59, s63\n"
"buffer_load_dwordx4 a[112:115], v16, s[48:51], s59 offen sc1\n"
"v_mfma_f32_16x16x32_bf16 v[168:171], a[160:163], a[152:155], v[168:171]\n"
"s_and_b32 s83, s83, 3\n"
"v_mfma_f32_16x16x32_bf16 v[184:187], a[164:167], a[152:155], v[184:187]\n"
"s_lshl_b32 s83, s83, 6\n"
"v_mfma_f32_16x16x32_bf16 v[232:235], a[168:171], a[152:155], v[232:235]\n"
"buffer_load_dwordx4 a[116:119], v17, s[48:51], s59 offen sc1\n"
"s_waitcnt vmcnt(19)\n"
"v_mfma_f32_16x16x32_bf16 v[140:143], v[200:203], a[156:159], v[140:143]\n"
"s_add_u32 s83, s83, s82\n"
"v_mfma_f32_16x16x32_bf16 v[156:159], v[204:207], a[156:159], v[156:159]\n"
"buffer_load_dword v24, v12, s[48:51], s83 offen sc1\n"
"v_mfma_f32_16x16x32_bf16 v[172:175], a[160:163], a[156:159], v[172:175]\n"
"buffer_load_dwordx4 a[120:123], v18, s[48:51], s59 offen sc1\n"
"v_mfma_f32_16x16x32_bf16 v[188:191], a[164:167], a[156:159], v[188:191]\n"
"ds_read_b128 a[0:3], v27 offset:0\n"
"v_mfma_f32_16x16x32_bf16 v[236:239], a[168:171], a[156:159], v[236:239]\n"
"ds_read_b128 a[4:7], v27 offset:1024\n"
"s_waitcnt vmcnt(18)\n"
"v_mfma_f32_16x16x32_bf16 v[128:131], a[172:175], v[80:83], v[128:131]\n"
"buffer_load_dwordx4 a[124:127], v19, s[48:51], s59 offen sc1\n"
"v_mfma_f32_16x16x32_bf16 v[144:147], a[176:179], v[80:83], v[144:147]\n"
"v_mfma_f32_16x16x32_bf16 v[160:163], a[180:183], v[80:83], v[160:163]\n"
"v_mfma_f32_16x16x32_bf16 v[176:179], a[184:187], v[80:83], v[176:179]\n"
"v_mfma_f32_16x16x32_bf16 v[224:227], a[188:191], v[80:83], v[224:227]\n"
"s_add_u32 s59, s57, 0\n"
"buffer_load_dwordx4 a[128:131], v16, s[48:51], s59 offen sc1\n"
"s_waitcnt vmcnt(19)\n"
"v_mfma_f32_16x16x32_bf16 v[132:135], a[172:175], v[84:87], v[132:135]\n"
"v_mfma_f32_16x16x32_bf16 v[148:151], a[176:179], v[84:87], v[148:151]\n"
"v_mfma_f32_16x16x32_bf16 v[164:167], a[180:183], v[84:87], v[164:167]\n"
"buffer_load_dwordx4 a[132:135], v17, s[48:51], s59 offen sc1\n"
"v_mfma_f32_16x16x32_bf16 v[180:183], a[184:187], v[84:87], v[180:183]\n"
"v_mfma_f32_16x16x32_bf16 v[228:231], a[188:191], v[84:87], v[228:231]\n"
"s_waitcnt vmcnt(19)\n"
"v_mfma_f32_16x16x32_bf16 v[136:139], a[172:175], v[88:91], v[136:139]\n"
"buffer_load_dwordx4 a[136:139], v18, s[48:51], s59 offen sc1\n"
"v_mfma_f32_16x16x32_bf16 v[152:155], a[176:179], v[88:91], v[152:155]\n"
"v_mfma_f32_16x16x32_bf16 v[168:171], a[180:183], v[88:91], v[168:171]\n"
"v_mfma_f32_16x16x32_bf16 v[184:187], a[184:187], v[88:91], v[184:187]\n"
"buffer_load_dwordx4 a[140:143], v19, s[48:51], s59 offen sc1\n"
"v_mfma_f32_16x16x32_bf16 v[232:235], a[188:191], v[88:91], v[232:235]\n"
"s_waitcnt vmcnt(20)\n"
"v_mfma_f32_16x16x32_bf16 v[140:143], a[172:175], v[92:95], v[140:143]\n"
"buffer_load_dwordx2 v[8:9], v32, s[48:51], s60 offen sc1\n"
"ds_read_b128 a[8:11], v27 offset:2048\n"
"v_mfma_f32_16x16x32_bf16 v[156:159], a[176:179], v[92:95], v[156:159]\n"
"ds_read_b128 a[12:15], v27 offset:3072\n"
"v_mfma_f32_16x16x32_bf16 v[172:175], a[180:183], v[92:95], v[172:175]\n"
"s_add_u32 s59, s57, 0\n"
"s_add_u32 s59, s59, s63\n"
"buffer_load_dwordx4 a[144:147], v16, s[48:51], s59 offen sc1\n"
"ds_read_b128 a[16:19], v27 offset:4096\n"
"v_mfma_f32_16x16x32_bf16 v[188:191], a[184:187], v[92:95], v[188:191]\n"
"ds_read_b128 a[20:23], v27 offset:5120\n"
"v_mfma_f32_16x16x32_bf16 v[236:239], a[188:191], v[92:95], v[236:239]\n"
"ds_read_b128 a[24:27], v27 offset:6144\n"
"s_waitcnt vmcnt(21)\n"
"v_mfma_f32_16x16x32_bf16 v[128:131], a[192:195], v[96:99], v[128:131]\n"
"buffer_load_dwordx4 a[148:151], v17, s[48:51], s59 offen sc1\n"
"v_mfma_f32_16x16x32_bf16 v[144:147], a[196:199], v[96:99], v[144:147]\n"
"v_mfma_f32_16x16x32_bf16 v[160:163], a[200:203], v[96:99], v[160:163]\n"
"s_waitcnt lgkmcnt(6)\n"
"v_mfma_f32_16x16x32_bf16 v[176:179], a[0:3], v[96:99], v[176:179]\n"
"buffer_load_dwordx4 a[152:155], v18, s[48:51], s59 offen sc1\n"
"s_waitcnt lgkmcnt(5)\n"
"v_mfma_f32_16x16x32_bf16 v[224:227], a[4:7], v[96:99], v[224:227]\n"
"s_waitcnt vmcnt(22)\n"
"v_mfma_f32_16x16x32_bf16 v[132:135], a[192:195], v[100:103], v[132:135]\n"
"v_mfma_f32_16x16x32_bf16 v[148:151], a[196:199], v[100:103], v[148:151]\n"
"buffer_load_dwordx4 a[156:159], v19, s[48:51], s59 offen sc1\n"
"v_mfma_f32_16x16x32_bf16 v[164:167], a[200:203], v[100:103], v[164:167]\n"
"v_mfma_f32_16x16x32_bf16 v[180:183], a[0:3], v[100:103], v[180:183]\n"
"v_mfma_f32_16x16x32_bf16 v[228:231], a[4:7], v[100:103], v[228:231]\n"
"buffer_load_dwordx4 v[124:127], v6, s[48:51], s7 offen offset:3072 sc1\n"
"s_waitcnt vmcnt(23)\n"
"v_mfma_f32_16x16x32_bf16 v[136:139], a[192:195], v[104:107], v[136:139]\n"
"v_mfma_f32_16x16x32_bf16 v[152:155], a[196:199], v[104:107], v[152:155]\n"
"v_mfma_f32_16x16x32_bf16 v[168:171], a[200:203], v[104:107], v[168:171]\n"
"v_mfma_f32_16x16x32_bf16 v[184:187], a[0:3], v[104:107], v[184:187]\n"
"s_add_u32 s7, s7, 1024\n"
"buffer_load_dwordx4 v[208:211], v6, s[48:51], s7 offen offset:3072 sc1\n"
"v_mfma_f32_16x16x32_bf16 v[232:235], a[4:7], v[104:107], v[232:235]\n"
"s_waitcnt vmcnt(22)\n"
"v_mfma_f32_16x16x32_bf16 v[140:143], a[192:195], v[108:111], v[140:143]\n"
"ds_read_b128 a[28:31], v27 offset:7168\n"
"v_mfma_f32_16x16x32_bf16 v[156:159], a[196:199], v[108:111], v[156:159]\n"
"s_add_u32 s7, s79, 20480\n"
"s_add_u32 s7, s7, s1\n"
"buffer_load_dwordx4 v[212:215], v6, s[48:51], s7 offen offset:0 sc1\n"
"ds_read_b128 a[32:35], v27 offset:8192\n"
"v_mfma_f32_16x16x32_bf16 v[172:175], a[200:203], v[108:111], v[172:175]\n"
"ds_read_b128 a[36:39], v27 offset:9216\n"
"v_mfma_f32_16x16x32_bf16 v[188:191], a[0:3], v[108:111], v[188:191]\n"
"ds_read_b128 a[40:43], v28 offset:0\n"
"v_mfma_f32_16x16x32_bf16 v[236:239], a[4:7], v[108:111], v[236:239]\n"
"buffer_load_dwordx4 v[216:219], v6, s[48:51], s7 offen offset:1024 sc1\n"
"ds_read_b128 a[44:47], v28 offset:1024\n"
"s_waitcnt vmcnt(23) lgkmcnt(9)\n"
"v_mfma_f32_16x16x32_bf16 v[128:131], a[8:11], a[96:99], v[128:131]\n"
"s_waitcnt lgkmcnt(8)\n"
"v_mfma_f32_16x16x32_bf16 v[144:147], a[12:15], a[96:99], v[144:147]\n"
"s_waitcnt lgkmcnt(7)\n"
"v_mfma_f32_16x16x32_bf16 v[160:163], a[16:19], a[96:99], v[160:163]\n"
"buffer_load_dwordx4 v[220:223], v6, s[48:51], s7 offen offset:2048 sc1\n"
"s_waitcnt lgkmcnt(6)\n"
"v_mfma_f32_16x16x32_bf16 v[176:179], a[20:23], a[96:99], v[176:179]\n"
"s_waitcnt lgkmcnt(5)\n"
"v_mfma_f32_16x16x32_bf16 v[224:227], a[24:27], a[96:99], v[224:227]\n"
"s_waitcnt vmcnt(23)\n"
"v_mfma_f32_16x16x32_bf16 v[132:135], a[8:11], a[100:103], v[132:135]\n"
"s_lshl_b32 s94, 0x44, s97\n"
"s_lshl_b32 s95, 0x0, s97\n"
"s_waitcnt vmcnt(9)\n"
"v_cmp_eq_u64 vcc, s[84:85], v[8:9]\n"
"s_and_b64 s[10:11], vcc, s[94:95]\n"
"s_cmp_eq_u64 s[10:11], s[94:95]\n"
"s_cbranch_scc0 L_att_m_slow\n"
"L_att_m_ok:\n"
"v_mfma_f32_16x16x32_bf16 v[148:151], a[12:15], a[100:103], v[148:151]\n"
"s_add_u32 s59, s56, 4096\n"
"buffer_load_dwordx4 v[80:83], v16, s[48:51], s59 offen sc1\n"
"v_mfma_f32_16x16x32_bf16 v[164:167], a[16:19], a[100:103], v[164:167]\n"
"v_mfma_f32_16x16x32_bf16 v[180:183], a[20:23], a[100:103], v[180:183]\n"
"v_mfma_f32_16x16x32_bf16 v[228:231], a[24:27], a[100:103], v[228:231]\n"
"buffer_load_dwordx4 v[84:87], v17, s[48:51], s59 offen sc1\n"
"v_mfma_f32_16x16x32_bf16 v[136:139], a[8:11], a[104:107], v[136:139]\n"
"v_mfma_f32_16x16x32_bf16 v[152:155], a[12:15], a[104:107], v[152:155]\n"
"v_mfma_f32_16x16x32_bf16 v[168:171], a[16:19], a[104:107], v[168:171]\n"
"buffer_load_dwordx4 v[88:91], v18, s[48:51], s59 offen sc1\n"
"v_mfma_f32_16x16x32_bf16 v[184:187], a[20:23], a[104:107], v[184:187]\n"
"v_mfma_f32_16x16x32_bf16 v[232:235], a[24:27], a[104:107], v[232:235]\n"
"v_mfma_f32_16x16x32_bf16 v[140:143], a[8:11], a[108:111], v[140:143]\n"
"buffer_load_dwordx4 v[92:95], v19, s[48:51], s59 offen sc1\n"
"ds_read_b128 a[48:51], v28 offset:2048\n"
"v_mfma_f32_16x16x32_bf16 v[156:159], a[12:15], a[108:111], v[156:159]\n"
"ds_read_b128 a[52:55], v28 offset:3072\n"
"v_mfma_f32_16x16x32_bf16 v[172:175], a[16:19], a[108:111], v[172:175]\n"
"ds_read_b128 a[56:59], v28 offset:4096\n"
"v_mfma_f32_16x16x32_bf16 v[188:191], a[20:23], a[108:111], v[188:191]\n"
"ds_read_b128 a[60:63], v28 offset:5120\n"
"v_mfma_f32_16x16x32_bf16 v[236:239], a[24:27], a[108:111], v[236:239]\n"
"s_add_u32 s59, s56, 4096\n"
"s_add_u32 s59, s59, s63\n"
"buffer_load_dwordx4 v[96:99], v16, s[48:51], s59 offen sc1\n"
"ds_read_b128 a[64:67], v28 offset:6144\n"
"s_waitcnt lgkmcnt(9)\n"
"v_mfma_f32_16x16x32_bf16 v[128:131], a[28:31], a[112:115], v[128:131]\n"
"s_waitcnt lgkmcnt(8)\n"
"v_mfma_f32_16x16x32_bf16 v[144:147], a[32:35], a[112:115], v[144:147]\n"
"s_waitcnt lgkmcnt(7)\n"
"v_mfma_f32_16x16x32_bf16 v[160:163], a[36:39], a[112:115], v[160:163]\n"
"buffer_load_dwordx4 v[100:103], v17, s[48:51], s59 offen sc1\n"
"s_waitcnt lgkmcnt(6)\n"
"v_mfma_f32_16x16x32_bf16 v[176:179], a[40:43], a[112:115], v[176:179]\n"
"s_waitcnt lgkmcnt(5)\n"
"v_mfma_f32_16x16x32_bf16 v[224:227], a[44:47], a[112:115], v[224:227]\n"
"v_mfma_f32_16x16x32_bf16 v[132:135], a[28:31], a[116:119], v[132:135]\n"
"buffer_load_dwordx4 v[104:107], v18, s[48:51], s59 offen sc1\n"
"v_mfma_f32_16x16x32_bf16 v[148:151], a[32:35], a[116:119], v[148:151]\n"
"v_mfma_f32_16x16x32_bf16 v[164:167], a[36:39], a[116:119], v[164:167]\n"
"v_mfma_f32_16x16x32_bf16 v[180:183], a[40:43], a[116:119], v[180:183]\n"
"buffer_load_dwordx4 v[108:111], v19, s[48:51], s59 offen sc1\n"
"v_mfma_f32_16x16x32_bf16 v[228:231], a[44:47], a[116:119], v[228:231]\n"
"v_mfma_f32_16x16x32_bf16 v[136:139], a[28:31], a[120:123], v[136:139]\n"
"v_mfma_f32_16x16x32_bf16 v[152:155], a[32:35], a[120:123], v[152:155]\n"
"v_mfma_f32_16x16x32_bf16 v[168:171], a[36:39], a[120:123], v[168:171]\n"
"s_add_u32 s59, s62, 4096\n"
"buffer_load_dwordx4 a[96:99], v16, s[48:51], s59 offen sc1\n"
"v_mfma_f32_16x16x32_bf16 v[184:187], a[40:43], a[120:123], v[184:187]\n"
"v_mfma_f32_16x16x32_bf16 v[232:235], a[44:47], a[120:123], v[232:235]\n"
"v_mfma_f32_16x16x32_bf16 v[140:143], a[28:31], a[124:127], v[140:143]\n"
"buffer_load_dwordx4 a[100:103], v17, s[48:51], s59 offen sc1\n"
"ds_read_b128 a[68:71], v28 offset:7168\n"
"v_mfma_f32_16x16x32_bf16 v[156:159], a[32:35], a[124:127], v[156:159]\n"
"ds_read_b128 a[72:75], v28 offset:8192\n"
"v_mfma_f32_16x16x32_bf16 v[172:175], a[36:39], a[124:127], v[172:175]\n"
"ds_read_b128 a[76:79], v28 offset:9216\n"
"v_mfma_f32_16x16x32_bf16 v[188:191], a[40:43], a[124:127], v[188:191]\n"
"buffer_load_dwordx4 a[104:107], v18, s[48:51], s59 offen sc1\n"
"ds_read_b128 a[80:83], v28 offset:10240\n"
"v_mfma_f32_16x16x32_bf16 v[236:239], a[44:47], a[124:127], v[236:239]\n"
"ds_read_b128 a[84:87], v28 offset:11264\n"
"s_waitcnt lgkmcnt(9)\n"
"v_mfma_f32_16x16x32_bf16 v[128:131], a[48:51], a[128:131], v[128:131]\n"
"s_waitcnt lgkmcnt(8)\n"
"v_mfma_f32_16x16x32_bf16 v[144:147], a[52:55], a[128:131], v[144:147]\n"
"buffer_load_dwordx4 a[108:111], v19, s[48:51], s59 offen sc1\n"
"s_waitcnt lgkmcnt(7)\n"
"v_mfma_f32_16x16x32_bf16 v[160:163], a[56:59], a[128:131], v[160:163]\n"
"s_waitcnt lgkmcnt(6)\n"
"v_mfma_f32_16x16x32_bf16 v[176:179], a[60:63], a[128:131], v[176:179]\n"
"s_waitcnt lgkmcnt(5)\n"
"v_mfma_f32_16x16x32_bf16 v[224:227], a[64:67], a[128:131], v[224:227]\n"
"v_mfma_f32_16x16x32_bf16 v[132:135], a[48:51], a[132:135], v[132:135]\n"
"s_add_u32 s59, s62, 4096\n"
"s_add_u32 s59, s59, s63\n"
"buffer_load_dwordx4 a[112:115], v16, s[48:51], s59 offen sc1\n"
"v_mfma_f32_16x16x32_bf16 v[148:151], a[52:55], a[132:135], v[148:151]\n"
"v_mfma_f32_16x16x32_bf16 v[164:167], a[56:59], a[132:135], v[164:167]\n"
"v_mfma_f32_16x16x32_bf16 v[180:183], a[60:63], a[132:135], v[180:183]\n"
"buffer_load_dwordx4 v[240:243], v6, s[48:51], s7 offen offset:3072 sc1\n"
"v_mfma_f32_16x16x32_bf16 v[228:231], a[64:67], a[132:135], v[228:231]\n"
"v_mfma_f32_16x16x32_bf16 v[136:139], a[48:51], a[136:139], v[136:139]\n"
"v_mfma_f32_16x16x32_bf16 v[152:155], a[52:55], a[136:139], v[152:155]\n"
"s_add_u32 s7, s7, 1024\n"
"buffer_load_dwordx4 v[244:247], v6, s[48:51], s7 offen offset:3072 sc1\n"
"v_mfma_f32_16x16x32_bf16 v[168:171], a[56:59], a[136:139], v[168:171]\n"
"v_mfma_f32_16x16x32_bf16 v[184:187], a[60:63], a[136:139], v[184:187]\n"
"v_mfma_f32_16x16x32_bf16 v[232:235], a[64:67], a[136:139], v[232:235]\n"
"buffer_load_dwordx4 a[116:119], v17, s[48:51], s59 offen sc1\n"
"v_mfma_f32_16x16x32_bf16 v[140:143], a[48:51], a[140:143], v[140:143]\n"
"buffer_load_dwordx2 v[8:9], v32, s[48:51], s60 offen sc1\n"
"ds_read_b128 a[88:91], v28 offset:12288\n"
"v_mfma_f32_16x16x32_bf16 v[156:159], a[52:55], a[140:143], v[156:159]\n"
"ds_read_b128 a[92:95], v29 offset:0\n"
"v_mfma_f32_16x16x32_bf16 v[172:175], a[56:59], a[140:143], v[172:175]\n"
"buffer_load_dwordx4 a[120:123], v18, s[48:51], s59 offen sc1\n"
"ds_read_b128 v[48:51], v29 offset:1024\n"
"v_mfma_f32_16x16x32_bf16 v[188:191], a[60:63], a[140:143], v[188:191]\n"
"ds_read_b128 v[52:55], v29 offset:2048\n"
"v_mfma_f32_16x16x32_bf16 v[236:239], a[64:67], a[140:143], v[236:239]\n"
"ds_read_b128 v[56:59], v29 offset:3072\n"
"s_waitcnt vmcnt(26) lgkmcnt(9)\n"
"v_mfma_f32_16x16x32_bf16 v[128:131], a[68:71], a[144:147], v[128:131]\n"
"s_waitcnt lgkmcnt(8)\n"
"v_mfma_f32_16x16x32_bf16 v[144:147], a[72:75], a[144:147], v[144:147]\n"
"buffer_load_dwordx4 a[124:127], v19, s[48:51], s59 offen sc1\n"
"s_waitcnt lgkmcnt(7)\n"
"v_mfma_f32_16x16x32_bf16 v[160:163], a[76:79], a[144:147], v[160:163]\n"
"s_waitcnt lgkmcnt(6)\n"
"v_mfma_f32_16x16x32_bf16 v[176:179], a[80:83], a[144:147], v[176:179]\n"
"s_waitcnt lgkmcnt(5)\n"
"v_mfma_f32_16x16x32_bf16 v[224:227], a[84:87], a[144:147], v[224:227]\n"
"s_add_u32 s7, s6, 0\n"
"buffer_load_dwordx4 v[248:251], v6, s[48:51], s7 offen offset:0 sc1\n"
"s_waitcnt vmcnt(27)\n"
"v_mfma_f32_16x16x32_bf16 v[132:135], a[68:71], a[148:151], v[132:135]\n"
"v_mfma_f32_16x16x32_bf16 v[148:151], a[72:75], a[148:151], v[148:151]\n"
"v_mfma_f32_16x16x32_bf16 v[164:167], a[76:79], a[148:151], v[164:167]\n"
"buffer_load_dwordx4 v[252:255], v6, s[48:51], s7 offen offset:1024 sc1\n"
"v_mfma_f32_16x16x32_bf16 v[180:183], a[80:83], a[148:151], v[180:183]\n"
"v_mul_f32 v128, v21, v128\n"
"v_mul_f32 v129, v21, v129\n"
"v_mfma_f32_16x16x32_bf16 v[228:231], a[84:87], a[148:151], v[228:231]\n"
"v_mul_f32 v130, v21, v130\n"
"v_mul_f32 v131, v21, v131\n"
"v_mul_f32 v144, v21, v144\n"
"v_mul_f32 v145, v21, v145\n"
"s_waitcnt vmcnt(27)\n"
"v_mfma_f32_16x16x32_bf16 v[136:139], a[68:71], a[152:155], v[136:139]\n"
"buffer_load_dwordx4 a[204:207], v6, s[48:51], s7 offen offset:2048 sc1\n"
"v_mul_f32 v146, v21, v146\n"
"v_mul_f32 v147, v21, v147\n"
"v_mul_f32 v160, v21, v160\n"
"v_mul_f32 v161, v21, v161\n"
"v_mfma_f32_16x16x32_bf16 v[152:155], a[72:75], a[152:155], v[152:155]\n"
"v_mul_f32 v162, v21, v162\n"
"v_mul_f32 v163, v21, v163\n"
"v_mul_f32 v176, v21, v176\n"
"v_mul_f32 v177, v21, v177\n"
"v_mfma_f32_16x16x32_bf16 v[168:171], a[76:79], a[152:155], v[168:171]\n"
"v_mul_f32 v178, v21, v178\n"
"v_mul_f32 v179, v21, v179\n"
"v_mul_f32 v224, v21, v224\n"
"v_mul_f32 v225, v21, v225\n"
"v_mfma_f32_16x16x32_bf16 v[184:187], a[80:83], a[152:155], v[184:187]\n"
"v_mul_f32 v226, v21, v226\n"
"v_mul_f32 v227, v21, v227\n"
"v_mul_f32 v132, v22, v132\n"
"v_mul_f32 v133, v22, v133\n"
"v_mfma_f32_16x16x32_bf16 v[232:235], a[84:87], a[152:155], v[232:235]\n"
"buffer_load_dwordx4 a[208:211], v6, s[48:51], s7 offen offset:3072 sc1\n"
"v_mul_f32 v134, v22, v134\n"
"v_mul_f32 v135, v22, v135\n"
"v_mul_f32 v148, v22, v148\n"
"v_mul_f32 v149, v22, v149\n"
"s_waitcnt vmcnt(28)\n"
"v_mfma_f32_16x16x32_bf16 v[140:143], a[68:71], a[156:159], v[140:143]\n"
"ds_read_b128 v[60:63], v29 offset:4096\n"
"v_mul_f32 v150, v22, v150\n"
"v_mul_f32 v151, v22, v151\n"
"v_mul_f32 v164, v22, v164\n"
"v_mul_f32 v165, v22, v165\n"
"v_mfma_f32_16x16x32_bf16 v[156:159], a[72:75], a[156:159], v[156:159]\n"
"ds_read_b128 v[64:67], v29 offset:5120\n"
"v_mul_f32 v166, v22, v166\n"
"v_mul_f32 v167, v22, v167\n"
"v_mul_f32 v180, v22, v180\n"
"v_mul_f32 v181, v22, v181\n"
"v_mfma_f32_16x16x32_bf16 v[172:175], a[76:79], a[156:159], v[172:175]\n"
"s_add_u32 s7, s7, 1024\n"
"buffer_load_dwordx4 a[212:215], v6, s[48:51], s7 offen offset:3072 sc1\n"
"ds_read_b128 v[68:71], v29 offset:6144\n"
"v_mul_f32 v182, v22, v182\n"
"v_mul_f32 v183, v22, v183\n"
"v_mul_f32 v228, v22, v228\n"
"v_mul_f32 v229, v22, v229\n"
"v_mfma_f32_16x16x32_bf16 v[188:191], a[80:83], a[156:159], v[188:191]\n"
"ds_read_b128 v[72:75], v29 offset:7168\n"
"v_mul_f32 v230, v22, v230\n"
"v_mul_f32 v231, v22, v231\n"
"v_mul_f32 v136, v23, v136\n"
"v_mul_f32 v137, v23, v137\n"
"v_mfma_f32_16x16x32_bf16 v[236:239], a[84:87], a[156:159], v[236:239]\n"
"ds_read_b128 v[76:79], v29 offset:8192\n"
"v_mul_f32 v138, v23, v138\n"
"v_mul_f32 v139, v23, v139\n"
"v_mul_f32 v152, v23, v152\n"
"v_mul_f32 v153, v23, v153\n"
"s_waitcnt vmcnt(23) lgkmcnt(9)\n"
"v_mfma_f32_16x16x32_bf16 v[128:131], a[88:91], v[80:83], v[128:131]\n"
"s_add_u32 s7, s6, 5120\n"
"s_cmp_ge_u32 s7, s12\n"
"s_cselect_b32 s91, 25600, 0\n"
"s_sub_u32 s7, s7, s91\n"
"buffer_load_dwordx4 a[216:219], v6, s[48:51], s7 offen offset:0 sc1\n"
"v_mul_f32 v154, v23, v154\n"
"v_mul_f32 v155, v23, v155\n"
"v_mul_f32 v168, v23, v168\n"
"v_mul_f32 v169, v23, v169\n"
"s_waitcnt lgkmcnt(8)\n"
"v_mfma_f32_16x16x32_bf16 v[144:147], a[92:95], v[80:83], v[144:147]\n"
"v_mul_f32 v170, v23, v170\n"
"v_mul_f32 v171, v23, v171\n"
"v_mul_f32 v184, v23, v184\n"
"v_mul_f32 v185, v23, v185\n"
"s_waitcnt lgkmcnt(7)\n"
"v_mfma_f32_16x16x32_bf16 v[160:163], v[48:51], v[80:83], v[160:163]\n"
"v_mul_f32 v186, v23, v186\n"
"v_mul_f32 v187, v23, v187\n"
"v_mul_f32 v232, v23, v232\n"
"v_mul_f32 v233, v23, v233\n"
"s_waitcnt lgkmcnt(6)\n"
"v_mfma_f32_16x16x32_bf16 v[176:179], v[52:55], v[80:83], v[176:179]\n"
"buffer_load_dwordx4 a[220:223], v6, s[48:51], s7 offen offset:1024 sc1\n"
"v_mul_f32 v234, v23, v234\n"
"v_mul_f32 v235, v23, v235\n"
"v_mul_f32 v140, v24, v140\n"
"v_mul_f32 v141, v24, v141\n"
"s_waitcnt lgkmcnt(5)\n"
"v_mfma_f32_16x16x32_bf16 v[224:227], v[56:59], v[80:83], v[224:227]\n"
"v_mul_f32 v142, v24, v142\n"
"v_mul_f32 v143, v24, v143\n"
"v_mul_f32 v156, v24, v156\n"
"v_mul_f32 v157, v24, v157\n"
"s_waitcnt vmcnt(24)\n"
"v_mfma_f32_16x16x32_bf16 v[132:135], a[88:91], v[84:87], v[132:135]\n"
"s_lshl_b32 s94, 0x0, s97\n"
"s_lshl_b32 s95, 0x22, s97\n"
"s_waitcnt vmcnt(9)\n"
"v_cmp_eq_u64 vcc, s[84:85], v[8:9]\n"
"s_and_b64 s[10:11], vcc, s[94:95]\n"
"s_cmp_eq_u64 s[10:11], s[94:95]\n"
"s_cbranch_scc0 L_att_o_slow\n"
"L_att_o_ok:\n"
"v_mul_f32 v158, v24, v158\n"
"v_mul_f32 v159, v24, v159\n"
"v_mul_f32 v172, v24, v172\n"
"v_mul_f32 v173, v24, v173\n"
"v_mfma_f32_16x16x32_bf16 v[148:151], a[92:95], v[84:87], v[148:151]\n"
"v_mul_f32 v174, v24, v174\n"
"v_mul_f32 v175, v24, v175\n"
"v_mul_f32 v188, v24, v188\n"
"v_mul_f32 v189, v24, v189\n"
"v_mfma_f32_16x16x32_bf16 v[164:167], v[48:51], v[84:87], v[164:167]\n"
"s_add_u32 s59, s58, 0\n"
"buffer_load_dwordx4 a[128:131], v16, s[48:51], s59 offen sc1\n"
"v_mul_f32 v190, v24, v190\n"
"v_mul_f32 v191, v24, v191\n"
"v_mul_f32 v236, v24, v236\n"
"v_mul_f32 v237, v24, v237\n"
"v_mfma_f32_16x16x32_bf16 v[180:183], v[52:55], v[84:87], v[180:183]\n"
"v_mul_f32 v238, v24, v238\n"
"v_mul_f32 v239, v24, v239\n"
"v_mfma_f32_16x16x32_bf16 v[228:231], v[56:59], v[84:87], v[228:231]\n"
"v_mfma_f32_16x16x32_bf16 v[136:139], a[88:91], v[88:91], v[136:139]\n"
"buffer_load_dwordx4 a[132:135], v17, s[48:51], s59 offen sc1\n"
"v_mfma_f32_16x16x32_bf16 v[152:155], a[92:95], v[88:91], v[152:155]\n"
"v_mfma_f32_16x16x32_bf16 v[168:171], v[48:51], v[88:91], v[168:171]\n"
"v_mfma_f32_16x16x32_bf16 v[184:187], v[52:55], v[88:91], v[184:187]\n"
"buffer_load_dwordx4 a[136:139], v18, s[48:51], s59 offen sc1\n"
"v_mfma_f32_16x16x32_bf16 v[232:235], v[56:59], v[88:91], v[232:235]\n"
"v_mfma_f32_16x16x32_bf16 v[140:143], a[88:91], v[92:95], v[140:143]\n"
"ds_read_b128 v[112:115], v29 offset:9216\n"
"v_mfma_f32_16x16x32_bf16 v[156:159], a[92:95], v[92:95], v[156:159]\n"
"buffer_load_dwordx4 a[140:143], v19, s[48:51], s59 offen sc1\n"
"v_mfma_f32_16x16x32_bf16 v[172:175], v[48:51], v[92:95], v[172:175]\n"
"v_mfma_f32_16x16x32_bf16 v[188:191], v[52:55], v[92:95], v[188:191]\n"
"v_mfma_f32_16x16x32_bf16 v[236:239], v[56:59], v[92:95], v[236:239]\n"
"s_waitcnt lgkmcnt(5)\n"
"v_mfma_f32_16x16x32_bf16 v[128:131], v[60:63], v[96:99], v[128:131]\n"
"s_add_u32 s59, s58, 1024\n"
"s_cmp_ge_u32 s59, s66\n"
"s_cselect_b32 s91, 5120, 0\n"
"s_sub_u32 s59, s59, s91\n"
"buffer_load_dwordx4 a[144:147], v16, s[48:51], s59 offen sc1\n"
"s_waitcnt lgkmcnt(4)\n"
"v_mfma_f32_16x16x32_bf16 v[144:147], v[64:67], v[96:99], v[144:147]\n"
"s_waitcnt lgkmcnt(3)\n"
"v_mfma_f32_16x16x32_bf16 v[160:163], v[68:71], v[96:99], v[160:163]\n"
"s_waitcnt lgkmcnt(2)\n"
"v_mfma_f32_16x16x32_bf16 v[176:179], v[72:75], v[96:99], v[176:179]\n"
"buffer_load_dwordx4 a[224:227], v6, s[48:51], s7 offen offset:2048 sc1\n"
"s_waitcnt lgkmcnt(1)\n"
"v_mfma_f32_16x16x32_bf16 v[224:227], v[76:79], v[96:99], v[224:227]\n"
"v_mfma_f32_16x16x32_bf16 v[132:135], v[60:63], v[100:103], v[132:135]\n"
"v_mfma_f32_16x16x32_bf16 v[148:151], v[64:67], v[100:103], v[148:151]\n"
"buffer_load_dwordx4 a[228:231], v6, s[48:51], s7 offen offset:3072 sc1\n"
"v_mfma_f32_16x16x32_bf16 v[164:167], v[68:71], v[100:103], v[164:167]\n"
"v_mfma_f32_16x16x32_bf16 v[180:183], v[72:75], v[100:103], v[180:183]\n"
"v_mfma_f32_16x16x32_bf16 v[228:231], v[76:79], v[100:103], v[228:231]\n"
"s_add_u32 s7, s7, 1024\n"
"buffer_load_dwordx4 a[232:235], v6, s[48:51], s7 offen offset:3072 sc1\n"
"v_mfma_f32_16x16x32_bf16 v[136:139], v[60:63], v[104:107], v[136:139]\n"
"v_mfma_f32_16x16x32_bf16 v[152:155], v[64:67], v[104:107], v[152:155]\n"
"v_mfma_f32_16x16x32_bf16 v[168:171], v[68:71], v[104:107], v[168:171]\n"
"buffer_load_dwordx4 a[148:151], v17, s[48:51], s59 offen sc1\n"
"v_mfma_f32_16x16x32_bf16 v[184:187], v[72:75], v[104:107], v[184:187]\n"
"v_mfma_f32_16x16x32_bf16 v[232:235], v[76:79], v[104:107], v[232:235]\n"
"v_mfma_f32_16x16x32_bf16 v[140:143], v[60:63], v[108:111], v[140:143]\n"
"v_mfma_f32_16x16x32_bf16 v[156:159], v[64:67], v[108:111], v[156:159]\n"
"buffer_load_dwordx4 a[152:155], v18, s[48:51], s59 offen sc1\n"
"v_mfma_f32_16x16x32_bf16 v[172:175], v[68:71], v[108:111], v[172:175]\n"
"v_mfma_f32_16x16x32_bf16 v[188:191], v[72:75], v[108:111], v[188:191]\n"
"v_mfma_f32_16x16x32_bf16 v[236:239], v[76:79], v[108:111], v[236:239]\n"
"buffer_load_dwordx4 a[156:159], v19, s[48:51], s59 offen sc1\n"
"s_waitcnt lgkmcnt(0)\n"
"v_mfma_f32_16x16x32_bf16 v[128:131], v[112:115], a[96:99], v[128:131]\n"
"v_mfma_f32_16x16x32_bf16 v[144:147], v[116:119], a[96:99], v[144:147]\n"
"v_mfma_f32_16x16x32_bf16 v[160:163], v[120:123], a[96:99], v[160:163]\n"
"s_add_u32 s7, s6, 10240\n"
"s_cmp_ge_u32 s7, s12\n"
"s_cselect_b32 s91, 25600, 0\n"
"s_sub_u32 s7, s7, s91\n"
"buffer_load_dwordx4 a[236:239], v6, s[48:51], s7 offen offset:0 sc1\n"
"v_mfma_f32_16x16x32_bf16 v[176:179], v[124:127], a[96:99], v[176:179]\n"
"v_mfma_f32_16x16x32_bf16 v[224:227], v[208:211], a[96:99], v[224:227]\n"
"v_mfma_f32_16x16x32_bf16 v[132:135], v[112:115], a[100:103], v[132:135]\n"
"s_add_u32 s59, s58, 2048\n"
"s_cmp_ge_u32 s59, s66\n"
"s_cselect_b32 s91, 5120, 0\n"
"s_sub_u32 s59, s59, s91\n"
"buffer_load_dwordx4 v[80:83], v16, s[48:51], s59 offen sc1\n"
"v_mfma_f32_16x16x32_bf16 v[148:151], v[116:119], a[100:103], v[148:151]\n"
"v_mfma_f32_16x16x32_bf16 v[164:167], v[120:123], a[100:103], v[164:167]\n"
"v_mfma_f32_16x16x32_bf16 v[180:183], v[124:127], a[100:103], v[180:183]\n"
"v_mfma_f32_16x16x32_bf16 v[228:231], v[208:211], a[100:103], v[228:231]\n"
"buffer_load_dwordx4 a[240:243], v6, s[48:51], s7 offen offset:1024 sc1\n"
"v_mfma_f32_16x16x32_bf16 v[136:139], v[112:115], a[104:107], v[136:139]\n"
"v_mfma_f32_16x16x32_bf16 v[152:155], v[116:119], a[104:107], v[152:155]\n"
"v_mfma_f32_16x16x32_bf16 v[168:171], v[120:123], a[104:107], v[168:171]\n"
"buffer_load_dwordx4 a[244:247], v6, s[48:51], s7 offen offset:2048 sc1\n"
"v_mfma_f32_16x16x32_bf16 v[184:187], v[124:127], a[104:107], v[184:187]\n"
"v_mfma_f32_16x16x32_bf16 v[232:235], v[208:211], a[104:107], v[232:235]\n"
"v_mfma_f32_16x16x32_bf16 v[140:143], v[112:115], a[108:111], v[140:143]\n"
"buffer_load_dwordx4 a[248:251], v6, s[48:51], s7 offen offset:3072 sc1\n"
"v_mfma_f32_16x16x32_bf16 v[156:159], v[116:119], a[108:111], v[156:159]\n"
"v_mfma_f32_16x16x32_bf16 v[172:175], v[120:123], a[108:111], v[172:175]\n"
"v_mfma_f32_16x16x32_bf16 v[188:191], v[124:127], a[108:111], v[188:191]\n"
"s_add_u32 s7, s7, 1024\n"
"buffer_load_dwordx4 a[252:255], v6, s[48:51], s7 offen offset:3072 sc1\n"
"v_mfma_f32_16x16x32_bf16 v[236:239], v[208:211], a[108:111], v[236:239]\n"
"v_mfma_f32_16x16x32_bf16 v[128:131], v[212:215], a[112:115], v[128:131]\n"
"v_mfma_f32_16x16x32_bf16 v[144:147], v[216:219], a[112:115], v[144:147]\n"
"v_mfma_f32_16x16x32_bf16 v[160:163], v[220:223], a[112:115], v[160:163]\n"
"buffer_load_dwordx4 v[84:87], v17, s[48:51], s59 offen sc1\n"
"v_mfma_f32_16x16x32_bf16 v[176:179], v[240:243], a[112:115], v[176:179]\n"
"v_mfma_f32_16x16x32_bf16 v[224:227], v[244:247], a[112:115], v[224:227]\n"
"v_mfma_f32_16x16x32_bf16 v[132:135], v[212:215], a[116:119], v[132:135]\n"
"buffer_load_dwordx4 v[88:91], v18, s[48:51], s59 offen sc1\n"
"v_mfma_f32_16x16x32_bf16 v[148:151], v[216:219], a[116:119], v[148:151]\n"
"v_mfma_f32_16x16x32_bf16 v[164:167], v[220:223], a[116:119], v[164:167]\n"
"v_mfma_f32_16x16x32_bf16 v[180:183], v[240:243], a[116:119], v[180:183]\n"
"buffer_load_dwordx4 v[92:95], v19, s[48:51], s59 offen sc1\n"
"v_mfma_f32_16x16x32_bf16 v[228:231], v[244:247], a[116:119], v[228:231]\n"
"s_waitcnt vmcnt(28)\n"
"v_mfma_f32_16x16x32_bf16 v[136:139], v[212:215], a[120:123], v[136:139]\n"
"v_mfma_f32_16x16x32_bf16 v[152:155], v[216:219], a[120:123], v[152:155]\n"
"s_add_u32 s7, s6, 15360\n"
"s_cmp_ge_u32 s7, s12\n"
"s_cselect_b32 s91, 25600, 0\n"
"s_sub_u32 s7, s7, s91\n"
"buffer_load_dwordx4 v[192:195], v6, s[48:51], s7 offen offset:0 sc1\n"
"v_mfma_f32_16x16x32_bf16 v[168:171], v[220:223], a[120:123], v[168:171]\n"
"v_mfma_f32_16x16x32_bf16 v[184:187], v[240:243], a[120:123], v[184:187]\n"
"v_mfma_f32_16x16x32_bf16 v[232:235], v[244:247], a[120:123], v[232:235]\n"
"s_waitcnt vmcnt(28)\n"
"v_mfma_f32_16x16x32_bf16 v[140:143], v[212:215], a[124:127], v[140:143]\n"
"s_add_u32 s59, s58, 3072\n"
"s_cmp_ge_u32 s59, s66\n"
"s_cselect_b32 s91, 5120, 0\n"
"s_sub_u32 s59, s59, s91\n"
"buffer_load_dwordx4 v[96:99], v16, s[48:51], s59 offen sc1\n"
"v_mfma_f32_16x16x32_bf16 v[156:159], v[216:219], a[124:127], v[156:159]\n"
"v_mfma_f32_16x16x32_bf16 v[172:175], v[220:223], a[124:127], v[172:175]\n"
"v_mfma_f32_16x16x32_bf16 v[188:191], v[240:243], a[124:127], v[188:191]\n"
"buffer_load_dwordx4 v[196:199], v6, s[48:51], s7 offen offset:1024 sc1\n"
"v_mfma_f32_16x16x32_bf16 v[236:239], v[244:247], a[124:127], v[236:239]\n"
"s_waitcnt vmcnt(22)\n"
"v_mfma_f32_16x16x32_bf16 v[128:131], v[248:251], a[128:131], v[128:131]\n"
"v_mfma_f32_16x16x32_bf16 v[144:147], v[252:255], a[128:131], v[144:147]\n"
"buffer_load_dwordx4 v[200:203], v6, s[48:51], s7 offen offset:2048 sc1\n"
"v_mfma_f32_16x16x32_bf16 v[160:163], a[204:207], a[128:131], v[160:163]\n"
"v_mfma_f32_16x16x32_bf16 v[176:179], a[208:211], a[128:131], v[176:179]\n"
"v_mfma_f32_16x16x32_bf16 v[224:227], a[212:215], a[128:131], v[224:227]\n"
"buffer_load_dwordx4 v[204:207], v6, s[48:51], s7 offen offset:3072 sc1\n"
"s_waitcnt vmcnt(23)\n"
"v_mfma_f32_16x16x32_bf16 v[132:135], v[248:251], a[132:135], v[132:135]\n"
"v_mfma_f32_16x16x32_bf16 v[148:151], v[252:255], a[132:135], v[148:151]\n"
"v_mfma_f32_16x16x32_bf16 v[164:167], a[204:207], a[132:135], v[164:167]\n"
"s_add_u32 s7, s7, 1024\n"
"buffer_load_dwordx4 a[160:163], v6, s[48:51], s7 offen offset:3072 sc1\n"
"v_mfma_f32_16x16x32_bf16 v[180:183], a[208:211], a[132:135], v[180:183]\n"
"v_mfma_f32_16x16x32_bf16 v[228:231], a[212:215], a[132:135], v[228:231]\n"
"s_waitcnt vmcnt(23)\n"
"v_mfma_f32_16x16x32_bf16 v[136:139], v[248:251], a[136:139], v[136:139]\n"
"v_mfma_f32_16x16x32_bf16 v[152:155], v[252:255], a[136:139], v[152:155]\n"
"buffer_load_dwordx4 v[100:103], v17, s[48:51], s59 offen sc1\n"
"v_mfma_f32_16x16x32_bf16 v[168:171], a[204:207], a[136:139], v[168:171]\n"
"v_mfma_f32_16x16x32_bf16 v[184:187], a[208:211], a[136:139], v[184:187]\n"
"v_mfma_f32_16x16x32_bf16 v[232:235], a[212:215], a[136:139], v[232:235]\n"
"buffer_load_dwordx4 v[104:107], v18, s[48:51], s59 offen sc1\n"
"s_waitcnt vmcnt(24)\n"
"v_mfma_f32_16x16x32_bf16 v[140:143], v[248:251], a[140:143], v[140:143]\n"
"v_mfma_f32_16x16x32_bf16 v[156:159], v[252:255], a[140:143], v[156:159]\n"
"v_mfma_f32_16x16x32_bf16 v[172:175], a[204:207], a[140:143], v[172:175]\n"
"buffer_load_dwordx4 v[108:111], v19, s[48:51], s59 offen sc1\n"
"v_mfma_f32_16x16x32_bf16 v[188:191], a[208:211], a[140:143], v[188:191]\n"
"v_mfma_f32_16x16x32_bf16 v[236:239], a[212:215], a[140:143], v[236:239]\n"
"s_waitcnt vmcnt(24)\n"
"v_mfma_f32_16x16x32_bf16 v[128:131], a[216:219], a[144:147], v[128:131]\n"
"s_add_u32 s7, s6, 20480\n"
"s_cmp_ge_u32 s7, s12\n"
"s_cselect_b32 s91, 25600, 0\n"
"s_sub_u32 s7, s7, s91\n"
"buffer_load_dwordx4 a[164:167], v6, s[48:51], s7 offen offset:0 sc1\n"
"v_mfma_f32_16x16x32_bf16 v[144:147], a[220:223], a[144:147], v[144:147]\n"
"s_add_u32 s82, s76, 1\n"
"s_waitcnt vmcnt(24)\n"
"v_mfma_f32_16x16x32_bf16 v[160:163], a[224:227], a[144:147], v[160:163]\n"
"s_and_b32 s82, s82, 3\n"
"s_waitcnt vmcnt(23)\n"
"v_mfma_f32_16x16x32_bf16 v[176:179], a[228:231], a[144:147], v[176:179]\n"
"s_waitcnt vmcnt(22)\n"
"v_mfma_f32_16x16x32_bf16 v[224:227], a[232:235], a[144:147], v[224:227]\n"
"s_add_u32 s59, s58, 4096\n"
"s_cmp_ge_u32 s59, s66\n"
"s_cselect_b32 s91, 5120, 0\n"
"s_sub_u32 s59, s59, s91\n"
"buffer_load_dwordx4 a[100:103], v17, s[48:51], s59 offen sc1\n"
"s_waitcnt vmcnt(22)\n"
"v_mfma_f32_16x16x32_bf16 v[132:135], a[216:219], a[148:151], v[132:135]\n"
"s_mul_i32 s82, s82, 15360\n"
"v_mfma_f32_16x16x32_bf16 v[148:151], a[220:223], a[148:151], v[148:151]\n"
"v_add_u32 v36, s82, v6\n"
"v_mfma_f32_16x16x32_bf16 v[164:167], a[224:227], a[148:151], v[164:167]\n"
"buffer_load_dwordx4 a[168:171], v6, s[48:51], s7 offen offset:1024 sc1\n"
"v_mfma_f32_16x16x32_bf16 v[180:183], a[228:231], a[148:151], v[180:183]\n"
"s_add_u32 s82, s76, 2\n"
"v_mfma_f32_16x16x32_bf16 v[228:231], a[232:235], a[148:151], v[228:231]\n"
"s_waitcnt vmcnt(22)\n"
"v_mfma_f32_16x16x32_bf16 v[136:139], a[216:219], a[152:155], v[136:139]\n"
"buffer_load_dwordx4 a[172:175], v6, s[48:51], s7 offen offset:2048 sc1\n"
"s_and_b32 s82, s82, 3\n"
"v_mfma_f32_16x16x32_bf16 v[152:155], a[220:223], a[152:155], v[152:155]\n"
"v_mfma_f32_16x16x32_bf16 v[168:171], a[224:227], a[152:155], v[168:171]\n"
"s_mul_i32 s82, s82, 15360\n"
"v_mfma_f32_16x16x32_bf16 v[184:187], a[228:231], a[152:155], v[184:187]\n"
"buffer_load_dwordx4 a[176:179], v6, s[48:51], s7 offen offset:3072 sc1\n"
"v_mfma_f32_16x16x32_bf16 v[232:235], a[232:235], a[152:155], v[232:235]\n"
"v_add_u32 v37, s82, v6\n"
"s_waitcnt vmcnt(23)\n"
"v_mfma_f32_16x16x32_bf16 v[140:143], a[216:219], a[156:159], v[140:143]\n"
"s_add_u32 s82, s76, 3\n"
"v_mfma_f32_16x16x32_bf16 v[156:159], a[220:223], a[156:159], v[156:159]\n"
"v_mfma_f32_16x16x32_bf16 v[172:175], a[224:227], a[156:159], v[172:175]\n"
"s_add_u32 s7, s7, 1024\n"
"buffer_load_dwordx4 a[180:183], v6, s[48:51], s7 offen offset:3072 sc1\n"
"v_mfma_f32_16x16x32_bf16 v[188:191], a[228:231], a[156:159], v[188:191]\n"
"s_and_b32 s82, s82, 3\n"
"v_mfma_f32_16x16x32_bf16 v[236:239], a[232:235], a[156:159], v[236:239]\n"
"s_mul_i32 s82, s82, 15360\n"
"s_waitcnt vmcnt(22)\n"
"v_mfma_f32_16x16x32_bf16 v[128:131], a[236:239], v[80:83], v[128:131]\n"
"buffer_load_dwordx4 a[104:107], v18, s[48:51], s59 offen sc1\n"
"s_waitcnt vmcnt(22)\n"
"v_mfma_f32_16x16x32_bf16 v[144:147], a[240:243], v[80:83], v[144:147]\n"
"v_add_u32 v38, s82, v6\n"
"s_waitcnt vmcnt(21)\n"
"v_mfma_f32_16x16x32_bf16 v[160:163], a[244:247], v[80:83], v[160:163]\n"
"s_waitcnt vmcnt(20)\n"
"v_mfma_f32_16x16x32_bf16 v[176:179], a[248:251], v[80:83], v[176:179]\n"
"buffer_load_dwordx4 a[108:111], v19, s[48:51], s59 offen sc1\n"
"s_mul_i32 s82, s76, 15360\n"
"s_waitcnt vmcnt(20)\n"
"v_mfma_f32_16x16x32_bf16 v[224:227], a[252:255], v[80:83], v[224:227]\n"
"s_waitcnt vmcnt(19)\n"
"v_mfma_f32_16x16x32_bf16 v[132:135], a[236:239], v[84:87], v[132:135]\n"
"v_add_u32 v26, s82, v6\n"
"v_mfma_f32_16x16x32_bf16 v[148:151], a[240:243], v[84:87], v[148:151]\n"
"buffer_load_dwordx4 a[96:99], v16, s[48:51], s59 offen sc1\n"
"v_mfma_f32_16x16x32_bf16 v[164:167], a[244:247], v[84:87], v[164:167]\n"
"s_lshl_b32 s82, s88, 2\n"
"v_mfma_f32_16x16x32_bf16 v[180:183], a[248:251], v[84:87], v[180:183]\n"
"s_add_u32 s82, s82, s76\n"
"v_mfma_f32_16x16x32_bf16 v[228:231], a[252:255], v[84:87], v[228:231]\n"
"s_waitcnt vmcnt(19)\n"
"v_mfma_f32_16x16x32_bf16 v[136:139], a[236:239], v[88:91], v[136:139]\n"
"s_mul_i32 s82, s82, 20480\n"
"s_barrier\n"
"v_mfma_f32_16x16x32_bf16 v[152:155], a[240:243], v[88:91], v[152:155]\n"
"s_mul_i32 s83, s89, 160\n"
"v_mfma_f32_16x16x32_bf16 v[168:171], a[244:247], v[88:91], v[168:171]\n"
"v_mfma_f32_16x16x32_bf16 v[184:187], a[248:251], v[88:91], v[184:187]\n"
"s_add_u32 s61, s82, s83\n"
"v_mfma_f32_16x16x32_bf16 v[232:235], a[252:255], v[88:91], v[232:235]\n"
"v_and_b32 v12, 15, v1\n"
"s_waitcnt vmcnt(18)\n"
"v_mfma_f32_16x16x32_bf16 v[140:143], a[236:239], v[92:95], v[140:143]\n"
"v_mul_u32_u24 v12, 1280, v12\n"
"v_mfma_f32_16x16x32_bf16 v[156:159], a[240:243], v[92:95], v[156:159]\n"
"v_mfma_f32_16x16x32_bf16 v[172:175], a[244:247], v[92:95], v[172:175]\n"
"v_lshrrev_b32 v13, 4, v1\n"
"v_mfma_f32_16x16x32_bf16 v[188:191], a[248:251], v[92:95], v[188:191]\n"
"v_lshl_add_u32 v25, v13, 3, v12\n"
"v_mfma_f32_16x16x32_bf16 v[236:239], a[252:255], v[92:95], v[236:239]\n"
"v_bfe_u32 v13, v1, 4, 1\n"
"s_waitcnt vmcnt(16)\n"
"v_mfma_f32_16x16x32_bf16 v[128:131], v[192:195], v[96:99], v[128:131]\n"
"s_waitcnt vmcnt(15)\n"
"v_mfma_f32_16x16x32_bf16 v[144:147], v[196:199], v[96:99], v[144:147]\n"
"v_lshl_add_u32 v39, v13, 5, v12\n"
"s_waitcnt vmcnt(14)\n"
"v_mfma_f32_16x16x32_bf16 v[160:163], v[200:203], v[96:99], v[160:163]\n"
"v_lshrrev_b32 v13, 5, v1\n"
"s_waitcnt vmcnt(13)\n"
"v_mfma_f32_16x16x32_bf16 v[176:179], v[204:207], v[96:99], v[176:179]\n"
"v_lshl_add_u32 v39, v13, 4, v39\n"
"s_waitcnt vmcnt(12)\n"
"v_mfma_f32_16x16x32_bf16 v[224:227], a[160:163], v[96:99], v[224:227]\n"
"s_waitcnt vmcnt(11)\n"
"v_mfma_f32_16x16x32_bf16 v[132:135], v[192:195], v[100:103], v[132:135]\n"
"s_mov_b32 s36, s32\n"
"v_mfma_f32_16x16x32_bf16 v[148:151], v[196:199], v[100:103], v[148:151]\n"
"s_and_b32 s37, s33, 0xffff\n"
"v_mfma_f32_16x16x32_bf16 v[164:167], v[200:203], v[100:103], v[164:167]\n"
"s_mov_b32 s38, 0x280000\n"
"v_mfma_f32_16x16x32_bf16 v[180:183], v[204:207], v[100:103], v[180:183]\n"
"v_mfma_f32_16x16x32_bf16 v[228:231], a[160:163], v[100:103], v[228:231]\n"
"s_mov_b32 s39, 0x20000\n"
"s_waitcnt vmcnt(10)\n"
"v_mfma_f32_16x16x32_bf16 v[136:139], v[192:195], v[104:107], v[136:139]\n"
"v_mfma_f32_16x16x32_bf16 v[152:155], v[196:199], v[104:107], v[152:155]\n"
"v_mfma_f32_16x16x32_bf16 v[168:171], v[200:203], v[104:107], v[168:171]\n"
"v_mfma_f32_16x16x32_bf16 v[184:187], v[204:207], v[104:107], v[184:187]\n"
"v_mfma_f32_16x16x32_bf16 v[232:235], a[160:163], v[104:107], v[232:235]\n"
"s_waitcnt vmcnt(9)\n"
"v_mfma_f32_16x16x32_bf16 v[140:143], v[192:195], v[108:111], v[140:143]\n"
"v_mfma_f32_16x16x32_bf16 v[156:159], v[196:199], v[108:111], v[156:159]\n"
"v_mfma_f32_16x16x32_bf16 v[172:175], v[200:203], v[108:111], v[172:175]\n"
"v_mfma_f32_16x16x32_bf16 v[188:191], v[204:207], v[108:111], v[188:191]\n"
"v_mfma_f32_16x16x32_bf16 v[236:239], a[160:163], v[108:111], v[236:239]\n"
"s_waitcnt vmcnt(7)\n"
"v_mfma_f32_16x16x32_bf16 v[132:135], a[164:167], a[100:103], v[132:135]\n"
"s_waitcnt vmcnt(6)\n"
"v_mfma_f32_16x16x32_bf16 v[148:151], a[168:171], a[100:103], v[148:151]\n"
"s_waitcnt vmcnt(5)\n"
"v_mfma_f32_16x16x32_bf16 v[164:167], a[172:175], a[100:103], v[164:167]\n"
"s_waitcnt vmcnt(4)\n"
"v_mfma_f32_16x16x32_bf16 v[180:183], a[176:179], a[100:103], v[180:183]\n"
"s_waitcnt vmcnt(3)\n"
"v_mfma_f32_16x16x32_bf16 v[228:231], a[180:183], a[100:103], v[228:231]\n"
"s_waitcnt vmcnt(2)\n"
"v_mfma_f32_16x16x32_bf16 v[136:139], a[164:167], a[104:107], v[136:139]\n"
"ds_write_b128 v36, v[132:135] offset:0\n"
"v_mfma_f32_16x16x32_bf16 v[152:155], a[168:171], a[104:107], v[152:155]\n"
"ds_write_b128 v36, v[148:151] offset:1024\n"
"v_mfma_f32_16x16x32_bf16 v[168:171], a[172:175], a[104:107], v[168:171]\n"
"ds_write_b128 v36, v[164:167] offset:2048\n"
"v_mfma_f32_16x16x32_bf16 v[184:187], a[176:179], a[104:107], v[184:187]\n"
"ds_write_b128 v36, v[180:183] offset:3072\n"
"v_mfma_f32_16x16x32_bf16 v[232:235], a[180:183], a[104:107], v[232:235]\n"
"ds_write_b128 v36, v[228:231] offset:4096\n"
"s_waitcnt vmcnt(1)\n"
"v_mfma_f32_16x16x32_bf16 v[140:143], a[164:167], a[108:111], v[140:143]\n"
"ds_write_b128 v37, v[136:139] offset:5120\n"
"v_mfma_f32_16x16x32_bf16 v[156:159], a[168:171], a[108:111], v[156:159]\n"
"ds_write_b128 v37, v[152:155] offset:6144\n"
"v_mfma_f32_16x16x32_bf16 v[172:175], a[172:175], a[108:111], v[172:175]\n"
"ds_write_b128 v37, v[168:171] offset:7168\n"
"v_mfma_f32_16x16x32_bf16 v[188:191], a[176:179], a[108:111], v[188:191]\n"
"ds_write_b128 v37, v[184:187] offset:8192\n"
"v_mfma_f32_16x16x32_bf16 v[236:239], a[180:183], a[108:111], v[236:239]\n"
"ds_write_b128 v37, v[232:235] offset:9216\n"
"s_waitcnt vmcnt(0)\n"
"v_mfma_f32_16x16x32_bf16 v[128:131], a[164:167], a[96:99], v[128:131]\n"
"ds_write_b128 v38, v[140:143] offset:10240\n"
"v_mfma_f32_16x16x32_bf16 v[144:147], a[168:171], a[96:99], v[144:147]\n"
"ds_write_b128 v38, v[156:159] offset:11264\n"
"v_mfma_f32_16x16x32_bf16 v[160:163], a[172:175], a[96:99], v[160:163]\n"
"ds_write_b128 v38, v[172:175] offset:12288\n"
"v_mfma_f32_16x16x32_bf16 v[176:179], a[176:179], a[96:99], v[176:179]\n"
"ds_write_b128 v38, v[188:191] offset:13312\n"
"v_mfma_f32_16x16x32_bf16 v[224:227], a[180:183], a[96:99], v[224:227]\n"
"ds_write_b128 v38, v[236:239] offset:14336\n"
"s_waitcnt lgkmcnt(0)\n"
"s_barrier\n"
"ds_read_b128 v[48:51], v26 offset:0\n"
"ds_read_b128 v[52:55], v26 offset:5120\n"
"ds_read_b128 v[56:59], v26 offset:10240\n"
"ds_read_b128 v[60:63], v26 offset:1024\n"
"ds_read_b128 v[64:67], v26 offset:6144\n"
"ds_read_b128 v[68:71], v26 offset:11264\n"
"ds_read_b128 v[72:75], v26 offset:2048\n"
"ds_read_b128 v[76:79], v26 offset:7168\n"
"ds_read_b128 v[80:83], v26 offset:12288\n"
"ds_read_b128 v[84:87], v26 offset:3072\n"
"ds_read_b128 v[88:91], v26 offset:8192\n"
"ds_read_b128 v[92:95], v26 offset:13312\n"
"ds_read_b128 v[96:99], v26 offset:4096\n"
"ds_read_b128 v[100:103], v26 offset:9216\n"
"ds_read_b128 v[104:107], v26 offset:14336\n"
"s_waitcnt lgkmcnt(14)\n"
"v_pk_add_f32 v[48:49], v[48:49], v[128:129]\n"
"v_pk_add_f32 v[50:51], v[50:51], v[130:131]\n"
"s_waitcnt lgkmcnt(12)\n"
"v_pk_add_f32 v[52:53], v[52:53], v[56:57]\n"
"v_pk_add_f32 v[54:55], v[54:55], v[58:59]\n"
"v_pk_add_f32 v[48:49], v[48:49], v[52:53]\n"
"v_pk_add_f32 v[50:51], v[50:51], v[54:55]\n"
"v_cvt_pk_bf16_f32 v112, v48, v49\n"
"v_cvt_pk_bf16_f32 v113, v50, v51\n"
"buffer_store_dwordx2 v[112:113], v25, s[36:39], s61 offen offset:0\n"
"s_waitcnt lgkmcnt(11)\n"
"v_pk_add_f32 v[60:61], v[60:61], v[144:145]\n"
"v_pk_add_f32 v[62:63], v[62:63], v[146:147]\n"
"s_waitcnt lgkmcnt(9)\n"
"v_pk_add_f32 v[64:65], v[64:65], v[68:69]\n"
"v_pk_add_f32 v[66:67], v[66:67], v[70:71]\n"
"v_pk_add_f32 v[60:61], v[60:61], v[64:65]\n"
"v_pk_add_f32 v[62:63], v[62:63], v[66:67]\n"
"v_cvt_pk_bf16_f32 v114, v60, v61\n"
"v_cvt_pk_bf16_f32 v115, v62, v63\n"
"buffer_store_dwordx2 v[114:115], v25, s[36:39], s61 offen offset:32\n"
"s_waitcnt lgkmcnt(8)\n"
"v_pk_add_f32 v[72:73], v[72:73], v[160:161]\n"
"v_pk_add_f32 v[74:75], v[74:75], v[162:163]\n"
"s_waitcnt lgkmcnt(6)\n"
"v_pk_add_f32 v[76:77], v[76:77], v[80:81]\n"
"v_pk_add_f32 v[78:79], v[78:79], v[82:83]\n"
"v_pk_add_f32 v[72:73], v[72:73], v[76:77]\n"
"v_pk_add_f32 v[74:75], v[74:75], v[78:79]\n"
"v_cvt_pk_bf16_f32 v116, v72, v73\n"
"v_cvt_pk_bf16_f32 v117, v74, v75\n"
"buffer_store_dwordx2 v[116:117], v25, s[36:39], s61 offen offset:64\n"
"s_waitcnt lgkmcnt(5)\n"
"v_pk_add_f32 v[84:85], v[84:85], v[176:177]\n"
"v_pk_add_f32 v[86:87], v[86:87], v[178:179]\n"
"s_waitcnt lgkmcnt(3)\n"
"v_pk_add_f32 v[88:89], v[88:89], v[92:93]\n"
"v_pk_add_f32 v[90:91], v[90:91], v[94:95]\n"
"v_pk_add_f32 v[84:85], v[84:85], v[88:89]\n"
"v_pk_add_f32 v[86:87], v[86:87], v[90:91]\n"
"v_cvt_pk_bf16_f32 v118, v84, v85\n"
"v_cvt_pk_bf16_f32 v119, v86, v87\n"
"buffer_store_dwordx2 v[118:119], v25, s[36:39], s61 offen offset:96\n"
"s_waitcnt lgkmcnt(2)\n"
"v_pk_add_f32 v[96:97], v[96:97], v[224:225]\n"
"v_pk_add_f32 v[98:99], v[98:99], v[226:227]\n"
"s_waitcnt lgkmcnt(0)\n"
"v_pk_add_f32 v[100:101], v[100:101], v[104:105]\n"
"v_pk_add_f32 v[102:103], v[102:103], v[106:107]\n"
"v_pk_add_f32 v[96:97], v[96:97], v[100:101]\n"
"v_pk_add_f32 v[98:99], v[98:99], v[102:103]\n"
"v_cvt_pk_bf16_f32 v120, v96, v97\n"
"v_cvt_pk_bf16_f32 v121, v98, v99\n"
"buffer_store_dwordx2 v[120:121], v25, s[36:39], s61 offen offset:128\n"
"s_endpgm\n"
"L_ffn_body:\n"
"s_mov_b32 s48, s34\n"
"s_and_b32 s49, s35, 0xffff\n"
"s_mov_b32 s50, 0xe00000\n"
"s_mov_b32 s51, 0x20000\n"
"v_mov_b32 v2, 0\n"
"buffer_load_dwordx2 v[2:3], v2, s[48:51], 0 offen sc1\n"
"v_cmp_eq_u32 vcc, 0, %2\n"
"v_mov_b32 v13, 0x40000000\n"
"s_nop 0\n"
"v_cndmask_b32 v4, v13, v1, vcc\n"
"s_and_b32 s89, s100, 7\n"
"s_xor_b32 s82, s12, s100\n"
"s_and_b32 s82, s82, 1\n"
"s_bfe_u32 s83, s100, 0x10003\n"
"s_lshl_b32 s83, s83, 1\n"
"s_add_u32 s82, s82, s83\n"
"s_lshl_b32 s88, s101, 2\n"
"s_add_u32 s88, s88, s82\n"
"s_lshr_b32 s82, s100, 1\n"
"s_lshr_b32 s83, s100, 2\n"
"s_and_b32 s83, s83, 2\n"
"s_add_u32 s82, s82, s83\n"
"s_and_b32 s0, s82, 0\n"
"s_lshr_b32 s0, s101, 0\n"
"s_and_b32 s0, s0, 3\n"
"s_add_u32 s57, s56, 128\n"
"s_cmp_eq_u32 s57, 1280\n"
"s_cselect_b32 s57, 0, s57\n"
"s_mul_i32 s67, s57, 1728\n"
"s_add_u32 s58, s57, 128\n"
"s_cmp_eq_u32 s58, 1280\n"
"s_cselect_b32 s58, 0, s58\n"
"s_mul_i32 s68, s58, 1728\n"
"s_add_u32 s59, s58, 128\n"
"s_cmp_eq_u32 s59, 1280\n"
"s_cselect_b32 s59, 0, s59\n"
"s_mul_i32 s69, s59, 1728\n"
"s_add_u32 s60, s59, 128\n"
"s_cmp_eq_u32 s60, 1280\n"
"s_cselect_b32 s60, 0, s60\n"
"s_mul_i32 s70, s60, 1728\n"
"s_add_u32 s61, s60, 128\n"
"s_cmp_eq_u32 s61, 1280\n"
"s_cselect_b32 s61, 0, s61\n"
"s_mul_i32 s71, s61, 1728\n"
"s_add_u32 s62, s61, 128\n"
"s_cmp_eq_u32 s62, 1280\n"
"s_cselect_b32 s62, 0, s62\n"
"s_mul_i32 s72, s62, 1728\n"
"s_add_u32 s63, s62, 128\n"
"s_cmp_eq_u32 s63, 1280\n"
"s_cselect_b32 s63, 0, s63\n"
"s_mul_i32 s73, s63, 1728\n"
"s_add_u32 s64, s63, 128\n"
"s_cmp_eq_u32 s64, 1280\n"
"s_cselect_b32 s64, 0, s64\n"
"s_mul_i32 s74, s64, 1728\n"
"s_add_u32 s65, s64, 128\n"
"s_cmp_eq_u32 s65, 1280\n"
"s_cselect_b32 s65, 0, s65\n"
"s_mul_i32 s75, s65, 1728\n"
"v_and_b32 v12, 31, v1\n"
"v_bfe_u32 v13, v1, 1, 3\n"
"v_lshrrev_b32 v14, 5, v1\n"
"v_xor_b32 v13, v13, v14\n"
"v_lshlrev_b32 v12, 7, v12\n"
"v_add_u32 v12, s81, v12\n"
"v_add_u32 v12, 0x1e000, v12\n"
"v_xor_b32 v14, 0, v13\n"
"v_lshl_add_u32 v32, v14, 4, v12\n"
"v_xor_b32 v14, 2, v13\n"
"v_lshl_add_u32 v33, v14, 4, v12\n"
"v_xor_b32 v14, 4, v13\n"
"v_lshl_add_u32 v34, v14, 4, v12\n"
"v_xor_b32 v14, 6, v13\n"
"v_lshl_add_u32 v35, v14, 4, v12\n"
"v_bfe_u32 v12, v1, 1, 1\n"
"v_bfe_u32 v13, v1, 3, 1\n"
"v_lshl_add_u32 v13, v12, 1, v13\n"
"v_lshlrev_b32 v12, 11, v12\n"
"v_lshrrev_b32 v14, 5, v1\n"
"v_lshl_add_u32 v12, v14, 10, v12\n"
"v_bfe_u32 v14, v1, 2, 2\n"
"v_lshl_add_u32 v12, v14, 7, v12\n"
"v_bfe_u32 v14, v1, 4, 1\n"
"v_lshl_add_u32 v12, v14, 4, v12\n"
"v_and_b32 v14, 1, v1\n"
"v_lshl_add_u32 v12, v14, 3, v12\n"
"v_xor_b32 v14, 0, v13\n"
"v_lshl_add_u32 v20, v14, 5, v12\n"
"v_xor_b32 v14, 1, v13\n"
"v_lshl_add_u32 v21, v14, 5, v12\n"
"v_xor_b32 v14, 2, v13\n"
"v_lshl_add_u32 v22, v14, 5, v12\n"
"v_xor_b32 v14, 3, v13\n"
"v_lshl_add_u32 v23, v14, 5, v12\n"
"v_bfe_u32 v12, v1, 1, 1\n"
"v_mul_u32_u24 v26, 1056, v12\n"
"v_lshrrev_b32 v13, 5, v1\n"
"v_lshl_add_u32 v26, v13, 9, v26\n"
"v_bfe_u32 v13, v1, 2, 2\n"
"v_lshl_add_u32 v26, v13, 6, v26\n"
"v_bfe_u32 v13, v1, 4, 1\n"
"v_lshl_add_u32 v26, v13, 4, v26\n"
"v_and_b32 v13, 1, v1\n"
"v_lshl_add_u32 v26, v13, 3, v26\n"
"v_xor_b32 v27, 32, v26\n"
"v_add_u32 v12, 0, v1\n"
"v_mul_u32_u24 v13, 1639, v12\n"
"v_lshrrev_b32 v13, 16, v13\n"
"v_mul_u32_u24 v41, 40, v13\n"
"v_sub_u32 v12, v12, v41\n"
"v_and_b32 v13, 3, v13\n"
"v_mul_u32_u24 v13, 1280, v13\n"
"v_lshl_add_u32 v36, v12, 4, v13\n"
"v_add_u32 v12, 64, v1\n"
"v_mul_u32_u24 v13, 1639, v12\n"
"v_lshrrev_b32 v13, 16, v13\n"
"v_mul_u32_u24 v41, 40, v13\n"
"v_sub_u32 v12, v12, v41\n"
"v_and_b32 v13, 3, v13\n"
"v_mul_u32_u24 v13, 1280, v13\n"
"v_lshl_add_u32 v37, v12, 4, v13\n"
"v_add_u32 v12, 128, v1\n"
"v_mul_u32_u24 v13, 1639, v12\n"
"v_lshrrev_b32 v13, 16, v13\n"
"v_mul_u32_u24 v41, 40, v13\n"
"v_sub_u32 v12, v12, v41\n"
"v_and_b32 v13, 3, v13\n"
"v_mul_u32_u24 v13, 1280, v13\n"
"v_lshl_add_u32 v38, v12, 4, v13\n"
"v_add_u32 v12, 192, v1\n"
"v_mul_u32_u24 v13, 1639, v12\n"
"v_lshrrev_b32 v13, 16, v13\n"
"v_mul_u32_u24 v41, 40, v13\n"
"v_sub_u32 v12, v12, v41\n"
"v_and_b32 v13, 3, v13\n"
"v_mul_u32_u24 v13, 1280, v13\n"
"v_lshl_add_u32 v39, v12, 4, v13\n"
"v_add_u32 v12, 256, v1\n"
"v_mul_u32_u24 v13, 1639, v12\n"
"v_lshrrev_b32 v13, 16, v13\n"
"v_mul_u32_u24 v41, 40, v13\n"
"v_sub_u32 v12, v12, v41\n"
"v_and_b32 v13, 3, v13\n"
"v_mul_u32_u24 v13, 1280, v13\n"
"v_lshl_add_u32 v40, v12, 4, v13\n"
"v_lshrrev_b32 v12, 4, v1\n"
"v_bfe_u32 v13, v1, 2, 2\n"
"v_lshl_add_u32 v12, v12, 3, v13\n"
"v_mul_u32_u24 v12, 640, v12\n"
"v_and_b32 v13, 3, v1\n"
"v_lshl_add_u32 v12, v13, 3, v12\n"
"s_mul_i32 s82, s76, 160\n"
"s_add_u32 s82, s82, 0x22000\n"
"v_add_u32 v41, s82, v12\n"
"s_lshl_b32 s82, s78, 4\n"
"s_add_u32 s82, s82, s77\n"
"s_lshr_b32 s83, s82, 1\n"
"s_and_b32 s90, s82, 1\n"
"s_lshl_b32 s91, s90, 2\n"
"s_add_u32 s91, s91, s76\n"
"s_mul_i32 s91, s91, 74\n"
"s_add_u32 s91, s91, s83\n"
"s_mul_i32 s91, s91, 5120\n"
"s_add_u32 s15, s91, 0x100000\n"
"s_lshl_b32 s91, s90, 10\n"
"s_lshl_b32 s92, s83, 3\n"
"s_add_u32 s91, s91, s92\n"
"s_add_u32 s14, s91, 10240\n"
"s_lshr_b32 s91, s76, 1\n"
"s_lshl_b32 s91, s91, 4\n"
"s_and_b32 s92, s76, 1\n"
"s_lshl_b32 s92, s92, 2\n"
"s_add_u32 s91, s91, s92\n"
"s_lshl_b32 s92, s83, 5\n"
"s_add_u32 s13, s91, s92\n"
"s_lshl_b32 s91, s83, 2\n"
"s_add_u32 s91, s91, s76\n"
"s_sub_u32 s91, s91, 216\n"
"s_and_b32 s91, s91, 0xff\n"
"s_mul_i32 s92, s91, 205\n"
"s_lshr_b32 s92, s92, 11\n"
"s_mul_i32 s93, s92, 10\n"
"s_sub_u32 s91, s91, s93\n"
"s_mul_i32 s92, s92, 80\n"
"s_and_b32 s93, s91, 1\n"
"s_mul_i32 s93, s93, 20\n"
"s_add_u32 s92, s92, s93\n"
"s_and_b32 s91, s91, 14\n"
"s_lshl_b32 s93, s91, 2\n"
"s_add_u32 s92, s92, s93\n"
"s_cmp_ge_u32 s91, 5\n"
"s_cselect_b32 s93, 20, 0\n"
"s_add_u32 s92, s92, s93\n"
"s_cmp_eq_u32 s91, 4\n"
"s_cselect_b32 s91, 24, 4\n"
"s_cmp_ge_u32 s83, 54\n"
"s_cselect_b32 s13, s92, s13\n"
"s_cselect_b32 s91, s91, 8\n"
"s_cselect_b32 s52, s22, s28\n"
"s_cselect_b32 s53, s23, s29\n"
"s_mov_b32 s92, 819200\n"
"s_mov_b32 s93, 2211840\n"
"s_cselect_b32 s54, s92, s93\n"
"s_and_b32 s53, s53, 0xffff\n"
"s_mov_b32 s55, 0x20000\n"
"s_mul_i32 s13, s13, 1280\n"
"s_mul_i32 s90, s90, 640\n"
"s_add_u32 s13, s13, s90\n"
"s_mul_i32 s91, s91, 1280\n"
"v_cmp_lt_u32 vcc, 31, v1\n"
"v_mov_b32 v12, s91\n"
"s_add_u32 s97, s91, s13\n"
"s_mul_i32 s93, s76, 5120\n"
"v_cndmask_b32 v12, 0, v12, vcc\n"
"v_add_u32 v42, v12, v38\n"
"s_lshl_b32 s82, s78, 4\n"
"s_add_u32 s82, s82, s77\n"
"s_add_u32 s82, s82, 32\n"
"s_lshr_b32 s83, s82, 1\n"
"s_and_b32 s90, s82, 1\n"
"s_lshl_b32 s91, s90, 2\n"
"s_add_u32 s91, s91, s76\n"
"s_mul_i32 s91, s91, 74\n"
"s_add_u32 s91, s91, s83\n"
"s_mul_i32 s91, s91, 5120\n"
"s_add_u32 s12, s91, 0x100000\n"
"s_lshl_b32 s91, s90, 10\n"
"s_lshl_b32 s92, s83, 3\n"
"s_add_u32 s91, s91, s92\n"
"s_add_u32 s3, s91, 10240\n"
"s_lshr_b32 s91, s76, 1\n"
"s_lshl_b32 s91, s91, 4\n"
"s_and_b32 s92, s76, 1\n"
"s_lshl_b32 s92, s92, 2\n"
"s_add_u32 s91, s91, s92\n"
"s_lshl_b32 s92, s83, 5\n"
"s_add_u32 s94, s91, s92\n"
"s_lshl_b32 s91, s83, 2\n"
"s_add_u32 s91, s91, s76\n"
"s_sub_u32 s91, s91, 216\n"
"s_and_b32 s91, s91, 0xff\n"
"s_mul_i32 s92, s91, 205\n"
"s_lshr_b32 s92, s92, 11\n"
"s_mul_i32 s93, s92, 10\n"
"s_sub_u32 s91, s91, s93\n"
"s_mul_i32 s92, s92, 80\n"
"s_and_b32 s93, s91, 1\n"
"s_mul_i32 s93, s93, 20\n"
"s_add_u32 s92, s92, s93\n"
"s_and_b32 s91, s91, 14\n"
"s_lshl_b32 s93, s91, 2\n"
"s_add_u32 s92, s92, s93\n"
"s_cmp_ge_u32 s91, 5\n"
"s_cselect_b32 s93, 20, 0\n"
"s_add_u32 s92, s92, s93\n"
"s_cmp_eq_u32 s91, 4\n"
"s_cselect_b32 s91, 24, 4\n"
"s_cmp_ge_u32 s83, 54\n"
"s_cselect_b32 s94, s92, s94\n"
"s_cselect_b32 s91, s91, 8\n"
"s_cselect_b32 s4, s22, s28\n"
"s_cselect_b32 s5, s23, s29\n"
"s_mov_b32 s92, 819200\n"
"s_mov_b32 s93, 2211840\n"
"s_cselect_b32 s6, s92, s93\n"
"s_and_b32 s5, s5, 0xffff\n"
"s_mov_b32 s7, 0x20000\n"
"s_mul_i32 s94, s94, 1280\n"
"s_mul_i32 s90, s90, 640\n"
"s_add_u32 s94, s94, s90\n"
"s_mul_i32 s91, s91, 1280\n"
"s_sub_u32 s82, s82, 128\n"
"s_cmp_ge_u32 s82, 20\n"
"s_mov_b32 s92, 0x40000000\n"
"s_cselect_b32 s94, s92, s94\n"
"s_cselect_b32 s12, s92, s12\n"
"s_cselect_b32 s3, s92, s3\n"
"v_cmp_lt_u32 vcc, 31, v1\n"
"v_mov_b32 v12, s91\n"
"s_add_u32 s95, s91, s94\n"
"s_mul_i32 s93, s76, 5120\n"
"v_cndmask_b32 v12, 0, v12, vcc\n"
"v_add_u32 v43, v12, v38\n"
"v_bfe_u32 v12, v1, 4, 1\n"
"v_mul_u32_u24 v12, 0x12800, v12\n"
"v_and_b32 v13, 15, v1\n"
"v_lshrrev_b32 v14, 5, v1\n"
"v_lshl_add_u32 v13, v14, 4, v13\n"
"v_lshl_add_u32 v5, v13, 4, v12\n"
"s_lshl_b32 s82, s77, 2\n"
"s_add_u32 s82, s82, s76\n"
"s_mul_i32 s82, s82, 151552\n"
"s_add_u32 s86, s82, 0x400000\n"
"s_lshl_b32 s82, s77, 9\n"
"s_add_u32 s87, s82, 1024\n"
"s_lshl_b32 s82, s78, 5\n"
"s_add_u32 s87, s87, s82\n"
"s_lshl_b32 s82, s78, 10\n"
"s_add_u32 s82, s82, s86\n"
"s_add_u32 s82, s82, 49152\n"
"s_cmp_ge_u32 s78, 6\n"
"s_cselect_b32 s98, 0x40000000, s82\n"
"s_mul_i32 s82, s78, 6144\n"
"s_add_u32 s86, s86, s82\n"
"s_add_u32 m0, s81, 0xa000\n"
"s_nop 0\n"
"buffer_load_dwordx4 v16, s[40:43], s67 offen lds\n"
"buffer_load_dwordx4 v17, s[40:43], s67 offen offset:1024 lds\n"
"buffer_load_dwordx4 v18, s[44:47], s67 offen offset:2048 lds\n"
"buffer_load_dwordx4 v19, s[44:47], s67 offen offset:3072 lds\n"
"s_add_u32 s79, s67, 128\n"
"s_add_u32 m0, s81, 0xe000\n"
"s_nop 0\n"
"buffer_load_dwordx4 v16, s[40:43], s79 offen lds\n"
"buffer_load_dwordx4 v17, s[40:43], s79 offen offset:1024 lds\n"
"buffer_load_dwordx4 v18, s[44:47], s79 offen offset:2048 lds\n"
"buffer_load_dwordx4 v19, s[44:47], s79 offen offset:3072 lds\n"
"s_add_u32 s79, s67, s99\n"
"s_add_u32 m0, s80, 0x12000\n"
"s_nop 0\n"
"buffer_load_dwordx4 v24, s[40:43], s79 offen lds\n"
"buffer_load_dwordx4 v25, s[44:47], s79 offen offset:1024 lds\n"
"s_waitcnt vmcnt(11)\n"
"s_barrier\n"
"ds_read_b128 a[0:3], v32 offset:0\n"
"ds_read_b128 a[4:7], v33 offset:0\n"
"ds_read_b64_tr_b16 v[80:81], v20 offset:0\n"
"ds_read_b64_tr_b16 v[82:83], v20 offset:512\n"
"ds_read_b128 a[8:11], v34 offset:0\n"
"ds_read_b128 a[12:15], v35 offset:0\n"
"ds_read_b64_tr_b16 v[84:85], v21 offset:0\n"
"ds_read_b64_tr_b16 v[86:87], v21 offset:512\n"
"ds_read_b64_tr_b16 v[88:89], v22 offset:0\n"
"ds_read_b64_tr_b16 v[90:91], v22 offset:512\n"
"ds_read_b64_tr_b16 v[92:93], v23 offset:0\n"
"ds_read_b64_tr_b16 v[94:95], v23 offset:512\n"
"s_waitcnt lgkmcnt(4)\n"
"v_mfma_f32_32x32x16_bf16 v[128:143], v[80:83], a[0:3], 0\n"
"ds_read_b64_tr_b16 v[96:97], v20 offset:16384\n"
"ds_read_b64_tr_b16 v[98:99], v20 offset:16896\n"
"s_add_u32 m0, s81, 0x1e000\n"
"s_nop 0\n"
"buffer_load_dwordx4 v28, s[36:39], s57 offen lds\n"
"v_mfma_f32_32x32x16_bf16 v[144:159], v[84:87], a[0:3], 0\n"
"ds_read_b64_tr_b16 v[100:101], v21 offset:16384\n"
"ds_read_b64_tr_b16 v[102:103], v21 offset:16896\n"
"buffer_load_dwordx4 v29, s[36:39], s57 offen offset:1024 lds\n"
"s_waitcnt lgkmcnt(4)\n"
"v_mfma_f32_32x32x16_bf16 v[160:175], v[88:91], a[0:3], 0\n"
"ds_read_b64_tr_b16 v[104:105], v22 offset:16384\n"
"ds_read_b64_tr_b16 v[106:107], v22 offset:16896\n"
"buffer_load_dwordx4 v30, s[36:39], s57 offen offset:2048 lds\n"
"v_mfma_f32_32x32x16_bf16 v[176:191], v[92:95], a[0:3], 0\n"
"ds_read_b64_tr_b16 v[108:109], v23 offset:16384\n"
"ds_read_b64_tr_b16 v[110:111], v23 offset:16896\n"
"buffer_load_dwordx4 v31, s[36:39], s57 offen offset:3072 lds\n"
"s_waitcnt lgkmcnt(4)\n"
"v_mfma_f32_32x32x16_bf16 v[192:207], v[96:99], a[0:3], 0\n"
"ds_read_b64_tr_b16 v[80:81], v26 offset:32768\n"
"ds_read_b64_tr_b16 v[82:83], v26 offset:33024\n"
"v_mfma_f32_32x32x16_bf16 v[208:223], v[100:103], a[0:3], 0\n"
"s_add_u32 m0, s81, 0x14000\n"
"ds_read_b64_tr_b16 v[84:85], v27 offset:32768\n"
"ds_read_b64_tr_b16 v[86:87], v27 offset:33024\n"
"buffer_load_dwordx4 v16, s[40:43], s68 offen lds\n"
"s_waitcnt lgkmcnt(4)\n"
"v_mfma_f32_32x32x16_bf16 v[224:239], v[104:107], a[0:3], 0\n"
"ds_read_b64_tr_b16 v[88:89], v20 offset:4096\n"
"ds_read_b64_tr_b16 v[90:91], v20 offset:4608\n"
"v_mfma_f32_32x32x16_bf16 v[240:255], v[108:111], a[0:3], 0\n"
"ds_read_b64_tr_b16 v[92:93], v21 offset:4096\n"
"ds_read_b64_tr_b16 v[94:95], v21 offset:4608\n"
"buffer_load_dwordx4 v17, s[40:43], s68 offen offset:1024 lds\n"
"s_waitcnt lgkmcnt(4)\n"
"v_mfma_f32_32x32x16_bf16 v[48:63], v[80:83], a[0:3], 0\n"
"ds_read_b64_tr_b16 v[96:97], v22 offset:4096\n"
"ds_read_b64_tr_b16 v[98:99], v22 offset:4608\n"
"v_mfma_f32_32x32x16_bf16 v[64:79], v[84:87], a[0:3], 0\n"
"ds_read_b64_tr_b16 v[100:101], v23 offset:4096\n"
"ds_read_b64_tr_b16 v[102:103], v23 offset:4608\n"
"ds_read_b64_tr_b16 v[104:105], v20 offset:20480\n"
"ds_read_b64_tr_b16 v[106:107], v20 offset:20992\n"
"s_waitcnt lgkmcnt(6)\n"
"v_mfma_f32_32x32x16_bf16 v[128:143], v[88:91], a[4:7], v[128:143]\n"
"ds_read_b64_tr_b16 v[108:109], v21 offset:20480\n"
"ds_read_b64_tr_b16 v[110:111], v21 offset:20992\n"
"v_mfma_f32_32x32x16_bf16 v[144:159], v[92:95], a[4:7], v[144:159]\n"
"ds_read_b64_tr_b16 v[80:81], v22 offset:20480\n"
"ds_read_b64_tr_b16 v[82:83], v22 offset:20992\n"
"buffer_load_dwordx4 v18, s[44:47], s68 offen offset:2048 lds\n"
"s_waitcnt lgkmcnt(6)\n"
"v_mfma_f32_32x32x16_bf16 v[160:175], v[96:99], a[4:7], v[160:175]\n"
"s_add_u32 m0, s93, 0x22000\n"
"ds_read_b64_tr_b16 v[84:85], v23 offset:20480\n"
"ds_read_b64_tr_b16 v[86:87], v23 offset:20992\n"
"buffer_load_dwordx4 v36, s[52:55], s13 offen lds\n"
"v_mfma_f32_32x32x16_bf16 v[176:191], v[100:103], a[4:7], v[176:191]\n"
"s_add_u32 m0, s81, 0x14000\n"
"ds_read_b64_tr_b16 v[88:89], v26 offset:34816\n"
"ds_read_b64_tr_b16 v[90:91], v26 offset:35072\n"
"buffer_load_dwordx4 v19, s[44:47], s68 offen offset:3072 lds\n"
"s_waitcnt lgkmcnt(6)\n"
"v_mfma_f32_32x32x16_bf16 v[192:207], v[104:107], a[4:7], v[192:207]\n"
"ds_read_b64_tr_b16 v[92:93], v27 offset:34816\n"
"ds_read_b64_tr_b16 v[94:95], v27 offset:35072\n"
"s_waitcnt vmcnt(19)\n"
"v_readfirstlane_b32 s90, v2\n"
"v_mfma_f32_32x32x16_bf16 v[208:223], v[108:111], a[4:7], v[208:223]\n"
"ds_read_b64_tr_b16 v[96:97], v20 offset:8192\n"
"ds_read_b64_tr_b16 v[98:99], v20 offset:8704\n"
"v_readfirstlane_b32 s91, v3\n"
"s_waitcnt lgkmcnt(6)\n"
"v_mfma_f32_32x32x16_bf16 v[224:239], v[80:83], a[4:7], v[224:239]\n"
"s_add_u32 m0, s93, 0x22400\n"
"ds_read_b64_tr_b16 v[100:101], v21 offset:8192\n"
"ds_read_b64_tr_b16 v[102:103], v21 offset:8704\n"
"buffer_load_dwordx4 v37, s[52:55], s13 offen lds\n"
"v_mfma_f32_32x32x16_bf16 v[240:255], v[84:87], a[4:7], v[240:255]\n"
"s_add_u32 s79, s68, 128\n"
"s_add_u32 m0, s81, 0x18000\n"
"ds_read_b64_tr_b16 v[104:105], v22 offset:8192\n"
"ds_read_b64_tr_b16 v[106:107], v22 offset:8704\n"
"buffer_load_dwordx4 v16, s[40:43], s79 offen lds\n"
"s_waitcnt lgkmcnt(6)\n"
"v_mfma_f32_32x32x16_bf16 v[48:63], v[88:91], a[4:7], v[48:63]\n"
"ds_read_b64_tr_b16 v[108:109], v23 offset:8192\n"
"ds_read_b64_tr_b16 v[110:111], v23 offset:8704\n"
"v_mfma_f32_32x32x16_bf16 v[64:79], v[92:95], a[4:7], v[64:79]\n"
"ds_read_b64_tr_b16 v[80:81], v20 offset:24576\n"
"ds_read_b64_tr_b16 v[82:83], v20 offset:25088\n"
"s_mul_i32 s84, s90, 0x4c957f2d\n"
"s_waitcnt lgkmcnt(6)\n"
"v_mfma_f32_32x32x16_bf16 v[128:143], v[96:99], a[8:11], v[128:143]\n"
"ds_read_b64_tr_b16 v[84:85], v21 offset:24576\n"
"ds_read_b64_tr_b16 v[86:87], v21 offset:25088\n"
"s_mul_hi_u32 s85, s90, 0x4c957f2d\n"
"v_mfma_f32_32x32x16_bf16 v[144:159], v[100:103], a[8:11], v[144:159]\n"
"ds_read_b64_tr_b16 v[88:89], v22 offset:24576\n"
"ds_read_b64_tr_b16 v[90:91], v22 offset:25088\n"
"ds_read_b64_tr_b16 v[92:93], v23 offset:24576\n"
"ds_read_b64_tr_b16 v[94:95], v23 offset:25088\n"
"buffer_load_dwordx4 v17, s[40:43], s79 offen offset:1024 lds\n"
"s_add_u32 m0, s93, 0x22800\n"
"s_nop 0\n"
"buffer_load_dwordx4 v42, s[52:55], s13 offen lds\n"
"s_waitcnt lgkmcnt(8)\n"
"v_mfma_f32_32x32x16_bf16 v[160:175], v[104:107], a[8:11], v[160:175]\n"
"ds_read_b64_tr_b16 v[96:97], v26 offset:36864\n"
"ds_read_b64_tr_b16 v[98:99], v26 offset:37120\n"
"v_mfma_f32_32x32x16_bf16 v[176:191], v[108:111], a[8:11], v[176:191]\n"
"s_add_u32 m0, s81, 0x18000\n"
"ds_read_b64_tr_b16 v[100:101], v27 offset:36864\n"
"ds_read_b64_tr_b16 v[102:103], v27 offset:37120\n"
"buffer_load_dwordx4 v18, s[44:47], s79 offen offset:2048 lds\n"
"s_mul_i32 s90, s90, 0x5851f42d\n"
"s_waitcnt lgkmcnt(8)\n"
"v_mfma_f32_32x32x16_bf16 v[192:207], v[80:83], a[8:11], v[192:207]\n"
"ds_read_b64_tr_b16 v[104:105], v20 offset:12288\n"
"ds_read_b64_tr_b16 v[106:107], v20 offset:12800\n"
"v_mfma_f32_32x32x16_bf16 v[208:223], v[84:87], a[8:11], v[208:223]\n"
"ds_read_b64_tr_b16 v[108:109], v21 offset:12288\n"
"ds_read_b64_tr_b16 v[110:111], v21 offset:12800\n"
"s_mul_i32 s91, s91, 0x4c957f2d\n"
"s_waitcnt lgkmcnt(8)\n"
"v_mfma_f32_32x32x16_bf16 v[224:239], v[88:91], a[8:11], v[224:239]\n"
"s_add_u32 m0, s93, 0x22c00\n"
"ds_read_b64_tr_b16 v[80:81], v22 offset:12288\n"
"ds_read_b64_tr_b16 v[82:83], v22 offset:12800\n"
"buffer_load_dwordx4 v39, s[52:55], s97 offen lds\n"
"v_mfma_f32_32x32x16_bf16 v[240:255], v[92:95], a[8:11], v[240:255]\n"
"s_add_u32 m0, s81, 0x18000\n"
"ds_read_b64_tr_b16 v[84:85], v23 offset:12288\n"
"ds_read_b64_tr_b16 v[86:87], v23 offset:12800\n"
"buffer_load_dwordx4 v19, s[44:47], s79 offen offset:3072 lds\n"
"s_add_u32 s85, s85, s90\n"
"s_waitcnt lgkmcnt(8)\n"
"v_mfma_f32_32x32x16_bf16 v[48:63], v[96:99], a[8:11], v[48:63]\n"
"ds_read_b64_tr_b16 v[88:89], v20 offset:28672\n"
"ds_read_b64_tr_b16 v[90:91], v20 offset:29184\n"
"v_mfma_f32_32x32x16_bf16 v[64:79], v[100:103], a[8:11], v[64:79]\n"
"s_add_u32 s79, s68, s99\n"
"s_add_u32 m0, s80, 0x1c000\n"
"ds_read_b64_tr_b16 v[92:93], v21 offset:28672\n"
"ds_read_b64_tr_b16 v[94:95], v21 offset:29184\n"
"buffer_load_dwordx4 v24, s[40:43], s79 offen lds\n"
"s_waitcnt lgkmcnt(8)\n"
"v_mfma_f32_32x32x16_bf16 v[128:143], v[104:107], a[12:15], v[128:143]\n"
"ds_read_b64_tr_b16 v[96:97], v22 offset:28672\n"
"ds_read_b64_tr_b16 v[98:99], v22 offset:29184\n"
"s_add_u32 s85, s85, s91\n"
"v_mfma_f32_32x32x16_bf16 v[144:159], v[108:111], a[12:15], v[144:159]\n"
"s_add_u32 m0, s93, 0x23000\n"
"ds_read_b64_tr_b16 v[100:101], v23 offset:28672\n"
"ds_read_b64_tr_b16 v[102:103], v23 offset:29184\n"
"buffer_load_dwordx4 v40, s[52:55], s97 offen lds\n"
"s_waitcnt lgkmcnt(8)\n"
"v_mfma_f32_32x32x16_bf16 v[160:175], v[80:83], a[12:15], v[160:175]\n"
"ds_read_b64_tr_b16 v[104:105], v26 offset:38912\n"
"ds_read_b64_tr_b16 v[106:107], v26 offset:39168\n"
"v_mfma_f32_32x32x16_bf16 v[176:191], v[84:87], a[12:15], v[176:191]\n"
"s_add_u32 m0, s80, 0x1c000\n"
"ds_read_b64_tr_b16 v[108:109], v27 offset:38912\n"
"ds_read_b64_tr_b16 v[110:111], v27 offset:39168\n"
"buffer_load_dwordx4 v25, s[44:47], s79 offen offset:1024 lds\n"
"s_add_u32 s84, s84, 0xf767814f\n"
"s_addc_u32 s85, s85, 0x14057b7e\n"
"s_waitcnt lgkmcnt(8)\n"
"v_mfma_f32_32x32x16_bf16 v[192:207], v[88:91], a[12:15], v[192:207]\n"
"v_add_u32 v20, 0xa000, v20\n"
"v_add_u32 v21, 0xa000, v21\n"
"v_add_u32 v22, 0xa000, v22\n"
"v_mfma_f32_32x32x16_bf16 v[208:223], v[92:95], a[12:15], v[208:223]\n"
"v_add_u32 v23, 0xa000, v23\n"
"v_add_u32 v26, 0xa000, v26\n"
"v_add_u32 v27, 0xa000, v27\n"
"s_waitcnt lgkmcnt(4)\n"
"v_mfma_f32_32x32x16_bf16 v[224:239], v[96:99], a[12:15], v[224:239]\n"
"s_waitcnt vmcnt(15) lgkmcnt(0)\n"
"s_barrier\n"
"ds_read_b128 a[16:19], v32 offset:0\n"
"ds_read_b128 a[20:23], v33 offset:0\n"
"ds_read_b64_tr_b16 v[80:81], v20 offset:0\n"
"ds_read_b64_tr_b16 v[82:83], v20 offset:512\n"
"v_mfma_f32_32x32x16_bf16 v[240:255], v[100:103], a[12:15], v[240:255]\n"
"ds_read_b128 a[24:27], v34 offset:0\n"
"ds_read_b128 a[28:31], v35 offset:0\n"
"ds_read_b64_tr_b16 v[84:85], v21 offset:0\n"
"ds_read_b64_tr_b16 v[86:87], v21 offset:512\n"
"v_mfma_f32_32x32x16_bf16 v[48:63], v[104:107], a[12:15], v[48:63]\n"
"ds_read_b64_tr_b16 v[88:89], v22 offset:0\n"
"ds_read_b64_tr_b16 v[90:91], v22 offset:512\n"
"v_mov_b32 v2, s84\n"
"v_mfma_f32_32x32x16_bf16 v[64:79], v[108:111], a[12:15], v[64:79]\n"
"ds_read_b64_tr_b16 v[92:93], v23 offset:0\n"
"ds_read_b64_tr_b16 v[94:95], v23 offset:512\n"
"v_mov_b32 v3, s85\n"
"s_waitcnt lgkmcnt(4)\n"
"v_mfma_f32_32x32x16_bf16 v[128:143], v[80:83], a[16:19], v[128:143]\n"
"ds_read_b64_tr_b16 v[96:97], v20 offset:16384\n"
"ds_read_b64_tr_b16 v[98:99], v20 offset:16896\n"
"s_add_u32 m0, s81, 0x1e000\n"
"s_nop 0\n"
"buffer_load_dwordx4 v28, s[36:39], s58 offen lds\n"
"v_mfma_f32_32x32x16_bf16 v[144:159], v[84:87], a[16:19], v[144:159]\n"
"ds_read_b64_tr_b16 v[100:101], v21 offset:16384\n"
"ds_read_b64_tr_b16 v[102:103], v21 offset:16896\n"
"buffer_load_dwordx4 v29, s[36:39], s58 offen offset:1024 lds\n"
"s_waitcnt lgkmcnt(4)\n"
"v_mfma_f32_32x32x16_bf16 v[160:175], v[88:91], a[16:19], v[160:175]\n"
"ds_read_b64_tr_b16 v[104:105], v22 offset:16384\n"
"ds_read_b64_tr_b16 v[106:107], v22 offset:16896\n"
"buffer_load_dwordx4 v30, s[36:39], s58 offen offset:2048 lds\n"
"v_mfma_f32_32x32x16_bf16 v[176:191], v[92:95], a[16:19], v[176:191]\n"
"ds_read_b64_tr_b16 v[108:109], v23 offset:16384\n"
"ds_read_b64_tr_b16 v[110:111], v23 offset:16896\n"
"buffer_load_dwordx4 v31, s[36:39], s58 offen offset:3072 lds\n"
"s_waitcnt lgkmcnt(4)\n"
"v_mfma_f32_32x32x16_bf16 v[192:207], v[96:99], a[16:19], v[192:207]\n"
"ds_read_b64_tr_b16 v[80:81], v26 offset:32768\n"
"ds_read_b64_tr_b16 v[82:83], v26 offset:33024\n"
"v_mfma_f32_32x32x16_bf16 v[208:223], v[100:103], a[16:19], v[208:223]\n"
"s_add_u32 m0, s81, 0x0\n"
"ds_read_b64_tr_b16 v[84:85], v27 offset:32768\n"
"ds_read_b64_tr_b16 v[86:87], v27 offset:33024\n"
"buffer_load_dwordx4 v16, s[40:43], s69 offen lds\n"
"s_waitcnt lgkmcnt(4)\n"
"v_mfma_f32_32x32x16_bf16 v[224:239], v[104:107], a[16:19], v[224:239]\n"
"ds_read_b64_tr_b16 v[88:89], v20 offset:4096\n"
"ds_read_b64_tr_b16 v[90:91], v20 offset:4608\n"
"v_mfma_f32_32x32x16_bf16 v[240:255], v[108:111], a[16:19], v[240:255]\n"
"ds_read_b64_tr_b16 v[92:93], v21 offset:4096\n"
"ds_read_b64_tr_b16 v[94:95], v21 offset:4608\n"
"buffer_load_dwordx4 v17, s[40:43], s69 offen offset:1024 lds\n"
"s_waitcnt lgkmcnt(4)\n"
"v_mfma_f32_32x32x16_bf16 v[48:63], v[80:83], a[16:19], v[48:63]\n"
"ds_read_b64_tr_b16 v[96:97], v22 offset:4096\n"
"ds_read_b64_tr_b16 v[98:99], v22 offset:4608\n"
"v_mfma_f32_32x32x16_bf16 v[64:79], v[84:87], a[16:19], v[64:79]\n"
"ds_read_b64_tr_b16 v[100:101], v23 offset:4096\n"
"ds_read_b64_tr_b16 v[102:103], v23 offset:4608\n"
"ds_read_b64_tr_b16 v[104:105], v20 offset:20480\n"
"ds_read_b64_tr_b16 v[106:107], v20 offset:20992\n"
"s_waitcnt lgkmcnt(6)\n"
"v_mfma_f32_32x32x16_bf16 v[128:143], v[88:91], a[20:23], v[128:143]\n"
"ds_read_b64_tr_b16 v[108:109], v21 offset:20480\n"
"ds_read_b64_tr_b16 v[110:111], v21 offset:20992\n"
"v_mfma_f32_32x32x16_bf16 v[144:159], v[92:95], a[20:23], v[144:159]\n"
"ds_read_b64_tr_b16 v[80:81], v22 offset:20480\n"
"ds_read_b64_tr_b16 v[82:83], v22 offset:20992\n"
"buffer_load_dwordx4 v18, s[44:47], s69 offen offset:2048 lds\n"
"s_waitcnt lgkmcnt(6)\n"
"v_mfma_f32_32x32x16_bf16 v[160:175], v[96:99], a[20:23], v[160:175]\n"
"ds_read_b64_tr_b16 v[84:85], v23 offset:20480\n"
"ds_read_b64_tr_b16 v[86:87], v23 offset:20992\n"
"v_mfma_f32_32x32x16_bf16 v[176:191], v[100:103], a[20:23], v[176:191]\n"
"ds_read_b64_tr_b16 v[88:89], v26 offset:34816\n"
"ds_read_b64_tr_b16 v[90:91], v26 offset:35072\n"
"buffer_load_dwordx4 v19, s[44:47], s69 offen offset:3072 lds\n"
"s_waitcnt lgkmcnt(6)\n"
"v_mfma_f32_32x32x16_bf16 v[192:207], v[104:107], a[20:23], v[192:207]\n"
"ds_read_b64_tr_b16 v[92:93], v27 offset:34816\n"
"ds_read_b64_tr_b16 v[94:95], v27 offset:35072\n"
"v_mfma_f32_32x32x16_bf16 v[208:223], v[108:111], a[20:23], v[208:223]\n"
"ds_read_b64_tr_b16 v[96:97], v20 offset:8192\n"
"ds_read_b64_tr_b16 v[98:99], v20 offset:8704\n"
"s_waitcnt lgkmcnt(6)\n"
"v_mfma_f32_32x32x16_bf16 v[224:239], v[80:83], a[20:23], v[224:239]\n"
"ds_read_b64_tr_b16 v[100:101], v21 offset:8192\n"
"ds_read_b64_tr_b16 v[102:103], v21 offset:8704\n"
"v_mfma_f32_32x32x16_bf16 v[240:255], v[84:87], a[20:23], v[240:255]\n"
"s_add_u32 s79, s69, 128\n"
"s_add_u32 m0, s81, 0x4000\n"
"ds_read_b64_tr_b16 v[104:105], v22 offset:8192\n"
"ds_read_b64_tr_b16 v[106:107], v22 offset:8704\n"
"buffer_load_dwordx4 v16, s[40:43], s79 offen lds\n"
"s_waitcnt lgkmcnt(6)\n"
"v_mfma_f32_32x32x16_bf16 v[48:63], v[88:91], a[20:23], v[48:63]\n"
"ds_read_b64_tr_b16 v[108:109], v23 offset:8192\n"
"ds_read_b64_tr_b16 v[110:111], v23 offset:8704\n"
"v_mfma_f32_32x32x16_bf16 v[64:79], v[92:95], a[20:23], v[64:79]\n"
"ds_read_b64_tr_b16 v[80:81], v20 offset:24576\n"
"ds_read_b64_tr_b16 v[82:83], v20 offset:25088\n"
"s_waitcnt lgkmcnt(6)\n"
"v_mfma_f32_32x32x16_bf16 v[128:143], v[96:99], a[24:27], v[128:143]\n"
"ds_read_b64_tr_b16 v[84:85], v21 offset:24576\n"
"ds_read_b64_tr_b16 v[86:87], v21 offset:25088\n"
"v_mfma_f32_32x32x16_bf16 v[144:159], v[100:103], a[24:27], v[144:159]\n"
"ds_read_b64_tr_b16 v[88:89], v22 offset:24576\n"
"ds_read_b64_tr_b16 v[90:91], v22 offset:25088\n"
"ds_read_b64_tr_b16 v[92:93], v23 offset:24576\n"
"ds_read_b64_tr_b16 v[94:95], v23 offset:25088\n"
"buffer_load_dwordx4 v17, s[40:43], s79 offen offset:1024 lds\n"
"s_waitcnt lgkmcnt(8)\n"
"v_mfma_f32_32x32x16_bf16 v[160:175], v[104:107], a[24:27], v[160:175]\n"
"ds_read_b64_tr_b16 v[96:97], v26 offset:36864\n"
"ds_read_b64_tr_b16 v[98:99], v26 offset:37120\n"
"v_mfma_f32_32x32x16_bf16 v[176:191], v[108:111], a[24:27], v[176:191]\n"
"ds_read_b64_tr_b16 v[100:101], v27 offset:36864\n"
"ds_read_b64_tr_b16 v[102:103], v27 offset:37120\n"
"buffer_load_dwordx4 v18, s[44:47], s79 offen offset:2048 lds\n"
"s_waitcnt lgkmcnt(8)\n"
"v_mfma_f32_32x32x16_bf16 v[192:207], v[80:83], a[24:27], v[192:207]\n"
"ds_read_b64_tr_b16 v[104:105], v20 offset:12288\n"
"ds_read_b64_tr_b16 v[106:107], v20 offset:12800\n"
"v_mfma_f32_32x32x16_bf16 v[208:223], v[84:87], a[24:27], v[208:223]\n"
"ds_read_b64_tr_b16 v[108:109], v21 offset:12288\n"
"ds_read_b64_tr_b16 v[110:111], v21 offset:12800\n"
"s_waitcnt lgkmcnt(8)\n"
"v_mfma_f32_32x32x16_bf16 v[224:239], v[88:91], a[24:27], v[224:239]\n"
"ds_read_b64_tr_b16 v[80:81], v22 offset:12288\n"
"ds_read_b64_tr_b16 v[82:83], v22 offset:12800\n"
"v_mfma_f32_32x32x16_bf16 v[240:255], v[92:95], a[24:27], v[240:255]\n"
"ds_read_b64_tr_b16 v[84:85], v23 offset:12288\n"
"ds_read_b64_tr_b16 v[86:87], v23 offset:12800\n"
"buffer_load_dwordx4 v19, s[44:47], s79 offen offset:3072 lds\n"
"s_waitcnt lgkmcnt(8)\n"
"v_mfma_f32_32x32x16_bf16 v[48:63], v[96:99], a[24:27], v[48:63]\n"
"ds_read_b64_tr_b16 v[88:89], v20 offset:28672\n"
"ds_read_b64_tr_b16 v[90:91], v20 offset:29184\n"
"v_mfma_f32_32x32x16_bf16 v[64:79], v[100:103], a[24:27], v[64:79]\n"
"s_add_u32 s79, s69, s99\n"
"s_add_u32 m0, s80, 0x8000\n"
"ds_read_b64_tr_b16 v[92:93], v21 offset:28672\n"
"ds_read_b64_tr_b16 v[94:95], v21 offset:29184\n"
"buffer_load_dwordx4 v24, s[40:43], s79 offen lds\n"
"s_waitcnt lgkmcnt(8)\n"
"v_mfma_f32_32x32x16_bf16 v[128:143], v[104:107], a[28:31], v[128:143]\n"
"ds_read_b64_tr_b16 v[96:97], v22 offset:28672\n"
"ds_read_b64_tr_b16 v[98:99], v22 offset:29184\n"
"v_mfma_f32_32x32x16_bf16 v[144:159], v[108:111], a[28:31], v[144:159]\n"
"ds_read_b64_tr_b16 v[100:101], v23 offset:28672\n"
"ds_read_b64_tr_b16 v[102:103], v23 offset:29184\n"
"s_waitcnt lgkmcnt(8)\n"
"v_mfma_f32_32x32x16_bf16 v[160:175], v[80:83], a[28:31], v[160:175]\n"
"ds_read_b64_tr_b16 v[104:105], v26 offset:38912\n"
"ds_read_b64_tr_b16 v[106:107], v26 offset:39168\n"
"v_mfma_f32_32x32x16_bf16 v[176:191], v[84:87], a[28:31], v[176:191]\n"
"ds_read_b64_tr_b16 v[108:109], v27 offset:38912\n"
"ds_read_b64_tr_b16 v[110:111], v27 offset:39168\n"
"buffer_load_dwordx4 v25, s[44:47], s79 offen offset:1024 lds\n"
"s_waitcnt lgkmcnt(8)\n"
"v_mfma_f32_32x32x16_bf16 v[192:207], v[88:91], a[28:31], v[192:207]\n"
"v_add_u32 v20, 0xa000, v20\n"
"v_add_u32 v21, 0xa000, v21\n"
"v_add_u32 v22, 0xa000, v22\n"
"v_mfma_f32_32x32x16_bf16 v[208:223], v[92:95], a[28:31], v[208:223]\n"
"v_add_u32 v23, 0xa000, v23\n"
"v_add_u32 v26, 0xa000, v26\n"
"v_add_u32 v27, 0xa000, v27\n"
"s_waitcnt lgkmcnt(4)\n"
"v_mfma_f32_32x32x16_bf16 v[224:239], v[96:99], a[28:31], v[224:239]\n"
"s_waitcnt vmcnt(10) lgkmcnt(0)\n"
"s_barrier\n"
"ds_read_b128 a[32:35], v32 offset:0\n"
"ds_read_b128 a[36:39], v33 offset:0\n"
"ds_read_b64_tr_b16 v[80:81], v20 offset:0\n"
"ds_read_b64_tr_b16 v[82:83], v20 offset:512\n"
"v_mfma_f32_32x32x16_bf16 v[240:255], v[100:103], a[28:31], v[240:255]\n"
"ds_read_b128 a[40:43], v34 offset:0\n"
"ds_read_b128 a[44:47], v35 offset:0\n"
"ds_read_b64_tr_b16 v[84:85], v21 offset:0\n"
"ds_read_b64_tr_b16 v[86:87], v21 offset:512\n"
"v_mfma_f32_32x32x16_bf16 v[48:63], v[104:107], a[28:31], v[48:63]\n"
"ds_read_b64_tr_b16 v[88:89], v22 offset:0\n"
"ds_read_b64_tr_b16 v[90:91], v22 offset:512\n"
"ds_read_b64_tr_b16 a[160:161], v41 offset:0\n"
"v_mfma_f32_32x32x16_bf16 v[64:79], v[108:111], a[28:31], v[64:79]\n"
"ds_read_b64_tr_b16 v[92:93], v23 offset:0\n"
"ds_read_b64_tr_b16 v[94:95], v23 offset:512\n"
"ds_read_b64_tr_b16 a[162:163], v41 offset:2560\n"
"s_waitcnt lgkmcnt(6)\n"
"v_mfma_f32_32x32x16_bf16 v[128:143], v[80:83], a[32:35], v[128:143]\n"
"ds_read_b64_tr_b16 v[96:97], v20 offset:16384\n"
"ds_read_b64_tr_b16 v[98:99], v20 offset:16896\n"
"s_add_u32 m0, s81, 0x1e000\n"
"s_nop 0\n"
"buffer_load_dwordx4 v28, s[36:39], s59 offen lds\n"
"v_mfma_f32_32x32x16_bf16 v[144:159], v[84:87], a[32:35], v[144:159]\n"
"ds_read_b64_tr_b16 v[100:101], v21 offset:16384\n"
"ds_read_b64_tr_b16 v[102:103], v21 offset:16896\n"
"buffer_load_dwordx4 v29, s[36:39], s59 offen offset:1024 lds\n"
"s_waitcnt lgkmcnt(5)\n"
"v_mfma_f32_32x32x16_bf16 v[160:175], v[88:91], a[32:35], v[160:175]\n"
"ds_read_b64_tr_b16 v[104:105], v22 offset:16384\n"
"ds_read_b64_tr_b16 v[106:107], v22 offset:16896\n"
"buffer_load_dwordx4 v30, s[36:39], s59 offen offset:2048 lds\n"
"v_mfma_f32_32x32x16_bf16 v[176:191], v[92:95], a[32:35], v[176:191]\n"
"ds_read_b64_tr_b16 v[108:109], v23 offset:16384\n"
"ds_read_b64_tr_b16 v[110:111], v23 offset:16896\n"
"buffer_load_dwordx4 v31, s[36:39], s59 offen offset:3072 lds\n"
"s_waitcnt lgkmcnt(4)\n"
"v_mfma_f32_32x32x16_bf16 v[192:207], v[96:99], a[32:35], v[192:207]\n"
"ds_read_b64_tr_b16 v[80:81], v26 offset:32768\n"
"ds_read_b64_tr_b16 v[82:83], v26 offset:33024\n"
"ds_read_b64_tr_b16 a[164:165], v41 offset:32\n"
"v_mfma_f32_32x32x16_bf16 v[208:223], v[100:103], a[32:35], v[208:223]\n"
"s_add_u32 m0, s81, 0xa000\n"
"ds_read_b64_tr_b16 v[84:85], v27 offset:32768\n"
"ds_read_b64_tr_b16 v[86:87], v27 offset:33024\n"
"buffer_load_dwordx4 v16, s[40:43], s70 offen lds\n"
"s_waitcnt lgkmcnt(5)\n"
"v_mfma_f32_32x32x16_bf16 v[224:239], v[104:107], a[32:35], v[224:239]\n"
"ds_read_b64_tr_b16 v[88:89], v20 offset:4096\n"
"ds_read_b64_tr_b16 v[90:91], v20 offset:4608\n"
"ds_read_b64_tr_b16 a[166:167], v41 offset:2592\n"
"v_mfma_f32_32x32x16_bf16 v[240:255], v[108:111], a[32:35], v[240:255]\n"
"ds_read_b64_tr_b16 v[92:93], v21 offset:4096\n"
"ds_read_b64_tr_b16 v[94:95], v21 offset:4608\n"
"buffer_load_dwordx4 v17, s[40:43], s70 offen offset:1024 lds\n"
"s_waitcnt lgkmcnt(5)\n"
"v_mfma_f32_32x32x16_bf16 v[48:63], v[80:83], a[32:35], v[48:63]\n"
"ds_read_b64_tr_b16 v[96:97], v22 offset:4096\n"
"ds_read_b64_tr_b16 v[98:99], v22 offset:4608\n"
"ds_read_b64_tr_b16 a[168:169], v41 offset:64\n"
"v_mfma_f32_32x32x16_bf16 v[64:79], v[84:87], a[32:35], v[64:79]\n"
"ds_read_b64_tr_b16 v[100:101], v23 offset:4096\n"
"ds_read_b64_tr_b16 v[102:103], v23 offset:4608\n"
"ds_read_b64_tr_b16 v[104:105], v20 offset:20480\n"
"ds_read_b64_tr_b16 v[106:107], v20 offset:20992\n"
"s_waitcnt lgkmcnt(7)\n"
"v_mfma_f32_32x32x16_bf16 v[128:143], v[88:91], a[36:39], v[128:143]\n"
"ds_read_b64_tr_b16 v[108:109], v21 offset:20480\n"
"ds_read_b64_tr_b16 v[110:111], v21 offset:20992\n"
"ds_read_b64_tr_b16 a[170:171], v41 offset:2624\n"
"v_mfma_f32_32x32x16_bf16 v[144:159], v[92:95], a[36:39], v[144:159]\n"
"ds_read_b64_tr_b16 v[80:81], v22 offset:20480\n"
"ds_read_b64_tr_b16 v[82:83], v22 offset:20992\n"
"buffer_load_dwordx4 v18, s[44:47], s70 offen offset:2048 lds\n"
"s_waitcnt lgkmcnt(7)\n"
"v_mfma_f32_32x32x16_bf16 v[160:175], v[96:99], a[36:39], v[160:175]\n"
"ds_read_b64_tr_b16 v[84:85], v23 offset:20480\n"
"ds_read_b64_tr_b16 v[86:87], v23 offset:20992\n"
"ds_read_b64_tr_b16 a[172:173], v41 offset:96\n"
"v_mfma_f32_32x32x16_bf16 v[176:191], v[100:103], a[36:39], v[176:191]\n"
"ds_read_b64_tr_b16 v[88:89], v26 offset:34816\n"
"ds_read_b64_tr_b16 v[90:91], v26 offset:35072\n"
"buffer_load_dwordx4 v19, s[44:47], s70 offen offset:3072 lds\n"
"s_waitcnt lgkmcnt(8)\n"
"v_mfma_f32_32x32x16_bf16 v[192:207], v[104:107], a[36:39], v[192:207]\n"
"ds_read_b64_tr_b16 v[92:93], v27 offset:34816\n"
"ds_read_b64_tr_b16 v[94:95], v27 offset:35072\n"
"ds_read_b64_tr_b16 a[174:175], v41 offset:2656\n"
"v_mfma_f32_32x32x16_bf16 v[208:223], v[108:111], a[36:39], v[208:223]\n"
"ds_read_b64_tr_b16 v[96:97], v20 offset:8192\n"
"ds_read_b64_tr_b16 v[98:99], v20 offset:8704\n"
"ds_read_b64_tr_b16 a[176:177], v41 offset:128\n"
"s_waitcnt lgkmcnt(9)\n"
"v_mfma_f32_32x32x16_bf16 v[224:239], v[80:83], a[36:39], v[224:239]\n"
"ds_read_b64_tr_b16 v[100:101], v21 offset:8192\n"
"ds_read_b64_tr_b16 v[102:103], v21 offset:8704\n"
"ds_read_b64_tr_b16 a[178:179], v41 offset:2688\n"
"v_mfma_f32_32x32x16_bf16 v[240:255], v[84:87], a[36:39], v[240:255]\n"
"s_add_u32 s79, s70, 128\n"
"s_add_u32 m0, s81, 0xe000\n"
"ds_read_b64_tr_b16 v[104:105], v22 offset:8192\n"
"ds_read_b64_tr_b16 v[106:107], v22 offset:8704\n"
"buffer_load_dwordx4 v16, s[40:43], s79 offen lds\n"
"s_waitcnt lgkmcnt(9)\n"
"v_mfma_f32_32x32x16_bf16 v[48:63], v[88:91], a[36:39], v[48:63]\n"
"ds_read_b64_tr_b16 v[108:109], v23 offset:8192\n"
"ds_read_b64_tr_b16 v[110:111], v23 offset:8704\n"
"v_mfma_f32_32x32x16_bf16 v[64:79], v[92:95], a[36:39], v[64:79]\n"
"ds_read_b64_tr_b16 v[80:81], v20 offset:24576\n"
"ds_read_b64_tr_b16 v[82:83], v20 offset:25088\n"
"buffer_store_dwordx4 a[160:163], v6, s[48:51], s15 offen offset:0 sc1\n"
"s_waitcnt lgkmcnt(7)\n"
"v_mfma_f32_32x32x16_bf16 v[128:143], v[96:99], a[40:43], v[128:143]\n"
"ds_read_b64_tr_b16 v[84:85], v21 offset:24576\n"
"ds_read_b64_tr_b16 v[86:87], v21 offset:25088\n"
"v_mfma_f32_32x32x16_bf16 v[144:159], v[100:103], a[40:43], v[144:159]\n"
"ds_read_b64_tr_b16 v[88:89], v22 offset:24576\n"
"ds_read_b64_tr_b16 v[90:91], v22 offset:25088\n"
"ds_read_b64_tr_b16 v[92:93], v23 offset:24576\n"
"ds_read_b64_tr_b16 v[94:95], v23 offset:25088\n"
"buffer_load_dwordx4 v17, s[40:43], s79 offen offset:1024 lds\n"
"s_waitcnt lgkmcnt(8)\n"
"v_mfma_f32_32x32x16_bf16 v[160:175], v[104:107], a[40:43], v[160:175]\n"
"ds_read_b64_tr_b16 v[96:97], v26 offset:36864\n"
"ds_read_b64_tr_b16 v[98:99], v26 offset:37120\n"
"buffer_store_dwordx4 a[164:167], v6, s[48:51], s15 offen offset:1024 sc1\n"
"v_mfma_f32_32x32x16_bf16 v[176:191], v[108:111], a[40:43], v[176:191]\n"
"ds_read_b64_tr_b16 v[100:101], v27 offset:36864\n"
"ds_read_b64_tr_b16 v[102:103], v27 offset:37120\n"
"buffer_load_dwordx4 v18, s[44:47], s79 offen offset:2048 lds\n"
"s_waitcnt lgkmcnt(8)\n"
"v_mfma_f32_32x32x16_bf16 v[192:207], v[80:83], a[40:43], v[192:207]\n"
"ds_read_b64_tr_b16 v[104:105], v20 offset:12288\n"
"ds_read_b64_tr_b16 v[106:107], v20 offset:12800\n"
"v_mfma_f32_32x32x16_bf16 v[208:223], v[84:87], a[40:43], v[208:223]\n"
"ds_read_b64_tr_b16 v[108:109], v21 offset:12288\n"
"ds_read_b64_tr_b16 v[110:111], v21 offset:12800\n"
"buffer_store_dwordx4 a[168:171], v6, s[48:51], s15 offen offset:2048 sc1\n"
"s_waitcnt lgkmcnt(8)\n"
"v_mfma_f32_32x32x16_bf16 v[224:239], v[88:91], a[40:43], v[224:239]\n"
"ds_read_b64_tr_b16 v[80:81], v22 offset:12288\n"
"ds_read_b64_tr_b16 v[82:83], v22 offset:12800\n"
"v_mfma_f32_32x32x16_bf16 v[240:255], v[92:95], a[40:43], v[240:255]\n"
"ds_read_b64_tr_b16 v[84:85], v23 offset:12288\n"
"ds_read_b64_tr_b16 v[86:87], v23 offset:12800\n"
"buffer_load_dwordx4 v19, s[44:47], s79 offen offset:3072 lds\n"
"s_waitcnt lgkmcnt(8)\n"
"v_mfma_f32_32x32x16_bf16 v[48:63], v[96:99], a[40:43], v[48:63]\n"
"ds_read_b64_tr_b16 v[88:89], v20 offset:28672\n"
"ds_read_b64_tr_b16 v[90:91], v20 offset:29184\n"
"v_mfma_f32_32x32x16_bf16 v[64:79], v[100:103], a[40:43], v[64:79]\n"
"s_add_u32 s79, s70, s99\n"
"s_add_u32 m0, s80, 0x12000\n"
"ds_read_b64_tr_b16 v[92:93], v21 offset:28672\n"
"ds_read_b64_tr_b16 v[94:95], v21 offset:29184\n"
"buffer_load_dwordx4 v24, s[40:43], s79 offen lds\n"
"buffer_store_dwordx4 a[172:175], v6, s[48:51], s15 offen offset:3072 sc1\n"
"s_waitcnt lgkmcnt(8)\n"
"v_mfma_f32_32x32x16_bf16 v[128:143], v[104:107], a[44:47], v[128:143]\n"
"ds_read_b64_tr_b16 v[96:97], v22 offset:28672\n"
"ds_read_b64_tr_b16 v[98:99], v22 offset:29184\n"
"v_mfma_f32_32x32x16_bf16 v[144:159], v[108:111], a[44:47], v[144:159]\n"
"ds_read_b64_tr_b16 v[100:101], v23 offset:28672\n"
"ds_read_b64_tr_b16 v[102:103], v23 offset:29184\n"
"s_add_u32 s15, s15, 4096\n"
"s_waitcnt lgkmcnt(8)\n"
"v_mfma_f32_32x32x16_bf16 v[160:175], v[80:83], a[44:47], v[160:175]\n"
"ds_read_b64_tr_b16 v[104:105], v26 offset:38912\n"
"ds_read_b64_tr_b16 v[106:107], v26 offset:39168\n"
"v_mfma_f32_32x32x16_bf16 v[176:191], v[84:87], a[44:47], v[176:191]\n"
"ds_read_b64_tr_b16 v[108:109], v27 offset:38912\n"
"ds_read_b64_tr_b16 v[110:111], v27 offset:39168\n"
"buffer_load_dwordx4 v25, s[44:47], s79 offen offset:1024 lds\n"
"s_waitcnt lgkmcnt(8)\n"
"v_mfma_f32_32x32x16_bf16 v[192:207], v[88:91], a[44:47], v[192:207]\n"
"v_subrev_u32 v20, 0x14000, v20\n"
"v_subrev_u32 v21, 0x14000, v21\n"
"v_subrev_u32 v22, 0x14000, v22\n"
"buffer_store_dwordx4 a[176:179], v6, s[48:51], s15 offen sc1\n"
"v_mfma_f32_32x32x16_bf16 v[208:223], v[92:95], a[44:47], v[208:223]\n"
"v_subrev_u32 v23, 0x14000, v23\n"
"v_subrev_u32 v26, 0x14000, v26\n"
"v_subrev_u32 v27, 0x14000, v27\n"
"s_waitcnt lgkmcnt(4)\n"
"v_mfma_f32_32x32x16_bf16 v[224:239], v[96:99], a[44:47], v[224:239]\n"
"s_waitcnt vmcnt(15) lgkmcnt(0)\n"
"s_barrier\n"
"ds_read_b128 a[48:51], v32 offset:0\n"
"ds_read_b128 a[52:55], v33 offset:0\n"
"ds_read_b64_tr_b16 v[80:81], v20 offset:0\n"
"ds_read_b64_tr_b16 v[82:83], v20 offset:512\n"
"v_mfma_f32_32x32x16_bf16 v[240:255], v[100:103], a[44:47], v[240:255]\n"
"ds_read_b128 a[56:59], v34 offset:0\n"
"ds_read_b128 a[60:63], v35 offset:0\n"
"ds_read_b64_tr_b16 v[84:85], v21 offset:0\n"
"ds_read_b64_tr_b16 v[86:87], v21 offset:512\n"
"v_mfma_f32_32x32x16_bf16 v[48:63], v[104:107], a[44:47], v[48:63]\n"
"ds_read_b64_tr_b16 v[88:89], v22 offset:0\n"
"ds_read_b64_tr_b16 v[90:91], v22 offset:512\n"
"v_mfma_f32_32x32x16_bf16 v[64:79], v[108:111], a[44:47], v[64:79]\n"
"ds_read_b64_tr_b16 v[92:93], v23 offset:0\n"
"ds_read_b64_tr_b16 v[94:95], v23 offset:512\n"
"s_cmp_eq_u32 s3, 0x40000000\n"
"s_cbranch_scc1 L_pt1a\n"
"s_add_u32 m0, s93, 0x22000\n"
"s_nop 0\n"
"buffer_load_dwordx4 v36, s[4:7], s94 offen lds\n"
"s_add_u32 m0, s93, 0x22400\n"
"s_nop 0\n"
"buffer_load_dwordx4 v37, s[4:7], s94 offen lds\n"
"s_add_u32 m0, s93, 0x22800\n"
"s_nop 0\n"
"buffer_load_dwordx4 v43, s[4:7], s94 offen lds\n"
"s_add_u32 m0, s93, 0x22c00\n"
"s_nop 0\n"
"buffer_load_dwordx4 v39, s[4:7], s95 offen lds\n"
"s_add_u32 m0, s93, 0x23000\n"
"s_nop 0\n"
"buffer_load_dwordx4 v40, s[4:7], s95 offen lds\n"
"L_pt1a:\n"
"s_waitcnt lgkmcnt(4)\n"
"v_mfma_f32_32x32x16_bf16 v[128:143], v[80:83], a[48:51], v[128:143]\n"
"ds_read_b64_tr_b16 v[96:97], v20 offset:16384\n"
"ds_read_b64_tr_b16 v[98:99], v20 offset:16896\n"
"s_add_u32 m0, s81, 0x1e000\n"
"s_nop 0\n"
"buffer_load_dwordx4 v28, s[36:39], s60 offen lds\n"
"v_mfma_f32_32x32x16_bf16 v[144:159], v[84:87], a[48:51], v[144:159]\n"
"ds_read_b64_tr_b16 v[100:101], v21 offset:16384\n"
"ds_read_b64_tr_b16 v[102:103], v21 offset:16896\n"
"buffer_load_dwordx4 v29, s[36:39], s60 offen offset:1024 lds\n"
"s_waitcnt lgkmcnt(4)\n"
"v_mfma_f32_32x32x16_bf16 v[160:175], v[88:91], a[48:51], v[160:175]\n"
"ds_read_b64_tr_b16 v[104:105], v22 offset:16384\n"
"ds_read_b64_tr_b16 v[106:107], v22 offset:16896\n"
"buffer_load_dwordx4 v30, s[36:39], s60 offen offset:2048 lds\n"
"v_mfma_f32_32x32x16_bf16 v[176:191], v[92:95], a[48:51], v[176:191]\n"
"ds_read_b64_tr_b16 v[108:109], v23 offset:16384\n"
"ds_read_b64_tr_b16 v[110:111], v23 offset:16896\n"
"buffer_load_dwordx4 v31, s[36:39], s60 offen offset:3072 lds\n"
"s_waitcnt lgkmcnt(4)\n"
"v_mfma_f32_32x32x16_bf16 v[192:207], v[96:99], a[48:51], v[192:207]\n"
"ds_read_b64_tr_b16 v[80:81], v26 offset:32768\n"
"ds_read_b64_tr_b16 v[82:83], v26 offset:33024\n"
"v_mfma_f32_32x32x16_bf16 v[208:223], v[100:103], a[48:51], v[208:223]\n"
"s_add_u32 m0, s81, 0x14000\n"
"ds_read_b64_tr_b16 v[84:85], v27 offset:32768\n"
"ds_read_b64_tr_b16 v[86:87], v27 offset:33024\n"
"buffer_load_dwordx4 v16, s[40:43], s71 offen lds\n"
"s_waitcnt lgkmcnt(4)\n"
"v_mfma_f32_32x32x16_bf16 v[224:239], v[104:107], a[48:51], v[224:239]\n"
"ds_read_b64_tr_b16 v[88:89], v20 offset:4096\n"
"ds_read_b64_tr_b16 v[90:91], v20 offset:4608\n"
"v_mfma_f32_32x32x16_bf16 v[240:255], v[108:111], a[48:51], v[240:255]\n"
"ds_read_b64_tr_b16 v[92:93], v21 offset:4096\n"
"ds_read_b64_tr_b16 v[94:95], v21 offset:4608\n"
"buffer_load_dwordx4 v17, s[40:43], s71 offen offset:1024 lds\n"
"s_waitcnt lgkmcnt(4)\n"
"v_mfma_f32_32x32x16_bf16 v[48:63], v[80:83], a[48:51], v[48:63]\n"
"ds_read_b64_tr_b16 v[96:97], v22 offset:4096\n"
"ds_read_b64_tr_b16 v[98:99], v22 offset:4608\n"
"v_mfma_f32_32x32x16_bf16 v[64:79], v[84:87], a[48:51], v[64:79]\n"
"ds_read_b64_tr_b16 v[100:101], v23 offset:4096\n"
"ds_read_b64_tr_b16 v[102:103], v23 offset:4608\n"
"ds_read_b64_tr_b16 v[104:105], v20 offset:20480\n"
"ds_read_b64_tr_b16 v[106:107], v20 offset:20992\n"
"s_waitcnt lgkmcnt(6)\n"
"v_mfma_f32_32x32x16_bf16 v[128:143], v[88:91], a[52:55], v[128:143]\n"
"ds_read_b64_tr_b16 v[108:109], v21 offset:20480\n"
"ds_read_b64_tr_b16 v[110:111], v21 offset:20992\n"
"v_mfma_f32_32x32x16_bf16 v[144:159], v[92:95], a[52:55], v[144:159]\n"
"ds_read_b64_tr_b16 v[80:81], v22 offset:20480\n"
"ds_read_b64_tr_b16 v[82:83], v22 offset:20992\n"
"buffer_load_dwordx4 v18, s[44:47], s71 offen offset:2048 lds\n"
"s_waitcnt lgkmcnt(6)\n"
"v_mfma_f32_32x32x16_bf16 v[160:175], v[96:99], a[52:55], v[160:175]\n"
"ds_read_b64_tr_b16 v[84:85], v23 offset:20480\n"
"ds_read_b64_tr_b16 v[86:87], v23 offset:20992\n"
"v_mfma_f32_32x32x16_bf16 v[176:191], v[100:103], a[52:55], v[176:191]\n"
"ds_read_b64_tr_b16 v[88:89], v26 offset:34816\n"
"ds_read_b64_tr_b16 v[90:91], v26 offset:35072\n"
"buffer_load_dwordx4 v19, s[44:47], s71 offen offset:3072 lds\n"
"s_waitcnt lgkmcnt(6)\n"
"v_mfma_f32_32x32x16_bf16 v[192:207], v[104:107], a[52:55], v[192:207]\n"
"ds_read_b64_tr_b16 v[92:93], v27 offset:34816\n"
"ds_read_b64_tr_b16 v[94:95], v27 offset:35072\n"
"v_mfma_f32_32x32x16_bf16 v[208:223], v[108:111], a[52:55], v[208:223]\n"
"ds_read_b64_tr_b16 v[96:97], v20 offset:8192\n"
"ds_read_b64_tr_b16 v[98:99], v20 offset:8704\n"
"s_waitcnt lgkmcnt(6)\n"
"v_mfma_f32_32x32x16_bf16 v[224:239], v[80:83], a[52:55], v[224:239]\n"
"ds_read_b64_tr_b16 v[100:101], v21 offset:8192\n"
"ds_read_b64_tr_b16 v[102:103], v21 offset:8704\n"
"v_mfma_f32_32x32x16_bf16 v[240:255], v[84:87], a[52:55], v[240:255]\n"
"s_add_u32 s79, s71, 128\n"
"s_add_u32 m0, s81, 0x18000\n"
"ds_read_b64_tr_b16 v[104:105], v22 offset:8192\n"
"ds_read_b64_tr_b16 v[106:107], v22 offset:8704\n"
"buffer_load_dwordx4 v16, s[40:43], s79 offen lds\n"
"s_waitcnt lgkmcnt(6)\n"
"v_mfma_f32_32x32x16_bf16 v[48:63], v[88:91], a[52:55], v[48:63]\n"
"ds_read_b64_tr_b16 v[108:109], v23 offset:8192\n"
"ds_read_b64_tr_b16 v[110:111], v23 offset:8704\n"
"v_mfma_f32_32x32x16_bf16 v[64:79], v[92:95], a[52:55], v[64:79]\n"
"ds_read_b64_tr_b16 v[80:81], v20 offset:24576\n"
"ds_read_b64_tr_b16 v[82:83], v20 offset:25088\n"
"s_waitcnt lgkmcnt(6)\n"
"v_mfma_f32_32x32x16_bf16 v[128:143], v[96:99], a[56:59], v[128:143]\n"
"ds_read_b64_tr_b16 v[84:85], v21 offset:24576\n"
"ds_read_b64_tr_b16 v[86:87], v21 offset:25088\n"
"v_mfma_f32_32x32x16_bf16 v[144:159], v[100:103], a[56:59], v[144:159]\n"
"ds_read_b64_tr_b16 v[88:89], v22 offset:24576\n"
"ds_read_b64_tr_b16 v[90:91], v22 offset:25088\n"
"ds_read_b64_tr_b16 v[92:93], v23 offset:24576\n"
"ds_read_b64_tr_b16 v[94:95], v23 offset:25088\n"
"buffer_load_dwordx4 v17, s[40:43], s79 offen offset:1024 lds\n"
"s_waitcnt lgkmcnt(8)\n"
"v_mfma_f32_32x32x16_bf16 v[160:175], v[104:107], a[56:59], v[160:175]\n"
"ds_read_b64_tr_b16 v[96:97], v26 offset:36864\n"
"ds_read_b64_tr_b16 v[98:99], v26 offset:37120\n"
"v_mfma_f32_32x32x16_bf16 v[176:191], v[108:111], a[56:59], v[176:191]\n"
"ds_read_b64_tr_b16 v[100:101], v27 offset:36864\n"
"ds_read_b64_tr_b16 v[102:103], v27 offset:37120\n"
"buffer_load_dwordx4 v18, s[44:47], s79 offen offset:2048 lds\n"
"s_waitcnt lgkmcnt(8)\n"
"v_mfma_f32_32x32x16_bf16 v[192:207], v[80:83], a[56:59], v[192:207]\n"
"ds_read_b64_tr_b16 v[104:105], v20 offset:12288\n"
"ds_read_b64_tr_b16 v[106:107], v20 offset:12800\n"
"v_mfma_f32_32x32x16_bf16 v[208:223], v[84:87], a[56:59], v[208:223]\n"
"ds_read_b64_tr_b16 v[108:109], v21 offset:12288\n"
"ds_read_b64_tr_b16 v[110:111], v21 offset:12800\n"
"s_waitcnt lgkmcnt(8)\n"
"v_mfma_f32_32x32x16_bf16 v[224:239], v[88:91], a[56:59], v[224:239]\n"
"ds_read_b64_tr_b16 v[80:81], v22 offset:12288\n"
"ds_read_b64_tr_b16 v[82:83], v22 offset:12800\n"
"v_mfma_f32_32x32x16_bf16 v[240:255], v[92:95], a[56:59], v[240:255]\n"
"ds_read_b64_tr_b16 v[84:85], v23 offset:12288\n"
"ds_read_b64_tr_b16 v[86:87], v23 offset:12800\n"
"buffer_load_dwordx4 v19, s[44:47], s79 offen offset:3072 lds\n"
"s_waitcnt lgkmcnt(8)\n"
"v_mfma_f32_32x32x16_bf16 v[48:63], v[96:99], a[56:59], v[48:63]\n"
"ds_read_b64_tr_b16 v[88:89], v20 offset:28672\n"
"ds_read_b64_tr_b16 v[90:91], v20 offset:29184\n"
"v_mfma_f32_32x32x16_bf16 v[64:79], v[100:103], a[56:59], v[64:79]\n"
"s_add_u32 s79, s71, s99\n"
"s_add_u32 m0, s80, 0x1c000\n"
"ds_read_b64_tr_b16 v[92:93], v21 offset:28672\n"
"ds_read_b64_tr_b16 v[94:95], v21 offset:29184\n"
"buffer_load_dwordx4 v24, s[40:43], s79 offen lds\n"
"s_waitcnt lgkmcnt(8)\n"
"v_mfma_f32_32x32x16_bf16 v[128:143], v[104:107], a[60:63], v[128:143]\n"
"ds_read_b64_tr_b16 v[96:97], v22 offset:28672\n"
"ds_read_b64_tr_b16 v[98:99], v22 offset:29184\n"
"v_mfma_f32_32x32x16_bf16 v[144:159], v[108:111], a[60:63], v[144:159]\n"
"ds_read_b64_tr_b16 v[100:101], v23 offset:28672\n"
"ds_read_b64_tr_b16 v[102:103], v23 offset:29184\n"
"s_waitcnt lgkmcnt(8)\n"
"v_mfma_f32_32x32x16_bf16 v[160:175], v[80:83], a[60:63], v[160:175]\n"
"ds_read_b64_tr_b16 v[104:105], v26 offset:38912\n"
"ds_read_b64_tr_b16 v[106:107], v26 offset:39168\n"
"v_mfma_f32_32x32x16_bf16 v[176:191], v[84:87], a[60:63], v[176:191]\n"
"ds_read_b64_tr_b16 v[108:109], v27 offset:38912\n"
"ds_read_b64_tr_b16 v[110:111], v27 offset:39168\n"
"buffer_load_dwordx4 v25, s[44:47], s79 offen offset:1024 lds\n"
"s_waitcnt lgkmcnt(8)\n"
"v_mfma_f32_32x32x16_bf16 v[192:207], v[88:91], a[60:63], v[192:207]\n"
"v_add_u32 v20, 0xa000, v20\n"
"v_add_u32 v21, 0xa000, v21\n"
"v_add_u32 v22, 0xa000, v22\n"
"v_mfma_f32_32x32x16_bf16 v[208:223], v[92:95], a[60:63], v[208:223]\n"
"v_add_u32 v23, 0xa000, v23\n"
"v_add_u32 v26, 0xa000, v26\n"
"v_add_u32 v27, 0xa000, v27\n"
"s_waitcnt lgkmcnt(4)\n"
"v_mfma_f32_32x32x16_bf16 v[224:239], v[96:99], a[60:63], v[224:239]\n"
"s_waitcnt vmcnt(10) lgkmcnt(0)\n"
"s_barrier\n"
"ds_read_b128 a[64:67], v32 offset:0\n"
"ds_read_b128 a[68:71], v33 offset:0\n"
"ds_read_b64_tr_b16 v[80:81], v20 offset:0\n"
"ds_read_b64_tr_b16 v[82:83], v20 offset:512\n"
"v_mfma_f32_32x32x16_bf16 v[240:255], v[100:103], a[60:63], v[240:255]\n"
"ds_read_b128 a[72:75], v34 offset:0\n"
"ds_read_b128 a[76:79], v35 offset:0\n"
"ds_read_b64_tr_b16 v[84:85], v21 offset:0\n"
"ds_read_b64_tr_b16 v[86:87], v21 offset:512\n"
"buffer_store_dwordx2 v[2:3], v4, s[48:51], s14 offen offset:0 sc1\n"
"v_mfma_f32_32x32x16_bf16 v[48:63], v[104:107], a[60:63], v[48:63]\n"
"ds_read_b64_tr_b16 v[88:89], v22 offset:0\n"
"ds_read_b64_tr_b16 v[90:91], v22 offset:512\n"
"v_mfma_f32_32x32x16_bf16 v[64:79], v[108:111], a[60:63], v[64:79]\n"
"ds_read_b64_tr_b16 v[92:93], v23 offset:0\n"
"ds_read_b64_tr_b16 v[94:95], v23 offset:512\n"
"s_cmp_eq_u32 s3, 0x40000000\n"
"s_cbranch_scc1 L_pt1b\n"
"ds_read_b64_tr_b16 a[160:161], v41 offset:0\n"
"ds_read_b64_tr_b16 a[162:163], v41 offset:2560\n"
"ds_read_b64_tr_b16 a[164:165], v41 offset:32\n"
"ds_read_b64_tr_b16 a[166:167], v41 offset:2592\n"
"ds_read_b64_tr_b16 a[168:169], v41 offset:64\n"
"ds_read_b64_tr_b16 a[170:171], v41 offset:2624\n"
"ds_read_b64_tr_b16 a[172:173], v41 offset:96\n"
"ds_read_b64_tr_b16 a[174:175], v41 offset:2656\n"
"ds_read_b64_tr_b16 a[176:177], v41 offset:128\n"
"ds_read_b64_tr_b16 a[178:179], v41 offset:2688\n"
"s_waitcnt lgkmcnt(0)\n"
"buffer_store_dwordx4 a[160:163], v6, s[48:51], s12 offen offset:0 sc1\n"
"buffer_store_dwordx4 a[164:167], v6, s[48:51], s12 offen offset:1024 sc1\n"
"buffer_store_dwordx4 a[168:171], v6, s[48:51], s12 offen offset:2048 sc1\n"
"buffer_store_dwordx4 a[172:175], v6, s[48:51], s12 offen offset:3072 sc1\n"
"s_add_u32 s12, s12, 4096\n"
"buffer_store_dwordx4 a[176:179], v6, s[48:51], s12 offen sc1\n"
"L_pt1b:\n"
"s_waitcnt lgkmcnt(4)\n"
"v_mfma_f32_32x32x16_bf16 v[128:143], v[80:83], a[64:67], v[128:143]\n"
"ds_read_b64_tr_b16 v[96:97], v20 offset:16384\n"
"ds_read_b64_tr_b16 v[98:99], v20 offset:16896\n"
"s_add_u32 m0, s81, 0x1e000\n"
"s_nop 0\n"
"buffer_load_dwordx4 v28, s[36:39], s61 offen lds\n"
"v_mfma_f32_32x32x16_bf16 v[144:159], v[84:87], a[64:67], v[144:159]\n"
"ds_read_b64_tr_b16 v[100:101], v21 offset:16384\n"
"ds_read_b64_tr_b16 v[102:103], v21 offset:16896\n"
"buffer_load_dwordx4 v29, s[36:39], s61 offen offset:1024 lds\n"
"s_waitcnt lgkmcnt(4)\n"
"v_mfma_f32_32x32x16_bf16 v[160:175], v[88:91], a[64:67], v[160:175]\n"
"ds_read_b64_tr_b16 v[104:105], v22 offset:16384\n"
"ds_read_b64_tr_b16 v[106:107], v22 offset:16896\n"
"buffer_load_dwordx4 v30, s[36:39], s61 offen offset:2048 lds\n"
"v_mfma_f32_32x32x16_bf16 v[176:191], v[92:95], a[64:67], v[176:191]\n"
"ds_read_b64_tr_b16 v[108:109], v23 offset:16384\n"
"ds_read_b64_tr_b16 v[110:111], v23 offset:16896\n"
"buffer_load_dwordx4 v31, s[36:39], s61 offen offset:3072 lds\n"
"s_waitcnt lgkmcnt(4)\n"
"v_mfma_f32_32x32x16_bf16 v[192:207], v[96:99], a[64:67], v[192:207]\n"
"ds_read_b64_tr_b16 v[80:81], v26 offset:32768\n"
"ds_read_b64_tr_b16 v[82:83], v26 offset:33024\n"
"v_mfma_f32_32x32x16_bf16 v[208:223], v[100:103], a[64:67], v[208:223]\n"
"s_add_u32 m0, s81, 0x0\n"
"ds_read_b64_tr_b16 v[84:85], v27 offset:32768\n"
"ds_read_b64_tr_b16 v[86:87], v27 offset:33024\n"
"buffer_load_dwordx4 v16, s[40:43], s72 offen lds\n"
"s_waitcnt lgkmcnt(4)\n"
"v_mfma_f32_32x32x16_bf16 v[224:239], v[104:107], a[64:67], v[224:239]\n"
"ds_read_b64_tr_b16 v[88:89], v20 offset:4096\n"
"ds_read_b64_tr_b16 v[90:91], v20 offset:4608\n"
"v_mfma_f32_32x32x16_bf16 v[240:255], v[108:111], a[64:67], v[240:255]\n"
"ds_read_b64_tr_b16 v[92:93], v21 offset:4096\n"
"ds_read_b64_tr_b16 v[94:95], v21 offset:4608\n"
"buffer_load_dwordx4 v17, s[40:43], s72 offen offset:1024 lds\n"
"s_waitcnt lgkmcnt(4)\n"
"v_mfma_f32_32x32x16_bf16 v[48:63], v[80:83], a[64:67], v[48:63]\n"
"ds_read_b64_tr_b16 v[96:97], v22 offset:4096\n"
"ds_read_b64_tr_b16 v[98:99], v22 offset:4608\n"
"v_mfma_f32_32x32x16_bf16 v[64:79], v[84:87], a[64:67], v[64:79]\n"
"ds_read_b64_tr_b16 v[100:101], v23 offset:4096\n"
"ds_read_b64_tr_b16 v[102:103], v23 offset:4608\n"
"ds_read_b64_tr_b16 v[104:105], v20 offset:20480\n"
"ds_read_b64_tr_b16 v[106:107], v20 offset:20992\n"
"s_waitcnt lgkmcnt(6)\n"
"v_mfma_f32_32x32x16_bf16 v[128:143], v[88:91], a[68:71], v[128:143]\n"
"ds_read_b64_tr_b16 v[108:109], v21 offset:20480\n"
"ds_read_b64_tr_b16 v[110:111], v21 offset:20992\n"
"v_mfma_f32_32x32x16_bf16 v[144:159], v[92:95], a[68:71], v[144:159]\n"
"ds_read_b64_tr_b16 v[80:81], v22 offset:20480\n"
"ds_read_b64_tr_b16 v[82:83], v22 offset:20992\n"
"buffer_load_dwordx4 v18, s[44:47], s72 offen offset:2048 lds\n"
"s_waitcnt lgkmcnt(6)\n"
"v_mfma_f32_32x32x16_bf16 v[160:175], v[96:99], a[68:71], v[160:175]\n"
"ds_read_b64_tr_b16 v[84:85], v23 offset:20480\n"
"ds_read_b64_tr_b16 v[86:87], v23 offset:20992\n"
"v_mfma_f32_32x32x16_bf16 v[176:191], v[100:103], a[68:71], v[176:191]\n"
"ds_read_b64_tr_b16 v[88:89], v26 offset:34816\n"
"ds_read_b64_tr_b16 v[90:91], v26 offset:35072\n"
"buffer_load_dwordx4 v19, s[44:47], s72 offen offset:3072 lds\n"
"s_waitcnt lgkmcnt(6)\n"
"v_mfma_f32_32x32x16_bf16 v[192:207], v[104:107], a[68:71], v[192:207]\n"
"ds_read_b64_tr_b16 v[92:93], v27 offset:34816\n"
"ds_read_b64_tr_b16 v[94:95], v27 offset:35072\n"
"v_mfma_f32_32x32x16_bf16 v[208:223], v[108:111], a[68:71], v[208:223]\n"
"ds_read_b64_tr_b16 v[96:97], v20 offset:8192\n"
"ds_read_b64_tr_b16 v[98:99], v20 offset:8704\n"
"s_waitcnt lgkmcnt(6)\n"
"v_mfma_f32_32x32x16_bf16 v[224:239], v[80:83], a[68:71], v[224:239]\n"
"ds_read_b64_tr_b16 v[100:101], v21 offset:8192\n"
"ds_read_b64_tr_b16 v[102:103], v21 offset:8704\n"
"v_mfma_f32_32x32x16_bf16 v[240:255], v[84:87], a[68:71], v[240:255]\n"
"s_add_u32 s79, s72, 128\n"
"s_add_u32 m0, s81, 0x4000\n"
"ds_read_b64_tr_b16 v[104:105], v22 offset:8192\n"
"ds_read_b64_tr_b16 v[106:107], v22 offset:8704\n"
"buffer_load_dwordx4 v16, s[40:43], s79 offen lds\n"
"s_waitcnt lgkmcnt(6)\n"
"v_mfma_f32_32x32x16_bf16 v[48:63], v[88:91], a[68:71], v[48:63]\n"
"ds_read_b64_tr_b16 v[108:109], v23 offset:8192\n"
"ds_read_b64_tr_b16 v[110:111], v23 offset:8704\n"
"v_mfma_f32_32x32x16_bf16 v[64:79], v[92:95], a[68:71], v[64:79]\n"
"ds_read_b64_tr_b16 v[80:81], v20 offset:24576\n"
"ds_read_b64_tr_b16 v[82:83], v20 offset:25088\n"
"s_waitcnt lgkmcnt(6)\n"
"v_mfma_f32_32x32x16_bf16 v[128:143], v[96:99], a[72:75], v[128:143]\n"
"ds_read_b64_tr_b16 v[84:85], v21 offset:24576\n"
"ds_read_b64_tr_b16 v[86:87], v21 offset:25088\n"
"v_mfma_f32_32x32x16_bf16 v[144:159], v[100:103], a[72:75], v[144:159]\n"
"ds_read_b64_tr_b16 v[88:89], v22 offset:24576\n"
"ds_read_b64_tr_b16 v[90:91], v22 offset:25088\n"
"ds_read_b64_tr_b16 v[92:93], v23 offset:24576\n"
"ds_read_b64_tr_b16 v[94:95], v23 offset:25088\n"
"buffer_load_dwordx4 v17, s[40:43], s79 offen offset:1024 lds\n"
"s_waitcnt lgkmcnt(8)\n"
"v_mfma_f32_32x32x16_bf16 v[160:175], v[104:107], a[72:75], v[160:175]\n"
"ds_read_b64_tr_b16 v[96:97], v26 offset:36864\n"
"ds_read_b64_tr_b16 v[98:99], v26 offset:37120\n"
"v_mfma_f32_32x32x16_bf16 v[176:191], v[108:111], a[72:75], v[176:191]\n"
"ds_read_b64_tr_b16 v[100:101], v27 offset:36864\n"
"ds_read_b64_tr_b16 v[102:103], v27 offset:37120\n"
"buffer_load_dwordx4 v18, s[44:47], s79 offen offset:2048 lds\n"
"s_waitcnt lgkmcnt(8)\n"
"v_mfma_f32_32x32x16_bf16 v[192:207], v[80:83], a[72:75], v[192:207]\n"
"ds_read_b64_tr_b16 v[104:105], v20 offset:12288\n"
"ds_read_b64_tr_b16 v[106:107], v20 offset:12800\n"
"v_mfma_f32_32x32x16_bf16 v[208:223], v[84:87], a[72:75], v[208:223]\n"
"ds_read_b64_tr_b16 v[108:109], v21 offset:12288\n"
"ds_read_b64_tr_b16 v[110:111], v21 offset:12800\n"
"s_waitcnt lgkmcnt(8)\n"
"v_mfma_f32_32x32x16_bf16 v[224:239], v[88:91], a[72:75], v[224:239]\n"
"ds_read_b64_tr_b16 v[80:81], v22 offset:12288\n"
"ds_read_b64_tr_b16 v[82:83], v22 offset:12800\n"
"v_mfma_f32_32x32x16_bf16 v[240:255], v[92:95], a[72:75], v[240:255]\n"
"ds_read_b64_tr_b16 v[84:85], v23 offset:12288\n"
"ds_read_b64_tr_b16 v[86:87], v23 offset:12800\n"
"buffer_load_dwordx4 v19, s[44:47], s79 offen offset:3072 lds\n"
"s_waitcnt lgkmcnt(8)\n"
"v_mfma_f32_32x32x16_bf16 v[48:63], v[96:99], a[72:75], v[48:63]\n"
"ds_read_b64_tr_b16 v[88:89], v20 offset:28672\n"
"ds_read_b64_tr_b16 v[90:91], v20 offset:29184\n"
"v_mfma_f32_32x32x16_bf16 v[64:79], v[100:103], a[72:75], v[64:79]\n"
"s_add_u32 s79, s72, s99\n"
"s_add_u32 m0, s80, 0x8000\n"
"ds_read_b64_tr_b16 v[92:93], v21 offset:28672\n"
"ds_read_b64_tr_b16 v[94:95], v21 offset:29184\n"
"buffer_load_dwordx4 v24, s[40:43], s79 offen lds\n"
"s_waitcnt lgkmcnt(8)\n"
"v_mfma_f32_32x32x16_bf16 v[128:143], v[104:107], a[76:79], v[128:143]\n"
"ds_read_b64_tr_b16 v[96:97], v22 offset:28672\n"
"ds_read_b64_tr_b16 v[98:99], v22 offset:29184\n"
"v_mfma_f32_32x32x16_bf16 v[144:159], v[108:111], a[76:79], v[144:159]\n"
"ds_read_b64_tr_b16 v[100:101], v23 offset:28672\n"
"ds_read_b64_tr_b16 v[102:103], v23 offset:29184\n"
"s_waitcnt lgkmcnt(8)\n"
"v_mfma_f32_32x32x16_bf16 v[160:175], v[80:83], a[76:79], v[160:175]\n"
"ds_read_b64_tr_b16 v[104:105], v26 offset:38912\n"
"ds_read_b64_tr_b16 v[106:107], v26 offset:39168\n"
"v_mfma_f32_32x32x16_bf16 v[176:191], v[84:87], a[76:79], v[176:191]\n"
"ds_read_b64_tr_b16 v[108:109], v27 offset:38912\n"
"ds_read_b64_tr_b16 v[110:111], v27 offset:39168\n"
"buffer_load_dwordx4 v25, s[44:47], s79 offen offset:1024 lds\n"
"s_waitcnt lgkmcnt(8)\n"
"v_mfma_f32_32x32x16_bf16 v[192:207], v[88:91], a[76:79], v[192:207]\n"
"v_add_u32 v20, 0xa000, v20\n"
"v_add_u32 v21, 0xa000, v21\n"
"v_add_u32 v22, 0xa000, v22\n"
"v_mfma_f32_32x32x16_bf16 v[208:223], v[92:95], a[76:79], v[208:223]\n"
"v_add_u32 v23, 0xa000, v23\n"
"v_add_u32 v26, 0xa000, v26\n"
"v_add_u32 v27, 0xa000, v27\n"
"s_waitcnt lgkmcnt(4)\n"
"v_mfma_f32_32x32x16_bf16 v[224:239], v[96:99], a[76:79], v[224:239]\n"
"s_waitcnt vmcnt(10) lgkmcnt(0)\n"
"s_barrier\n"
"ds_read_b128 a[80:83], v32 offset:0\n"
"ds_read_b128 a[84:87], v33 offset:0\n"
"ds_read_b64_tr_b16 v[80:81], v20 offset:0\n"
"ds_read_b64_tr_b16 v[82:83], v20 offset:512\n"
"v_mfma_f32_32x32x16_bf16 v[240:255], v[100:103], a[76:79], v[240:255]\n"
"ds_read_b128 a[88:91], v34 offset:0\n"
"ds_read_b128 a[92:95], v35 offset:0\n"
"ds_read_b64_tr_b16 v[84:85], v21 offset:0\n"
"ds_read_b64_tr_b16 v[86:87], v21 offset:512\n"
"buffer_store_dwordx2 v[2:3], v4, s[48:51], s3 offen offset:0 sc1\n"
"v_mfma_f32_32x32x16_bf16 v[48:63], v[104:107], a[76:79], v[48:63]\n"
"ds_read_b64_tr_b16 v[88:89], v22 offset:0\n"
"ds_read_b64_tr_b16 v[90:91], v22 offset:512\n"
"v_mfma_f32_32x32x16_bf16 v[64:79], v[108:111], a[76:79], v[64:79]\n"
"ds_read_b64_tr_b16 v[92:93], v23 offset:0\n"
"ds_read_b64_tr_b16 v[94:95], v23 offset:512\n"
"s_waitcnt lgkmcnt(4)\n"
"v_mfma_f32_32x32x16_bf16 v[128:143], v[80:83], a[80:83], v[128:143]\n"
"ds_read_b64_tr_b16 v[96:97], v20 offset:16384\n"
"ds_read_b64_tr_b16 v[98:99], v20 offset:16896\n"
"s_add_u32 m0, s81, 0x1e000\n"
"s_nop 0\n"
"buffer_load_dwordx4 v28, s[36:39], s62 offen lds\n"
"v_mfma_f32_32x32x16_bf16 v[144:159], v[84:87], a[80:83], v[144:159]\n"
"ds_read_b64_tr_b16 v[100:101], v21 offset:16384\n"
"ds_read_b64_tr_b16 v[102:103], v21 offset:16896\n"
"buffer_load_dwordx4 v29, s[36:39], s62 offen offset:1024 lds\n"
"s_waitcnt lgkmcnt(4)\n"
"v_mfma_f32_32x32x16_bf16 v[160:175], v[88:91], a[80:83], v[160:175]\n"
"ds_read_b64_tr_b16 v[104:105], v22 offset:16384\n"
"ds_read_b64_tr_b16 v[106:107], v22 offset:16896\n"
"buffer_load_dwordx4 v30, s[36:39], s62 offen offset:2048 lds\n"
"v_mfma_f32_32x32x16_bf16 v[176:191], v[92:95], a[80:83], v[176:191]\n"
"ds_read_b64_tr_b16 v[108:109], v23 offset:16384\n"
"ds_read_b64_tr_b16 v[110:111], v23 offset:16896\n"
"buffer_load_dwordx4 v31, s[36:39], s62 offen offset:3072 lds\n"
"s_waitcnt lgkmcnt(4)\n"
"v_mfma_f32_32x32x16_bf16 v[192:207], v[96:99], a[80:83], v[192:207]\n"
"ds_read_b64_tr_b16 v[80:81], v26 offset:32768\n"
"ds_read_b64_tr_b16 v[82:83], v26 offset:33024\n"
"v_mfma_f32_32x32x16_bf16 v[208:223], v[100:103], a[80:83], v[208:223]\n"
"s_add_u32 m0, s81, 0xa000\n"
"ds_read_b64_tr_b16 v[84:85], v27 offset:32768\n"
"ds_read_b64_tr_b16 v[86:87], v27 offset:33024\n"
"buffer_load_dwordx4 v16, s[40:43], s73 offen lds\n"
"s_waitcnt lgkmcnt(4)\n"
"v_mfma_f32_32x32x16_bf16 v[224:239], v[104:107], a[80:83], v[224:239]\n"
"ds_read_b64_tr_b16 v[88:89], v20 offset:4096\n"
"ds_read_b64_tr_b16 v[90:91], v20 offset:4608\n"
"v_mfma_f32_32x32x16_bf16 v[240:255], v[108:111], a[80:83], v[240:255]\n"
"ds_read_b64_tr_b16 v[92:93], v21 offset:4096\n"
"ds_read_b64_tr_b16 v[94:95], v21 offset:4608\n"
"buffer_load_dwordx4 v17, s[40:43], s73 offen offset:1024 lds\n"
"s_waitcnt lgkmcnt(4)\n"
"v_mfma_f32_32x32x16_bf16 v[48:63], v[80:83], a[80:83], v[48:63]\n"
"ds_read_b64_tr_b16 v[96:97], v22 offset:4096\n"
"ds_read_b64_tr_b16 v[98:99], v22 offset:4608\n"
"v_mfma_f32_32x32x16_bf16 v[64:79], v[84:87], a[80:83], v[64:79]\n"
"ds_read_b64_tr_b16 v[100:101], v23 offset:4096\n"
"ds_read_b64_tr_b16 v[102:103], v23 offset:4608\n"
"ds_read_b64_tr_b16 v[104:105], v20 offset:20480\n"
"ds_read_b64_tr_b16 v[106:107], v20 offset:20992\n"
"s_waitcnt lgkmcnt(6)\n"
"v_mfma_f32_32x32x16_bf16 v[128:143], v[88:91], a[84:87], v[128:143]\n"
"ds_read_b64_tr_b16 v[108:109], v21 offset:20480\n"
"ds_read_b64_tr_b16 v[110:111], v21 offset:20992\n"
"v_mfma_f32_32x32x16_bf16 v[144:159], v[92:95], a[84:87], v[144:159]\n"
"ds_read_b64_tr_b16 v[80:81], v22 offset:20480\n"
"ds_read_b64_tr_b16 v[82:83], v22 offset:20992\n"
"buffer_load_dwordx4 v18, s[44:47], s73 offen offset:2048 lds\n"
"s_waitcnt lgkmcnt(6)\n"
"v_mfma_f32_32x32x16_bf16 v[160:175], v[96:99], a[84:87], v[160:175]\n"
"ds_read_b64_tr_b16 v[84:85], v23 offset:20480\n"
"ds_read_b64_tr_b16 v[86:87], v23 offset:20992\n"
"v_mfma_f32_32x32x16_bf16 v[176:191], v[100:103], a[84:87], v[176:191]\n"
"ds_read_b64_tr_b16 v[88:89], v26 offset:34816\n"
"ds_read_b64_tr_b16 v[90:91], v26 offset:35072\n"
"buffer_load_dwordx4 v19, s[44:47], s73 offen offset:3072 lds\n"
"s_waitcnt lgkmcnt(6)\n"
"v_mfma_f32_32x32x16_bf16 v[192:207], v[104:107], a[84:87], v[192:207]\n"
"ds_read_b64_tr_b16 v[92:93], v27 offset:34816\n"
"ds_read_b64_tr_b16 v[94:95], v27 offset:35072\n"
"v_mfma_f32_32x32x16_bf16 v[208:223], v[108:111], a[84:87], v[208:223]\n"
"ds_read_b64_tr_b16 v[96:97], v20 offset:8192\n"
"ds_read_b64_tr_b16 v[98:99], v20 offset:8704\n"
"s_waitcnt lgkmcnt(6)\n"
"v_mfma_f32_32x32x16_bf16 v[224:239], v[80:83], a[84:87], v[224:239]\n"
"ds_read_b64_tr_b16 v[100:101], v21 offset:8192\n"
"ds_read_b64_tr_b16 v[102:103], v21 offset:8704\n"
"v_mfma_f32_32x32x16_bf16 v[240:255], v[84:87], a[84:87], v[240:255]\n"
"s_add_u32 s79, s73, 128\n"
"s_add_u32 m0, s81, 0xe000\n"
"ds_read_b64_tr_b16 v[104:105], v22 offset:8192\n"
"ds_read_b64_tr_b16 v[106:107], v22 offset:8704\n"
"buffer_load_dwordx4 v16, s[40:43], s79 offen lds\n"
"s_waitcnt lgkmcnt(6)\n"
"v_mfma_f32_32x32x16_bf16 v[48:63], v[88:91], a[84:87], v[48:63]\n"
"ds_read_b64_tr_b16 v[108:109], v23 offset:8192\n"
"ds_read_b64_tr_b16 v[110:111], v23 offset:8704\n"
"v_mfma_f32_32x32x16_bf16 v[64:79], v[92:95], a[84:87], v[64:79]\n"
"ds_read_b64_tr_b16 v[80:81], v20 offset:24576\n"
"ds_read_b64_tr_b16 v[82:83], v20 offset:25088\n"
"s_waitcnt lgkmcnt(6)\n"
"v_mfma_f32_32x32x16_bf16 v[128:143], v[96:99], a[88:91], v[128:143]\n"
"ds_read_b64_tr_b16 v[84:85], v21 offset:24576\n"
"ds_read_b64_tr_b16 v[86:87], v21 offset:25088\n"
"v_mfma_f32_32x32x16_bf16 v[144:159], v[100:103], a[88:91], v[144:159]\n"
"ds_read_b64_tr_b16 v[88:89], v22 offset:24576\n"
"ds_read_b64_tr_b16 v[90:91], v22 offset:25088\n"
"ds_read_b64_tr_b16 v[92:93], v23 offset:24576\n"
"ds_read_b64_tr_b16 v[94:95], v23 offset:25088\n"
"buffer_load_dwordx4 v17, s[40:43], s79 offen offset:1024 lds\n"
"s_waitcnt lgkmcnt(8)\n"
"v_mfma_f32_32x32x16_bf16 v[160:175], v[104:107], a[88:91], v[160:175]\n"
"ds_read_b64_tr_b16 v[96:97], v26 offset:36864\n"
"ds_read_b64_tr_b16 v[98:99], v26 offset:37120\n"
"v_mfma_f32_32x32x16_bf16 v[176:191], v[108:111], a[88:91], v[176:191]\n"
"ds_read_b64_tr_b16 v[100:101], v27 offset:36864\n"
"ds_read_b64_tr_b16 v[102:103], v27 offset:37120\n"
"buffer_load_dwordx4 v18, s[44:47], s79 offen offset:2048 lds\n"
"s_waitcnt lgkmcnt(8)\n"
"v_mfma_f32_32x32x16_bf16 v[192:207], v[80:83], a[88:91], v[192:207]\n"
"ds_read_b64_tr_b16 v[104:105], v20 offset:12288\n"
"ds_read_b64_tr_b16 v[106:107], v20 offset:12800\n"
"v_mfma_f32_32x32x16_bf16 v[208:223], v[84:87], a[88:91], v[208:223]\n"
"ds_read_b64_tr_b16 v[108:109], v21 offset:12288\n"
"ds_read_b64_tr_b16 v[110:111], v21 offset:12800\n"
"s_waitcnt lgkmcnt(8)\n"
"v_mfma_f32_32x32x16_bf16 v[224:239], v[88:91], a[88:91], v[224:239]\n"
"ds_read_b64_tr_b16 v[80:81], v22 offset:12288\n"
"ds_read_b64_tr_b16 v[82:83], v22 offset:12800\n"
"v_mfma_f32_32x32x16_bf16 v[240:255], v[92:95], a[88:91], v[240:255]\n"
"ds_read_b64_tr_b16 v[84:85], v23 offset:12288\n"
"ds_read_b64_tr_b16 v[86:87], v23 offset:12800\n"
"buffer_load_dwordx4 v19, s[44:47], s79 offen offset:3072 lds\n"
"s_waitcnt lgkmcnt(8)\n"
"v_mfma_f32_32x32x16_bf16 v[48:63], v[96:99], a[88:91], v[48:63]\n"
"ds_read_b64_tr_b16 v[88:89], v20 offset:28672\n"
"ds_read_b64_tr_b16 v[90:91], v20 offset:29184\n"
"v_mfma_f32_32x32x16_bf16 v[64:79], v[100:103], a[88:91], v[64:79]\n"
"s_add_u32 s79, s73, s99\n"
"s_add_u32 m0, s80, 0x12000\n"
"ds_read_b64_tr_b16 v[92:93], v21 offset:28672\n"
"ds_read_b64_tr_b16 v[94:95], v21 offset:29184\n"
"buffer_load_dwordx4 v24, s[40:43], s79 offen lds\n"
"s_waitcnt lgkmcnt(8)\n"
"v_mfma_f32_32x32x16_bf16 v[128:143], v[104:107], a[92:95], v[128:143]\n"
"ds_read_b64_tr_b16 v[96:97], v22 offset:28672\n"
"ds_read_b64_tr_b16 v[98:99], v22 offset:29184\n"
"v_mfma_f32_32x32x16_bf16 v[144:159], v[108:111], a[92:95], v[144:159]\n"
"ds_read_b64_tr_b16 v[100:101], v23 offset:28672\n"
"ds_read_b64_tr_b16 v[102:103], v23 offset:29184\n"
"s_waitcnt lgkmcnt(8)\n"
"v_mfma_f32_32x32x16_bf16 v[160:175], v[80:83], a[92:95], v[160:175]\n"
"ds_read_b64_tr_b16 v[104:105], v26 offset:38912\n"
"ds_read_b64_tr_b16 v[106:107], v26 offset:39168\n"
"v_mfma_f32_32x32x16_bf16 v[176:191], v[84:87], a[92:95], v[176:191]\n"
"ds_read_b64_tr_b16 v[108:109], v27 offset:38912\n"
"ds_read_b64_tr_b16 v[110:111], v27 offset:39168\n"
"buffer_load_dwordx4 v25, s[44:47], s79 offen offset:1024 lds\n"
"s_waitcnt lgkmcnt(8)\n"
"v_mfma_f32_32x32x16_bf16 v[192:207], v[88:91], a[92:95], v[192:207]\n"
"v_subrev_u32 v20, 0x14000, v20\n"
"v_subrev_u32 v21, 0x14000, v21\n"
"v_subrev_u32 v22, 0x14000, v22\n"
"v_mfma_f32_32x32x16_bf16 v[208:223], v[92:95], a[92:95], v[208:223]\n"
"v_subrev_u32 v23, 0x14000, v23\n"
"v_subrev_u32 v26, 0x14000, v26\n"
"v_subrev_u32 v27, 0x14000, v27\n"
"s_waitcnt lgkmcnt(4)\n"
"v_mfma_f32_32x32x16_bf16 v[224:239], v[96:99], a[92:95], v[224:239]\n"
"s_waitcnt vmcnt(10) lgkmcnt(0)\n"
"s_barrier\n"
"ds_read_b128 a[96:99], v32 offset:0\n"
"ds_read_b128 a[100:103], v33 offset:0\n"
"ds_read_b64_tr_b16 v[80:81], v20 offset:0\n"
"ds_read_b64_tr_b16 v[82:83], v20 offset:512\n"
"v_mfma_f32_32x32x16_bf16 v[240:255], v[100:103], a[92:95], v[240:255]\n"
"ds_read_b128 a[104:107], v34 offset:0\n"
"ds_read_b128 a[108:111], v35 offset:0\n"
"ds_read_b64_tr_b16 v[84:85], v21 offset:0\n"
"ds_read_b64_tr_b16 v[86:87], v21 offset:512\n"
"v_mfma_f32_32x32x16_bf16 v[48:63], v[104:107], a[92:95], v[48:63]\n"
"ds_read_b64_tr_b16 v[88:89], v22 offset:0\n"
"ds_read_b64_tr_b16 v[90:91], v22 offset:512\n"
"v_mfma_f32_32x32x16_bf16 v[64:79], v[108:111], a[92:95], v[64:79]\n"
"ds_read_b64_tr_b16 v[92:93], v23 offset:0\n"
"ds_read_b64_tr_b16 v[94:95], v23 offset:512\n"
"s_waitcnt lgkmcnt(4)\n"
"v_mfma_f32_32x32x16_bf16 v[128:143], v[80:83], a[96:99], v[128:143]\n"
"ds_read_b64_tr_b16 v[96:97], v20 offset:16384\n"
"ds_read_b64_tr_b16 v[98:99], v20 offset:16896\n"
"s_add_u32 m0, s81, 0x1e000\n"
"s_nop 0\n"
"buffer_load_dwordx4 v28, s[36:39], s63 offen lds\n"
"v_mfma_f32_32x32x16_bf16 v[144:159], v[84:87], a[96:99], v[144:159]\n"
"ds_read_b64_tr_b16 v[100:101], v21 offset:16384\n"
"ds_read_b64_tr_b16 v[102:103], v21 offset:16896\n"
"buffer_load_dwordx4 v29, s[36:39], s63 offen offset:1024 lds\n"
"s_waitcnt lgkmcnt(4)\n"
"v_mfma_f32_32x32x16_bf16 v[160:175], v[88:91], a[96:99], v[160:175]\n"
"ds_read_b64_tr_b16 v[104:105], v22 offset:16384\n"
"ds_read_b64_tr_b16 v[106:107], v22 offset:16896\n"
"buffer_load_dwordx4 v30, s[36:39], s63 offen offset:2048 lds\n"
"v_mfma_f32_32x32x16_bf16 v[176:191], v[92:95], a[96:99], v[176:191]\n"
"ds_read_b64_tr_b16 v[108:109], v23 offset:16384\n"
"ds_read_b64_tr_b16 v[110:111], v23 offset:16896\n"
"buffer_load_dwordx4 v31, s[36:39], s63 offen offset:3072 lds\n"
"s_waitcnt lgkmcnt(4)\n"
"v_mfma_f32_32x32x16_bf16 v[192:207], v[96:99], a[96:99], v[192:207]\n"
"ds_read_b64_tr_b16 v[80:81], v26 offset:32768\n"
"ds_read_b64_tr_b16 v[82:83], v26 offset:33024\n"
"v_mfma_f32_32x32x16_bf16 v[208:223], v[100:103], a[96:99], v[208:223]\n"
"s_add_u32 m0, s81, 0x14000\n"
"ds_read_b64_tr_b16 v[84:85], v27 offset:32768\n"
"ds_read_b64_tr_b16 v[86:87], v27 offset:33024\n"
"buffer_load_dwordx4 v16, s[40:43], s74 offen lds\n"
"s_waitcnt lgkmcnt(4)\n"
"v_mfma_f32_32x32x16_bf16 v[224:239], v[104:107], a[96:99], v[224:239]\n"
"ds_read_b64_tr_b16 v[88:89], v20 offset:4096\n"
"ds_read_b64_tr_b16 v[90:91], v20 offset:4608\n"
"v_mfma_f32_32x32x16_bf16 v[240:255], v[108:111], a[96:99], v[240:255]\n"
"ds_read_b64_tr_b16 v[92:93], v21 offset:4096\n"
"ds_read_b64_tr_b16 v[94:95], v21 offset:4608\n"
"buffer_load_dwordx4 v17, s[40:43], s74 offen offset:1024 lds\n"
"s_waitcnt lgkmcnt(4)\n"
"v_mfma_f32_32x32x16_bf16 v[48:63], v[80:83], a[96:99], v[48:63]\n"
"ds_read_b64_tr_b16 v[96:97], v22 offset:4096\n"
"ds_read_b64_tr_b16 v[98:99], v22 offset:4608\n"
"v_mfma_f32_32x32x16_bf16 v[64:79], v[84:87], a[96:99], v[64:79]\n"
"ds_read_b64_tr_b16 v[100:101], v23 offset:4096\n"
"ds_read_b64_tr_b16 v[102:103], v23 offset:4608\n"
"ds_read_b64_tr_b16 v[104:105], v20 offset:20480\n"
"ds_read_b64_tr_b16 v[106:107], v20 offset:20992\n"
"s_waitcnt lgkmcnt(6)\n"
"v_mfma_f32_32x32x16_bf16 v[128:143], v[88:91], a[100:103], v[128:143]\n"
"ds_read_b64_tr_b16 v[108:109], v21 offset:20480\n"
"ds_read_b64_tr_b16 v[110:111], v21 offset:20992\n"
"v_mfma_f32_32x32x16_bf16 v[144:159], v[92:95], a[100:103], v[144:159]\n"
"ds_read_b64_tr_b16 v[80:81], v22 offset:20480\n"
"ds_read_b64_tr_b16 v[82:83], v22 offset:20992\n"
"buffer_load_dwordx4 v18, s[44:47], s74 offen offset:2048 lds\n"
"s_waitcnt lgkmcnt(6)\n"
"v_mfma_f32_32x32x16_bf16 v[160:175], v[96:99], a[100:103], v[160:175]\n"
"ds_read_b64_tr_b16 v[84:85], v23 offset:20480\n"
"ds_read_b64_tr_b16 v[86:87], v23 offset:20992\n"
"v_mfma_f32_32x32x16_bf16 v[176:191], v[100:103], a[100:103], v[176:191]\n"
"ds_read_b64_tr_b16 v[88:89], v26 offset:34816\n"
"ds_read_b64_tr_b16 v[90:91], v26 offset:35072\n"
"buffer_load_dwordx4 v19, s[44:47], s74 offen offset:3072 lds\n"
"s_waitcnt lgkmcnt(6)\n"
"v_mfma_f32_32x32x16_bf16 v[192:207], v[104:107], a[100:103], v[192:207]\n"
"ds_read_b64_tr_b16 v[92:93], v27 offset:34816\n"
"ds_read_b64_tr_b16 v[94:95], v27 offset:35072\n"
"v_mfma_f32_32x32x16_bf16 v[208:223], v[108:111], a[100:103], v[208:223]\n"
"ds_read_b64_tr_b16 v[96:97], v20 offset:8192\n"
"ds_read_b64_tr_b16 v[98:99], v20 offset:8704\n"
"s_waitcnt lgkmcnt(6)\n"
"v_mfma_f32_32x32x16_bf16 v[224:239], v[80:83], a[100:103], v[224:239]\n"
"ds_read_b64_tr_b16 v[100:101], v21 offset:8192\n"
"ds_read_b64_tr_b16 v[102:103], v21 offset:8704\n"
"v_mfma_f32_32x32x16_bf16 v[240:255], v[84:87], a[100:103], v[240:255]\n"
"s_add_u32 s79, s74, 128\n"
"s_add_u32 m0, s81, 0x18000\n"
"ds_read_b64_tr_b16 v[104:105], v22 offset:8192\n"
"ds_read_b64_tr_b16 v[106:107], v22 offset:8704\n"
"buffer_load_dwordx4 v16, s[40:43], s79 offen lds\n"
"s_waitcnt lgkmcnt(6)\n"
"v_mfma_f32_32x32x16_bf16 v[48:63], v[88:91], a[100:103], v[48:63]\n"
"ds_read_b64_tr_b16 v[108:109], v23 offset:8192\n"
"ds_read_b64_tr_b16 v[110:111], v23 offset:8704\n"
"v_mfma_f32_32x32x16_bf16 v[64:79], v[92:95], a[100:103], v[64:79]\n"
"ds_read_b64_tr_b16 v[80:81], v20 offset:24576\n"
"ds_read_b64_tr_b16 v[82:83], v20 offset:25088\n"
"s_waitcnt lgkmcnt(6)\n"
"v_mfma_f32_32x32x16_bf16 v[128:143], v[96:99], a[104:107], v[128:143]\n"
"ds_read_b64_tr_b16 v[84:85], v21 offset:24576\n"
"ds_read_b64_tr_b16 v[86:87], v21 offset:25088\n"
"v_mfma_f32_32x32x16_bf16 v[144:159], v[100:103], a[104:107], v[144:159]\n"
"ds_read_b64_tr_b16 v[88:89], v22 offset:24576\n"
"ds_read_b64_tr_b16 v[90:91], v22 offset:25088\n"
"ds_read_b64_tr_b16 v[92:93], v23 offset:24576\n"
"ds_read_b64_tr_b16 v[94:95], v23 offset:25088\n"
"buffer_load_dwordx4 v17, s[40:43], s79 offen offset:1024 lds\n"
"s_waitcnt lgkmcnt(8)\n"
"v_mfma_f32_32x32x16_bf16 v[160:175], v[104:107], a[104:107], v[160:175]\n"
"ds_read_b64_tr_b16 v[96:97], v26 offset:36864\n"
"ds_read_b64_tr_b16 v[98:99], v26 offset:37120\n"
"v_mfma_f32_32x32x16_bf16 v[176:191], v[108:111], a[104:107], v[176:191]\n"
"ds_read_b64_tr_b16 v[100:101], v27 offset:36864\n"
"ds_read_b64_tr_b16 v[102:103], v27 offset:37120\n"
"buffer_load_dwordx4 v18, s[44:47], s79 offen offset:2048 lds\n"
"s_waitcnt lgkmcnt(8)\n"
"v_mfma_f32_32x32x16_bf16 v[192:207], v[80:83], a[104:107], v[192:207]\n"
"ds_read_b64_tr_b16 v[104:105], v20 offset:12288\n"
"ds_read_b64_tr_b16 v[106:107], v20 offset:12800\n"
"v_mfma_f32_32x32x16_bf16 v[208:223], v[84:87], a[104:107], v[208:223]\n"
"ds_read_b64_tr_b16 v[108:109], v21 offset:12288\n"
"ds_read_b64_tr_b16 v[110:111], v21 offset:12800\n"
"s_waitcnt lgkmcnt(8)\n"
"v_mfma_f32_32x32x16_bf16 v[224:239], v[88:91], a[104:107], v[224:239]\n"
"ds_read_b64_tr_b16 v[80:81], v22 offset:12288\n"
"ds_read_b64_tr_b16 v[82:83], v22 offset:12800\n"
"v_mfma_f32_32x32x16_bf16 v[240:255], v[92:95], a[104:107], v[240:255]\n"
"ds_read_b64_tr_b16 v[84:85], v23 offset:12288\n"
"ds_read_b64_tr_b16 v[86:87], v23 offset:12800\n"
"buffer_load_dwordx4 v19, s[44:47], s79 offen offset:3072 lds\n"
"s_waitcnt lgkmcnt(8)\n"
"v_mfma_f32_32x32x16_bf16 v[48:63], v[96:99], a[104:107], v[48:63]\n"
"ds_read_b64_tr_b16 v[88:89], v20 offset:28672\n"
"ds_read_b64_tr_b16 v[90:91], v20 offset:29184\n"
"v_mfma_f32_32x32x16_bf16 v[64:79], v[100:103], a[104:107], v[64:79]\n"
"s_add_u32 s79, s74, s99\n"
"s_add_u32 m0, s80, 0x1c000\n"
"ds_read_b64_tr_b16 v[92:93], v21 offset:28672\n"
"ds_read_b64_tr_b16 v[94:95], v21 offset:29184\n"
"buffer_load_dwordx4 v24, s[40:43], s79 offen lds\n"
"s_waitcnt lgkmcnt(8)\n"
"v_mfma_f32_32x32x16_bf16 v[128:143], v[104:107], a[108:111], v[128:143]\n"
"ds_read_b64_tr_b16 v[96:97], v22 offset:28672\n"
"ds_read_b64_tr_b16 v[98:99], v22 offset:29184\n"
"v_mfma_f32_32x32x16_bf16 v[144:159], v[108:111], a[108:111], v[144:159]\n"
"ds_read_b64_tr_b16 v[100:101], v23 offset:28672\n"
"ds_read_b64_tr_b16 v[102:103], v23 offset:29184\n"
"s_waitcnt lgkmcnt(8)\n"
"v_mfma_f32_32x32x16_bf16 v[160:175], v[80:83], a[108:111], v[160:175]\n"
"ds_read_b64_tr_b16 v[104:105], v26 offset:38912\n"
"ds_read_b64_tr_b16 v[106:107], v26 offset:39168\n"
"v_mfma_f32_32x32x16_bf16 v[176:191], v[84:87], a[108:111], v[176:191]\n"
"ds_read_b64_tr_b16 v[108:109], v27 offset:38912\n"
"ds_read_b64_tr_b16 v[110:111], v27 offset:39168\n"
"buffer_load_dwordx4 v25, s[44:47], s79 offen offset:1024 lds\n"
"s_waitcnt lgkmcnt(8)\n"
"v_mfma_f32_32x32x16_bf16 v[192:207], v[88:91], a[108:111], v[192:207]\n"
"v_add_u32 v20, 0xa000, v20\n"
"v_add_u32 v21, 0xa000, v21\n"
"v_add_u32 v22, 0xa000, v22\n"
"v_mfma_f32_32x32x16_bf16 v[208:223], v[92:95], a[108:111], v[208:223]\n"
"v_add_u32 v23, 0xa000, v23\n"
"v_add_u32 v26, 0xa000, v26\n"
"v_add_u32 v27, 0xa000, v27\n"
"s_waitcnt lgkmcnt(4)\n"
"v_mfma_f32_32x32x16_bf16 v[224:239], v[96:99], a[108:111], v[224:239]\n"
"s_waitcnt vmcnt(10) lgkmcnt(0)\n"
"s_barrier\n"
"ds_read_b128 a[112:115], v32 offset:0\n"
"ds_read_b128 a[116:119], v33 offset:0\n"
"ds_read_b64_tr_b16 v[80:81], v20 offset:0\n"
"ds_read_b64_tr_b16 v[82:83], v20 offset:512\n"
"v_mfma_f32_32x32x16_bf16 v[240:255], v[100:103], a[108:111], v[240:255]\n"
"ds_read_b128 a[120:123], v34 offset:0\n"
"ds_read_b128 a[124:127], v35 offset:0\n"
"ds_read_b64_tr_b16 v[84:85], v21 offset:0\n"
"ds_read_b64_tr_b16 v[86:87], v21 offset:512\n"
"v_mfma_f32_32x32x16_bf16 v[48:63], v[104:107], a[108:111], v[48:63]\n"
"ds_read_b64_tr_b16 v[88:89], v22 offset:0\n"
"ds_read_b64_tr_b16 v[90:91], v22 offset:512\n"
"v_mfma_f32_32x32x16_bf16 v[64:79], v[108:111], a[108:111], v[64:79]\n"
"ds_read_b64_tr_b16 v[92:93], v23 offset:0\n"
"ds_read_b64_tr_b16 v[94:95], v23 offset:512\n"
"s_waitcnt lgkmcnt(4)\n"
"v_mfma_f32_32x32x16_bf16 v[128:143], v[80:83], a[112:115], v[128:143]\n"
"ds_read_b64_tr_b16 v[96:97], v20 offset:16384\n"
"ds_read_b64_tr_b16 v[98:99], v20 offset:16896\n"
"s_add_u32 m0, s81, 0x1e000\n"
"s_nop 0\n"
"buffer_load_dwordx4 v28, s[36:39], s64 offen lds\n"
"v_mfma_f32_32x32x16_bf16 v[144:159], v[84:87], a[112:115], v[144:159]\n"
"ds_read_b64_tr_b16 v[100:101], v21 offset:16384\n"
"ds_read_b64_tr_b16 v[102:103], v21 offset:16896\n"
"buffer_load_dwordx4 v29, s[36:39], s64 offen offset:1024 lds\n"
"s_waitcnt lgkmcnt(4)\n"
"v_mfma_f32_32x32x16_bf16 v[160:175], v[88:91], a[112:115], v[160:175]\n"
"ds_read_b64_tr_b16 v[104:105], v22 offset:16384\n"
"ds_read_b64_tr_b16 v[106:107], v22 offset:16896\n"
"buffer_load_dwordx4 v30, s[36:39], s64 offen offset:2048 lds\n"
"v_mfma_f32_32x32x16_bf16 v[176:191], v[92:95], a[112:115], v[176:191]\n"
"ds_read_b64_tr_b16 v[108:109], v23 offset:16384\n"
"ds_read_b64_tr_b16 v[110:111], v23 offset:16896\n"
"buffer_load_dwordx4 v31, s[36:39], s64 offen offset:3072 lds\n"
"s_mul_i32 s82, s89, 378880\n"
"s_waitcnt lgkmcnt(4)\n"
"v_mfma_f32_32x32x16_bf16 v[192:207], v[96:99], a[112:115], v[192:207]\n"
"ds_read_b64_tr_b16 v[80:81], v26 offset:32768\n"
"ds_read_b64_tr_b16 v[82:83], v26 offset:33024\n"
"s_add_u32 s82, s82, 0x100000\n"
"v_mfma_f32_32x32x16_bf16 v[208:223], v[100:103], a[112:115], v[208:223]\n"
"s_add_u32 m0, s81, 0x0\n"
"ds_read_b64_tr_b16 v[84:85], v27 offset:32768\n"
"ds_read_b64_tr_b16 v[86:87], v27 offset:33024\n"
"buffer_load_dwordx4 v16, s[40:43], s75 offen lds\n"
"s_waitcnt lgkmcnt(4)\n"
"v_mfma_f32_32x32x16_bf16 v[224:239], v[104:107], a[112:115], v[224:239]\n"
"ds_read_b64_tr_b16 v[88:89], v20 offset:4096\n"
"ds_read_b64_tr_b16 v[90:91], v20 offset:4608\n"
"s_mul_i32 s83, s76, 61440\n"
"v_mfma_f32_32x32x16_bf16 v[240:255], v[108:111], a[112:115], v[240:255]\n"
"ds_read_b64_tr_b16 v[92:93], v21 offset:4096\n"
"ds_read_b64_tr_b16 v[94:95], v21 offset:4608\n"
"buffer_load_dwordx4 v17, s[40:43], s75 offen offset:1024 lds\n"
"s_add_u32 s4, s82, s83\n"
"s_waitcnt lgkmcnt(4)\n"
"v_mfma_f32_32x32x16_bf16 v[48:63], v[80:83], a[112:115], v[48:63]\n"
"ds_read_b64_tr_b16 v[96:97], v22 offset:4096\n"
"ds_read_b64_tr_b16 v[98:99], v22 offset:4608\n"
"v_mfma_f32_32x32x16_bf16 v[64:79], v[84:87], a[112:115], v[64:79]\n"
"ds_read_b64_tr_b16 v[100:101], v23 offset:4096\n"
"ds_read_b64_tr_b16 v[102:103], v23 offset:4608\n"
"ds_read_b64_tr_b16 v[104:105], v20 offset:20480\n"
"ds_read_b64_tr_b16 v[106:107], v20 offset:20992\n"
"s_bitcmp1_b32 s0, 1\n"
"s_cselect_b32 s83, 30720, 0\n"
"s_waitcnt lgkmcnt(6)\n"
"v_mfma_f32_32x32x16_bf16 v[128:143], v[88:91], a[116:119], v[128:143]\n"
"ds_read_b64_tr_b16 v[108:109], v21 offset:20480\n"
"ds_read_b64_tr_b16 v[110:111], v21 offset:20992\n"
"s_add_u32 s13, s4, 30720\n"
"v_mfma_f32_32x32x16_bf16 v[144:159], v[92:95], a[116:119], v[144:159]\n"
"ds_read_b64_tr_b16 v[80:81], v22 offset:20480\n"
"ds_read_b64_tr_b16 v[82:83], v22 offset:20992\n"
"buffer_load_dwordx4 v18, s[44:47], s75 offen offset:2048 lds\n"
"s_waitcnt lgkmcnt(6)\n"
"v_mfma_f32_32x32x16_bf16 v[160:175], v[96:99], a[116:119], v[160:175]\n"
"ds_read_b64_tr_b16 v[84:85], v23 offset:20480\n"
"ds_read_b64_tr_b16 v[86:87], v23 offset:20992\n"
"s_sub_u32 s13, s13, s83\n"
"s_add_u32 s4, s4, s83\n"
"v_mfma_f32_32x32x16_bf16 v[176:191], v[100:103], a[116:119], v[176:191]\n"
"ds_read_b64_tr_b16 v[88:89], v26 offset:34816\n"
"ds_read_b64_tr_b16 v[90:91], v26 offset:35072\n"
"buffer_load_dwordx4 v19, s[44:47], s75 offen offset:3072 lds\n"
"s_waitcnt lgkmcnt(6)\n"
"v_mfma_f32_32x32x16_bf16 v[192:207], v[104:107], a[116:119], v[192:207]\n"
"ds_read_b64_tr_b16 v[92:93], v27 offset:34816\n"
"ds_read_b64_tr_b16 v[94:95], v27 offset:35072\n"
"s_mul_i32 s83, s76, 10240\n"
"v_mfma_f32_32x32x16_bf16 v[208:223], v[108:111], a[116:119], v[208:223]\n"
"ds_read_b64_tr_b16 v[96:97], v20 offset:8192\n"
"ds_read_b64_tr_b16 v[98:99], v20 offset:8704\n"
"s_add_u32 s83, s83, s82\n"
"s_add_u32 s5, s83, 245760\n"
"s_waitcnt lgkmcnt(6)\n"
"v_mfma_f32_32x32x16_bf16 v[224:239], v[80:83], a[116:119], v[224:239]\n"
"ds_read_b64_tr_b16 v[100:101], v21 offset:8192\n"
"ds_read_b64_tr_b16 v[102:103], v21 offset:8704\n"
"s_mul_i32 s83, s76, 25600\n"
"v_mfma_f32_32x32x16_bf16 v[240:255], v[84:87], a[116:119], v[240:255]\n"
"s_add_u32 s79, s75, 128\n"
"s_add_u32 m0, s81, 0x4000\n"
"ds_read_b64_tr_b16 v[104:105], v22 offset:8192\n"
"ds_read_b64_tr_b16 v[106:107], v22 offset:8704\n"
"buffer_load_dwordx4 v16, s[40:43], s79 offen lds\n"
"s_add_u32 s83, s83, s82\n"
"s_waitcnt lgkmcnt(6)\n"
"v_mfma_f32_32x32x16_bf16 v[48:63], v[88:91], a[116:119], v[48:63]\n"
"ds_read_b64_tr_b16 v[108:109], v23 offset:8192\n"
"ds_read_b64_tr_b16 v[110:111], v23 offset:8704\n"
"s_add_u32 s6, s83, 276480\n"
"v_mfma_f32_32x32x16_bf16 v[64:79], v[92:95], a[116:119], v[64:79]\n"
"ds_read_b64_tr_b16 v[80:81], v20 offset:24576\n"
"ds_read_b64_tr_b16 v[82:83], v20 offset:25088\n"
"s_bitcmp1_b32 s0, 0\n"
"s_cselect_b32 s83, 5120, 0\n"
"s_waitcnt lgkmcnt(6)\n"
"v_mfma_f32_32x32x16_bf16 v[128:143], v[96:99], a[120:123], v[128:143]\n"
"ds_read_b64_tr_b16 v[84:85], v21 offset:24576\n"
"ds_read_b64_tr_b16 v[86:87], v21 offset:25088\n"
"s_lshl_b32 s1, s83, 1\n"
"v_mfma_f32_32x32x16_bf16 v[144:159], v[100:103], a[120:123], v[144:159]\n"
"ds_read_b64_tr_b16 v[88:89], v22 offset:24576\n"
"ds_read_b64_tr_b16 v[90:91], v22 offset:25088\n"
"ds_read_b64_tr_b16 v[92:93], v23 offset:24576\n"
"ds_read_b64_tr_b16 v[94:95], v23 offset:25088\n"
"buffer_load_dwordx4 v17, s[40:43], s79 offen offset:1024 lds\n"
"s_waitcnt lgkmcnt(8)\n"
"v_mfma_f32_32x32x16_bf16 v[160:175], v[104:107], a[120:123], v[160:175]\n"
"ds_read_b64_tr_b16 v[96:97], v26 offset:36864\n"
"ds_read_b64_tr_b16 v[98:99], v26 offset:37120\n"
"s_sub_u32 s1, 5120, s1\n"
"v_mfma_f32_32x32x16_bf16 v[176:191], v[108:111], a[120:123], v[176:191]\n"
"ds_read_b64_tr_b16 v[100:101], v27 offset:36864\n"
"ds_read_b64_tr_b16 v[102:103], v27 offset:37120\n"
"buffer_load_dwordx4 v18, s[44:47], s79 offen offset:2048 lds\n"
"s_add_u32 s4, s4, s83\n"
"s_waitcnt lgkmcnt(8)\n"
"v_mfma_f32_32x32x16_bf16 v[192:207], v[80:83], a[120:123], v[192:207]\n"
"ds_read_b64_tr_b16 v[104:105], v20 offset:12288\n"
"ds_read_b64_tr_b16 v[106:107], v20 offset:12800\n"
"s_add_u32 s13, s13, s83\n"
"v_mfma_f32_32x32x16_bf16 v[208:223], v[84:87], a[120:123], v[208:223]\n"
"ds_read_b64_tr_b16 v[108:109], v21 offset:12288\n"
"ds_read_b64_tr_b16 v[110:111], v21 offset:12800\n"
"s_add_u32 s5, s5, s83\n"
"s_add_u32 s12, s6, 25600\n"
"s_waitcnt lgkmcnt(8)\n"
"v_mfma_f32_32x32x16_bf16 v[224:239], v[88:91], a[120:123], v[224:239]\n"
"ds_read_b64_tr_b16 v[80:81], v22 offset:12288\n"
"ds_read_b64_tr_b16 v[82:83], v22 offset:12800\n"
"s_mul_i32 s83, s0, 5120\n"
"v_mfma_f32_32x32x16_bf16 v[240:255], v[92:95], a[120:123], v[240:255]\n"
"ds_read_b64_tr_b16 v[84:85], v23 offset:12288\n"
"ds_read_b64_tr_b16 v[86:87], v23 offset:12800\n"
"buffer_load_dwordx4 v19, s[44:47], s79 offen offset:3072 lds\n"
"s_waitcnt lgkmcnt(8)\n"
"v_mfma_f32_32x32x16_bf16 v[48:63], v[96:99], a[120:123], v[48:63]\n"
"ds_read_b64_tr_b16 v[88:89], v20 offset:28672\n"
"ds_read_b64_tr_b16 v[90:91], v20 offset:29184\n"
"s_add_u32 s6, s6, s83\n"
"v_mfma_f32_32x32x16_bf16 v[64:79], v[100:103], a[120:123], v[64:79]\n"
"s_add_u32 s79, s75, s99\n"
"s_add_u32 m0, s80, 0x8000\n"
"ds_read_b64_tr_b16 v[92:93], v21 offset:28672\n"
"ds_read_b64_tr_b16 v[94:95], v21 offset:29184\n"
"buffer_load_dwordx4 v24, s[40:43], s79 offen lds\n"
"s_waitcnt lgkmcnt(8)\n"
"v_mfma_f32_32x32x16_bf16 v[128:143], v[104:107], a[124:127], v[128:143]\n"
"ds_read_b64_tr_b16 v[96:97], v22 offset:28672\n"
"ds_read_b64_tr_b16 v[98:99], v22 offset:29184\n"
"s_cmp_eq_u32 s76, 3\n"
"s_cselect_b32 s5, 0x40000000, s5\n"
"v_mfma_f32_32x32x16_bf16 v[144:159], v[108:111], a[124:127], v[144:159]\n"
"ds_read_b64_tr_b16 v[100:101], v23 offset:28672\n"
"ds_read_b64_tr_b16 v[102:103], v23 offset:29184\n"
"s_mul_i32 s96, s76, 12288\n"
"s_add_u32 s96, s96, 114688\n"
"s_waitcnt lgkmcnt(8)\n"
"v_mfma_f32_32x32x16_bf16 v[160:175], v[80:83], a[124:127], v[160:175]\n"
"ds_read_b64_tr_b16 v[104:105], v26 offset:38912\n"
"ds_read_b64_tr_b16 v[106:107], v26 offset:39168\n"
"s_lshr_b32 s82, s89, 2\n"
"v_mfma_f32_32x32x16_bf16 v[176:191], v[84:87], a[124:127], v[176:191]\n"
"ds_read_b64_tr_b16 v[108:109], v27 offset:38912\n"
"ds_read_b64_tr_b16 v[110:111], v27 offset:39168\n"
"buffer_load_dwordx4 v25, s[44:47], s79 offen offset:1024 lds\n"
"s_waitcnt lgkmcnt(8)\n"
"v_mfma_f32_32x32x16_bf16 v[192:207], v[88:91], a[124:127], v[192:207]\n"
"v_add_u32 v20, 0xa000, v20\n"
"v_add_u32 v21, 0xa000, v21\n"
"v_add_u32 v22, 0xa000, v22\n"
"s_lshl_b32 s82, s82, 10\n"
"v_mfma_f32_32x32x16_bf16 v[208:223], v[92:95], a[124:127], v[208:223]\n"
"v_add_u32 v23, 0xa000, v23\n"
"v_add_u32 v26, 0xa000, v26\n"
"v_add_u32 v27, 0xa000, v27\n"
"s_add_u32 s2, s82, 10240\n"
"s_waitcnt lgkmcnt(4)\n"
"v_mfma_f32_32x32x16_bf16 v[224:239], v[96:99], a[124:127], v[224:239]\n"
"s_waitcnt vmcnt(10) lgkmcnt(0)\n"
"s_barrier\n"
"ds_read_b128 a[128:131], v32 offset:0\n"
"ds_read_b128 a[132:135], v33 offset:0\n"
"ds_read_b64_tr_b16 v[80:81], v20 offset:0\n"
"ds_read_b64_tr_b16 v[82:83], v20 offset:512\n"
"v_mfma_f32_32x32x16_bf16 v[240:255], v[100:103], a[124:127], v[240:255]\n"
"ds_read_b128 a[136:139], v34 offset:0\n"
"ds_read_b128 a[140:143], v35 offset:0\n"
"ds_read_b64_tr_b16 v[84:85], v21 offset:0\n"
"ds_read_b64_tr_b16 v[86:87], v21 offset:512\n"
"v_mfma_f32_32x32x16_bf16 v[48:63], v[104:107], a[124:127], v[48:63]\n"
"ds_read_b64_tr_b16 v[88:89], v22 offset:0\n"
"ds_read_b64_tr_b16 v[90:91], v22 offset:512\n"
"s_cmp_eq_u32 s76, 0\n"
"s_cselect_b32 s94, -1, 0\n"
"s_cselect_b32 s95, 0x1f, 0\n"
"s_cselect_b32 s2, s2, 0x40000000\n"
"v_mfma_f32_32x32x16_bf16 v[64:79], v[108:111], a[124:127], v[64:79]\n"
"ds_read_b64_tr_b16 v[92:93], v23 offset:0\n"
"ds_read_b64_tr_b16 v[94:95], v23 offset:512\n"
"s_waitcnt lgkmcnt(4)\n"
"v_mfma_f32_32x32x16_bf16 v[128:143], v[80:83], a[128:131], v[128:143]\n"
"ds_read_b64_tr_b16 v[96:97], v20 offset:16384\n"
"ds_read_b64_tr_b16 v[98:99], v20 offset:16896\n"
"s_add_u32 m0, s81, 0x1e000\n"
"s_nop 0\n"
"buffer_load_dwordx4 v28, s[36:39], s65 offen lds\n"
"buffer_load_dwordx4 v[8:11], v6, s[48:51], s2 offen sc1\n"
"v_mfma_f32_32x32x16_bf16 v[144:159], v[84:87], a[128:131], v[144:159]\n"
"ds_read_b64_tr_b16 v[100:101], v21 offset:16384\n"
"ds_read_b64_tr_b16 v[102:103], v21 offset:16896\n"
"buffer_load_dwordx4 v29, s[36:39], s65 offen offset:1024 lds\n"
"s_waitcnt lgkmcnt(4)\n"
"v_mfma_f32_32x32x16_bf16 v[160:175], v[88:91], a[128:131], v[160:175]\n"
"ds_read_b64_tr_b16 v[104:105], v22 offset:16384\n"
"ds_read_b64_tr_b16 v[106:107], v22 offset:16896\n"
"buffer_load_dwordx4 v30, s[36:39], s65 offen offset:2048 lds\n"
"v_mfma_f32_32x32x16_bf16 v[176:191], v[92:95], a[128:131], v[176:191]\n"
"ds_read_b64_tr_b16 v[108:109], v23 offset:16384\n"
"ds_read_b64_tr_b16 v[110:111], v23 offset:16896\n"
"buffer_load_dwordx4 v31, s[36:39], s65 offen offset:3072 lds\n"
"s_waitcnt lgkmcnt(4)\n"
"v_mfma_f32_32x32x16_bf16 v[192:207], v[96:99], a[128:131], v[192:207]\n"
"ds_read_b64_tr_b16 v[80:81], v26 offset:32768\n"
"ds_read_b64_tr_b16 v[82:83], v26 offset:33024\n"
"v_mfma_f32_32x32x16_bf16 v[208:223], v[100:103], a[128:131], v[208:223]\n"
"s_add_u32 s40, s40, 256\n"
"s_addc_u32 s41, s41, 0\n"
"s_sub_u32 s42, s42, 256\n"
"s_add_u32 s44, s44, 256\n"
"s_addc_u32 s45, s45, 0\n"
"s_sub_u32 s46, s46, 256\n"
"s_add_u32 m0, s81, 0xa000\n"
"ds_read_b64_tr_b16 v[84:85], v27 offset:32768\n"
"ds_read_b64_tr_b16 v[86:87], v27 offset:33024\n"
"buffer_load_dwordx4 v16, s[40:43], s66 offen lds\n"
"s_waitcnt lgkmcnt(4)\n"
"v_mfma_f32_32x32x16_bf16 v[224:239], v[104:107], a[128:131], v[224:239]\n"
"ds_read_b64_tr_b16 v[88:89], v20 offset:4096\n"
"ds_read_b64_tr_b16 v[90:91], v20 offset:4608\n"
"v_mfma_f32_32x32x16_bf16 v[240:255], v[108:111], a[128:131], v[240:255]\n"
"ds_read_b64_tr_b16 v[92:93], v21 offset:4096\n"
"ds_read_b64_tr_b16 v[94:95], v21 offset:4608\n"
"s_waitcnt lgkmcnt(4)\n"
"v_mfma_f32_32x32x16_bf16 v[48:63], v[80:83], a[128:131], v[48:63]\n"
"ds_read_b64_tr_b16 v[96:97], v22 offset:4096\n"
"ds_read_b64_tr_b16 v[98:99], v22 offset:4608\n"
"v_mfma_f32_32x32x16_bf16 v[64:79], v[84:87], a[128:131], v[64:79]\n"
"ds_read_b64_tr_b16 v[100:101], v23 offset:4096\n"
"ds_read_b64_tr_b16 v[102:103], v23 offset:4608\n"
"ds_read_b64_tr_b16 v[104:105], v20 offset:20480\n"
"ds_read_b64_tr_b16 v[106:107], v20 offset:20992\n"
"buffer_load_dwordx4 v17, s[40:43], s66 offen offset:1024 lds\n"
"s_waitcnt lgkmcnt(6)\n"
"v_mfma_f32_32x32x16_bf16 v[128:143], v[88:91], a[132:135], v[128:143]\n"
"ds_read_b64_tr_b16 v[108:109], v21 offset:20480\n"
"ds_read_b64_tr_b16 v[110:111], v21 offset:20992\n"
"v_mfma_f32_32x32x16_bf16 v[144:159], v[92:95], a[132:135], v[144:159]\n"
"ds_read_b64_tr_b16 v[80:81], v22 offset:20480\n"
"ds_read_b64_tr_b16 v[82:83], v22 offset:20992\n"
"s_waitcnt lgkmcnt(6)\n"
"v_mfma_f32_32x32x16_bf16 v[160:175], v[96:99], a[132:135], v[160:175]\n"
"ds_read_b64_tr_b16 v[84:85], v23 offset:20480\n"
"ds_read_b64_tr_b16 v[86:87], v23 offset:20992\n"
"v_mfma_f32_32x32x16_bf16 v[176:191], v[100:103], a[132:135], v[176:191]\n"
"ds_read_b64_tr_b16 v[88:89], v26 offset:34816\n"
"ds_read_b64_tr_b16 v[90:91], v26 offset:35072\n"
"buffer_load_dwordx4 v18, s[44:47], s66 offen offset:2048 lds\n"
"s_waitcnt lgkmcnt(6)\n"
"v_mfma_f32_32x32x16_bf16 v[192:207], v[104:107], a[132:135], v[192:207]\n"
"ds_read_b64_tr_b16 v[92:93], v27 offset:34816\n"
"ds_read_b64_tr_b16 v[94:95], v27 offset:35072\n"
"v_mfma_f32_32x32x16_bf16 v[208:223], v[108:111], a[132:135], v[208:223]\n"
"ds_read_b64_tr_b16 v[96:97], v20 offset:8192\n"
"ds_read_b64_tr_b16 v[98:99], v20 offset:8704\n"
"s_waitcnt lgkmcnt(6)\n"
"v_mfma_f32_32x32x16_bf16 v[224:239], v[80:83], a[132:135], v[224:239]\n"
"ds_read_b64_tr_b16 v[100:101], v21 offset:8192\n"
"ds_read_b64_tr_b16 v[102:103], v21 offset:8704\n"
"v_mfma_f32_32x32x16_bf16 v[240:255], v[84:87], a[132:135], v[240:255]\n"
"ds_read_b64_tr_b16 v[104:105], v22 offset:8192\n"
"ds_read_b64_tr_b16 v[106:107], v22 offset:8704\n"
"buffer_load_dwordx4 v19, s[44:47], s66 offen offset:3072 lds\n"
"s_waitcnt lgkmcnt(6)\n"
"v_mfma_f32_32x32x16_bf16 v[48:63], v[88:91], a[132:135], v[48:63]\n"
"ds_read_b64_tr_b16 v[108:109], v23 offset:8192\n"
"ds_read_b64_tr_b16 v[110:111], v23 offset:8704\n"
"v_mfma_f32_32x32x16_bf16 v[64:79], v[92:95], a[132:135], v[64:79]\n"
"ds_read_b64_tr_b16 v[80:81], v20 offset:24576\n"
"ds_read_b64_tr_b16 v[82:83], v20 offset:25088\n"
"s_waitcnt lgkmcnt(6)\n"
"v_mfma_f32_32x32x16_bf16 v[128:143], v[96:99], a[136:139], v[128:143]\n"
"ds_read_b64_tr_b16 v[84:85], v21 offset:24576\n"
"ds_read_b64_tr_b16 v[86:87], v21 offset:25088\n"
"v_mfma_f32_32x32x16_bf16 v[144:159], v[100:103], a[136:139], v[144:159]\n"
"s_add_u32 m0, s81, 0xe000\n"
"ds_read_b64_tr_b16 v[88:89], v22 offset:24576\n"
"ds_read_b64_tr_b16 v[90:91], v22 offset:25088\n"
"ds_read_b64_tr_b16 v[92:93], v23 offset:24576\n"
"ds_read_b64_tr_b16 v[94:95], v23 offset:25088\n"
"buffer_load_dwordx4 v16, s[40:43], s67 offen lds\n"
"s_waitcnt lgkmcnt(8)\n"
"v_mfma_f32_32x32x16_bf16 v[160:175], v[104:107], a[136:139], v[160:175]\n"
"ds_read_b64_tr_b16 v[96:97], v26 offset:36864\n"
"ds_read_b64_tr_b16 v[98:99], v26 offset:37120\n"
"v_mfma_f32_32x32x16_bf16 v[176:191], v[108:111], a[136:139], v[176:191]\n"
"ds_read_b64_tr_b16 v[100:101], v27 offset:36864\n"
"ds_read_b64_tr_b16 v[102:103], v27 offset:37120\n"
"s_waitcnt lgkmcnt(8)\n"
"v_mfma_f32_32x32x16_bf16 v[192:207], v[80:83], a[136:139], v[192:207]\n"
"ds_read_b64_tr_b16 v[104:105], v20 offset:12288\n"
"ds_read_b64_tr_b16 v[106:107], v20 offset:12800\n"
"v_mfma_f32_32x32x16_bf16 v[208:223], v[84:87], a[136:139], v[208:223]\n"
"ds_read_b64_tr_b16 v[108:109], v21 offset:12288\n"
"ds_read_b64_tr_b16 v[110:111], v21 offset:12800\n"
"buffer_load_dwordx4 v17, s[40:43], s67 offen offset:1024 lds\n"
"s_waitcnt lgkmcnt(8)\n"
"v_mfma_f32_32x32x16_bf16 v[224:239], v[88:91], a[136:139], v[224:239]\n"
"ds_read_b64_tr_b16 v[80:81], v22 offset:12288\n"
"ds_read_b64_tr_b16 v[82:83], v22 offset:12800\n"
"v_mfma_f32_32x32x16_bf16 v[240:255], v[92:95], a[136:139], v[240:255]\n"
"ds_read_b64_tr_b16 v[84:85], v23 offset:12288\n"
"ds_read_b64_tr_b16 v[86:87], v23 offset:12800\n"
"s_waitcnt lgkmcnt(8)\n"
"v_mfma_f32_32x32x16_bf16 v[48:63], v[96:99], a[136:139], v[48:63]\n"
"ds_read_b64_tr_b16 v[88:89], v20 offset:28672\n"
"ds_read_b64_tr_b16 v[90:91], v20 offset:29184\n"
"v_mfma_f32_32x32x16_bf16 v[64:79], v[100:103], a[136:139], v[64:79]\n"
"ds_read_b64_tr_b16 v[92:93], v21 offset:28672\n"
"ds_read_b64_tr_b16 v[94:95], v21 offset:29184\n"
"buffer_load_dwordx4 v18, s[44:47], s67 offen offset:2048 lds\n"
"s_waitcnt lgkmcnt(8)\n"
"v_mfma_f32_32x32x16_bf16 v[128:143], v[104:107], a[140:143], v[128:143]\n"
"ds_read_b64_tr_b16 v[96:97], v22 offset:28672\n"
"ds_read_b64_tr_b16 v[98:99], v22 offset:29184\n"
"v_mfma_f32_32x32x16_bf16 v[144:159], v[108:111], a[140:143], v[144:159]\n"
"ds_read_b64_tr_b16 v[100:101], v23 offset:28672\n"
"ds_read_b64_tr_b16 v[102:103], v23 offset:29184\n"
"s_waitcnt lgkmcnt(8)\n"
"v_mfma_f32_32x32x16_bf16 v[160:175], v[80:83], a[140:143], v[160:175]\n"
"ds_read_b64_tr_b16 v[104:105], v26 offset:38912\n"
"ds_read_b64_tr_b16 v[106:107], v26 offset:39168\n"
"v_mfma_f32_32x32x16_bf16 v[176:191], v[84:87], a[140:143], v[176:191]\n"
"ds_read_b64_tr_b16 v[108:109], v27 offset:38912\n"
"ds_read_b64_tr_b16 v[110:111], v27 offset:39168\n"
"buffer_load_dwordx4 v19, s[44:47], s67 offen offset:3072 lds\n"
"s_waitcnt lgkmcnt(8)\n"
"v_mfma_f32_32x32x16_bf16 v[192:207], v[88:91], a[140:143], v[192:207]\n"
"v_subrev_u32 v20, 0x14000, v20\n"
"v_subrev_u32 v21, 0x14000, v21\n"
"v_subrev_u32 v22, 0x14000, v22\n"
"v_mfma_f32_32x32x16_bf16 v[208:223], v[92:95], a[140:143], v[208:223]\n"
"v_subrev_u32 v23, 0x14000, v23\n"
"v_subrev_u32 v26, 0x14000, v26\n"
"v_subrev_u32 v27, 0x14000, v27\n"
"s_waitcnt vmcnt(11)\n"
"v_cmp_eq_u64 vcc, s[84:85], v[8:9]\n"
"v_cmp_eq_u64 s[10:11], s[84:85], v[10:11]\n"
"s_and_b64 vcc, vcc, s[10:11]\n"
"s_and_b64 s[10:11], vcc, s[94:95]\n"
"s_cmp_eq_u64 s[10:11], s[94:95]\n"
"s_cbranch_scc0 L_ffn_b_slow\n"
"L_ffn_b_ok:\n"
"s_waitcnt lgkmcnt(4)\n"
"v_mfma_f32_32x32x16_bf16 v[224:239], v[96:99], a[140:143], v[224:239]\n"
"s_waitcnt vmcnt(8) lgkmcnt(0)\n"
"s_barrier\n"
"ds_read_b128 a[144:147], v32 offset:0\n"
"ds_read_b128 a[148:151], v33 offset:0\n"
"ds_read_b64_tr_b16 v[80:81], v20 offset:0\n"
"ds_read_b64_tr_b16 v[82:83], v20 offset:512\n"
"v_mfma_f32_32x32x16_bf16 v[240:255], v[100:103], a[140:143], v[240:255]\n"
"ds_read_b128 a[152:155], v34 offset:0\n"
"ds_read_b128 a[156:159], v35 offset:0\n"
"ds_read_b64_tr_b16 v[84:85], v21 offset:0\n"
"ds_read_b64_tr_b16 v[86:87], v21 offset:512\n"
"v_mfma_f32_32x32x16_bf16 v[48:63], v[104:107], a[140:143], v[48:63]\n"
"s_add_u32 s7, s4, 0\n"
"ds_read_b64_tr_b16 v[88:89], v22 offset:0\n"
"ds_read_b64_tr_b16 v[90:91], v22 offset:512\n"
"buffer_load_dwordx4 a[160:163], v6, s[48:51], s7 offen offset:0 sc1\n"
"v_mfma_f32_32x32x16_bf16 v[64:79], v[108:111], a[140:143], v[64:79]\n"
"s_add_u32 m0, s81, 0x14000\n"
"ds_read_b64_tr_b16 v[92:93], v23 offset:0\n"
"ds_read_b64_tr_b16 v[94:95], v23 offset:512\n"
"buffer_load_dwordx4 v16, s[40:43], s68 offen lds\n"
"s_waitcnt lgkmcnt(4)\n"
"v_mfma_f32_32x32x16_bf16 v[128:143], v[80:83], a[144:147], v[128:143]\n"
"ds_read_b64_tr_b16 v[96:97], v20 offset:16384\n"
"ds_read_b64_tr_b16 v[98:99], v20 offset:16896\n"
"v_mfma_f32_32x32x16_bf16 v[144:159], v[84:87], a[144:147], v[144:159]\n"
"ds_read_b64_tr_b16 v[100:101], v21 offset:16384\n"
"ds_read_b64_tr_b16 v[102:103], v21 offset:16896\n"
"s_waitcnt lgkmcnt(4)\n"
"v_mfma_f32_32x32x16_bf16 v[160:175], v[88:91], a[144:147], v[160:175]\n"
"ds_read_b64_tr_b16 v[104:105], v22 offset:16384\n"
"ds_read_b64_tr_b16 v[106:107], v22 offset:16896\n"
"v_mfma_f32_32x32x16_bf16 v[176:191], v[92:95], a[144:147], v[176:191]\n"
"ds_read_b64_tr_b16 v[108:109], v23 offset:16384\n"
"ds_read_b64_tr_b16 v[110:111], v23 offset:16896\n"
"buffer_load_dwordx4 v17, s[40:43], s68 offen offset:1024 lds\n"
"s_waitcnt lgkmcnt(4)\n"
"v_mfma_f32_32x32x16_bf16 v[192:207], v[96:99], a[144:147], v[192:207]\n"
"ds_read_b64_tr_b16 v[80:81], v26 offset:32768\n"
"ds_read_b64_tr_b16 v[82:83], v26 offset:33024\n"
"v_mfma_f32_32x32x16_bf16 v[208:223], v[100:103], a[144:147], v[208:223]\n"
"ds_read_b64_tr_b16 v[84:85], v27 offset:32768\n"
"ds_read_b64_tr_b16 v[86:87], v27 offset:33024\n"
"s_waitcnt lgkmcnt(4)\n"
"v_mfma_f32_32x32x16_bf16 v[224:239], v[104:107], a[144:147], v[224:239]\n"
"ds_read_b64_tr_b16 v[88:89], v20 offset:4096\n"
"ds_read_b64_tr_b16 v[90:91], v20 offset:4608\n"
"buffer_load_dwordx4 a[164:167], v6, s[48:51], s7 offen offset:1024 sc1\n"
"v_mfma_f32_32x32x16_bf16 v[240:255], v[108:111], a[144:147], v[240:255]\n"
"ds_read_b64_tr_b16 v[92:93], v21 offset:4096\n"
"ds_read_b64_tr_b16 v[94:95], v21 offset:4608\n"
"buffer_load_dwordx4 v18, s[44:47], s68 offen offset:2048 lds\n"
"s_waitcnt lgkmcnt(4)\n"
"v_mfma_f32_32x32x16_bf16 v[48:63], v[80:83], a[144:147], v[48:63]\n"
"ds_read_b64_tr_b16 v[96:97], v22 offset:4096\n"
"ds_read_b64_tr_b16 v[98:99], v22 offset:4608\n"
"v_mfma_f32_32x32x16_bf16 v[64:79], v[84:87], a[144:147], v[64:79]\n"
"ds_read_b64_tr_b16 v[100:101], v23 offset:4096\n"
"ds_read_b64_tr_b16 v[102:103], v23 offset:4608\n"
"ds_read_b64_tr_b16 v[104:105], v20 offset:20480\n"
"ds_read_b64_tr_b16 v[106:107], v20 offset:20992\n"
"s_waitcnt lgkmcnt(6)\n"
"v_mfma_f32_32x32x16_bf16 v[128:143], v[88:91], a[148:151], v[128:143]\n"
"ds_read_b64_tr_b16 v[108:109], v21 offset:20480\n"
"ds_read_b64_tr_b16 v[110:111], v21 offset:20992\n"
"v_mfma_f32_32x32x16_bf16 v[144:159], v[92:95], a[148:151], v[144:159]\n"
"ds_read_b64_tr_b16 v[80:81], v22 offset:20480\n"
"ds_read_b64_tr_b16 v[82:83], v22 offset:20992\n"
"s_waitcnt lgkmcnt(6)\n"
"v_mfma_f32_32x32x16_bf16 v[160:175], v[96:99], a[148:151], v[160:175]\n"
"ds_read_b64_tr_b16 v[84:85], v23 offset:20480\n"
"ds_read_b64_tr_b16 v[86:87], v23 offset:20992\n"
"v_mfma_f32_32x32x16_bf16 v[176:191], v[100:103], a[148:151], v[176:191]\n"
"ds_read_b64_tr_b16 v[88:89], v26 offset:34816\n"
"ds_read_b64_tr_b16 v[90:91], v26 offset:35072\n"
"buffer_load_dwordx4 v19, s[44:47], s68 offen offset:3072 lds\n"
"s_waitcnt lgkmcnt(6)\n"
"v_mfma_f32_32x32x16_bf16 v[192:207], v[104:107], a[148:151], v[192:207]\n"
"ds_read_b64_tr_b16 v[92:93], v27 offset:34816\n"
"ds_read_b64_tr_b16 v[94:95], v27 offset:35072\n"
"v_mfma_f32_32x32x16_bf16 v[208:223], v[108:111], a[148:151], v[208:223]\n"
"ds_read_b64_tr_b16 v[96:97], v20 offset:8192\n"
"ds_read_b64_tr_b16 v[98:99], v20 offset:8704\n"
"buffer_load_dwordx4 a[168:171], v6, s[48:51], s7 offen offset:2048 sc1\n"
"s_waitcnt lgkmcnt(6)\n"
"v_mfma_f32_32x32x16_bf16 v[224:239], v[80:83], a[148:151], v[224:239]\n"
"ds_read_b64_tr_b16 v[100:101], v21 offset:8192\n"
"ds_read_b64_tr_b16 v[102:103], v21 offset:8704\n"
"v_mfma_f32_32x32x16_bf16 v[240:255], v[84:87], a[148:151], v[240:255]\n"
"s_add_u32 m0, s81, 0x18000\n"
"ds_read_b64_tr_b16 v[104:105], v22 offset:8192\n"
"ds_read_b64_tr_b16 v[106:107], v22 offset:8704\n"
"buffer_load_dwordx4 v16, s[40:43], s69 offen lds\n"
"s_waitcnt lgkmcnt(6)\n"
"v_mfma_f32_32x32x16_bf16 v[48:63], v[88:91], a[148:151], v[48:63]\n"
"ds_read_b64_tr_b16 v[108:109], v23 offset:8192\n"
"ds_read_b64_tr_b16 v[110:111], v23 offset:8704\n"
"v_mfma_f32_32x32x16_bf16 v[64:79], v[92:95], a[148:151], v[64:79]\n"
"ds_read_b64_tr_b16 v[80:81], v20 offset:24576\n"
"ds_read_b64_tr_b16 v[82:83], v20 offset:25088\n"
"s_waitcnt lgkmcnt(6)\n"
"v_mfma_f32_32x32x16_bf16 v[128:143], v[96:99], a[152:155], v[128:143]\n"
"ds_read_b64_tr_b16 v[84:85], v21 offset:24576\n"
"ds_read_b64_tr_b16 v[86:87], v21 offset:25088\n"
"v_mfma_f32_32x32x16_bf16 v[144:159], v[100:103], a[152:155], v[144:159]\n"
"ds_read_b64_tr_b16 v[88:89], v22 offset:24576\n"
"ds_read_b64_tr_b16 v[90:91], v22 offset:25088\n"
"ds_read_b64_tr_b16 v[92:93], v23 offset:24576\n"
"ds_read_b64_tr_b16 v[94:95], v23 offset:25088\n"
"buffer_load_dwordx4 v17, s[40:43], s69 offen offset:1024 lds\n"
"s_waitcnt lgkmcnt(8)\n"
"v_mfma_f32_32x32x16_bf16 v[160:175], v[104:107], a[152:155], v[160:175]\n"
"ds_read_b64_tr_b16 v[96:97], v26 offset:36864\n"
"ds_read_b64_tr_b16 v[98:99], v26 offset:37120\n"
"v_mfma_f32_32x32x16_bf16 v[176:191], v[108:111], a[152:155], v[176:191]\n"
"ds_read_b64_tr_b16 v[100:101], v27 offset:36864\n"
"ds_read_b64_tr_b16 v[102:103], v27 offset:37120\n"
"buffer_load_dwordx4 a[172:175], v6, s[48:51], s7 offen offset:3072 sc1\n"
"s_waitcnt lgkmcnt(8)\n"
"v_mfma_f32_32x32x16_bf16 v[192:207], v[80:83], a[152:155], v[192:207]\n"
"ds_read_b64_tr_b16 v[104:105], v20 offset:12288\n"
"ds_read_b64_tr_b16 v[106:107], v20 offset:12800\n"
"v_mfma_f32_32x32x16_bf16 v[208:223], v[84:87], a[152:155], v[208:223]\n"
"ds_read_b64_tr_b16 v[108:109], v21 offset:12288\n"
"ds_read_b64_tr_b16 v[110:111], v21 offset:12800\n"
"s_waitcnt lgkmcnt(8)\n"
"v_mfma_f32_32x32x16_bf16 v[224:239], v[88:91], a[152:155], v[224:239]\n"
"ds_read_b64_tr_b16 v[80:81], v22 offset:12288\n"
"ds_read_b64_tr_b16 v[82:83], v22 offset:12800\n"
"v_mfma_f32_32x32x16_bf16 v[240:255], v[92:95], a[152:155], v[240:255]\n"
"ds_read_b64_tr_b16 v[84:85], v23 offset:12288\n"
"ds_read_b64_tr_b16 v[86:87], v23 offset:12800\n"
"buffer_load_dwordx4 v18, s[44:47], s69 offen offset:2048 lds\n"
"s_waitcnt lgkmcnt(8)\n"
"v_mfma_f32_32x32x16_bf16 v[48:63], v[96:99], a[152:155], v[48:63]\n"
"ds_read_b64_tr_b16 v[88:89], v20 offset:28672\n"
"ds_read_b64_tr_b16 v[90:91], v20 offset:29184\n"
"v_mfma_f32_32x32x16_bf16 v[64:79], v[100:103], a[152:155], v[64:79]\n"
"ds_read_b64_tr_b16 v[92:93], v21 offset:28672\n"
"ds_read_b64_tr_b16 v[94:95], v21 offset:29184\n"
"s_waitcnt lgkmcnt(8)\n"
"v_mfma_f32_32x32x16_bf16 v[128:143], v[104:107], a[156:159], v[128:143]\n"
"s_add_u32 s7, s7, 1024\n"
"ds_read_b64_tr_b16 v[96:97], v22 offset:28672\n"
"ds_read_b64_tr_b16 v[98:99], v22 offset:29184\n"
"buffer_load_dwordx4 a[176:179], v6, s[48:51], s7 offen offset:3072 sc1\n"
"v_mfma_f32_32x32x16_bf16 v[144:159], v[108:111], a[156:159], v[144:159]\n"
"ds_read_b64_tr_b16 v[100:101], v23 offset:28672\n"
"ds_read_b64_tr_b16 v[102:103], v23 offset:29184\n"
"buffer_load_dwordx4 v19, s[44:47], s69 offen offset:3072 lds\n"
"s_waitcnt lgkmcnt(8)\n"
"v_mfma_f32_32x32x16_bf16 v[160:175], v[80:83], a[156:159], v[160:175]\n"
"ds_read_b64_tr_b16 v[104:105], v26 offset:38912\n"
"ds_read_b64_tr_b16 v[106:107], v26 offset:39168\n"
"v_mfma_f32_32x32x16_bf16 v[176:191], v[84:87], a[156:159], v[176:191]\n"
"ds_read_b64_tr_b16 v[108:109], v27 offset:38912\n"
"ds_read_b64_tr_b16 v[110:111], v27 offset:39168\n"
"s_waitcnt lgkmcnt(8)\n"
"v_mfma_f32_32x32x16_bf16 v[192:207], v[88:91], a[156:159], v[192:207]\n"
"v_add_u32 v20, 0xa000, v20\n"
"v_add_u32 v21, 0xa000, v21\n"
"v_mfma_f32_32x32x16_bf16 v[208:223], v[92:95], a[156:159], v[208:223]\n"
"v_add_u32 v22, 0xa000, v22\n"
"v_add_u32 v23, 0xa000, v23\n"
"s_waitcnt lgkmcnt(4)\n"
"v_mfma_f32_32x32x16_bf16 v[224:239], v[96:99], a[156:159], v[224:239]\n"
"s_waitcnt vmcnt(13) lgkmcnt(0)\n"
"s_barrier\n"
"ds_read_b64_tr_b16 v[80:81], v20 offset:0\n"
"ds_read_b64_tr_b16 v[82:83], v20 offset:512\n"
"v_mfma_f32_32x32x16_bf16 v[240:255], v[100:103], a[156:159], v[240:255]\n"
"ds_read_b64_tr_b16 v[84:85], v21 offset:0\n"
"ds_read_b64_tr_b16 v[86:87], v21 offset:512\n"
"v_mfma_f32_32x32x16_bf16 v[48:63], v[104:107], a[156:159], v[48:63]\n"
"ds_read_b64_tr_b16 v[88:89], v22 offset:0\n"
"ds_read_b64_tr_b16 v[90:91], v22 offset:512\n"
"v_mfma_f32_32x32x16_bf16 v[64:79], v[108:111], a[156:159], v[64:79]\n"
"s_add_u32 m0, s81, 0x0\n"
"ds_read_b64_tr_b16 v[92:93], v23 offset:0\n"
"ds_read_b64_tr_b16 v[94:95], v23 offset:512\n"
"buffer_load_dwordx4 v16, s[40:43], s70 offen lds\n"
"s_mov_b32 s64, 0xbfb8aa3b\n"
"s_mov_b32 s65, 1.0\n"
"v_pk_mul_f32 v[36:37], v[128:129], s[64:65] op_sel_hi:[1,0]\n"
"s_add_u32 s7, s4, 0\n"
"s_add_u32 s7, s7, s1\n"
"buffer_load_dwordx4 a[180:183], v6, s[48:51], s7 offen offset:0 sc1\n"
"v_pk_mul_f32 v[38:39], v[130:131], s[64:65] op_sel_hi:[1,0]\n"
"v_exp_f32 v36, v36\n"
"v_exp_f32 v37, v37\n"
"v_pk_mul_f32 v[132:133], v[128:129], v[132:133]\n"
"v_exp_f32 v38, v38\n"
"v_exp_f32 v39, v39\n"
"v_pk_mul_f32 v[134:135], v[130:131], v[134:135]\n"
"v_pk_add_f32 v[36:37], v[36:37], s[64:65] op_sel:[0,1] op_sel_hi:[1,1]\n"
"v_pk_add_f32 v[38:39], v[38:39], s[64:65] op_sel:[0,1] op_sel_hi:[1,1]\n"
"v_rcp_f32 v36, v36\n"
"v_rcp_f32 v37, v37\n"
"v_rcp_f32 v38, v38\n"
"v_rcp_f32 v39, v39\n"
"v_pk_mul_f32 v[132:133], v[132:133], v[36:37]\n"
"v_pk_mul_f32 v[134:135], v[134:135], v[38:39]\n"
"v_cvt_pk_bf16_f32 v40, v132, v133\n"
"v_cvt_pk_bf16_f32 v41, v134, v135\n"
"v_pk_mul_f32 v[36:37], v[136:137], s[64:65] op_sel_hi:[1,0]\n"
"buffer_load_dwordx4 a[184:187], v6, s[48:51], s7 offen offset:1024 sc1\n"
"v_pk_mul_f32 v[38:39], v[138:139], s[64:65] op_sel_hi:[1,0]\n"
"v_exp_f32 v36, v36\n"
"v_exp_f32 v37, v37\n"
"v_pk_mul_f32 v[140:141], v[136:137], v[140:141]\n"
"v_exp_f32 v38, v38\n"
"v_exp_f32 v39, v39\n"
"v_pk_mul_f32 v[142:143], v[138:139], v[142:143]\n"
"v_pk_add_f32 v[36:37], v[36:37], s[64:65] op_sel:[0,1] op_sel_hi:[1,1]\n"
"v_pk_add_f32 v[38:39], v[38:39], s[64:65] op_sel:[0,1] op_sel_hi:[1,1]\n"
"v_rcp_f32 v36, v36\n"
"v_rcp_f32 v37, v37\n"
"v_rcp_f32 v38, v38\n"
"v_rcp_f32 v39, v39\n"
"v_pk_mul_f32 v[140:141], v[140:141], v[36:37]\n"
"v_pk_mul_f32 v[142:143], v[142:143], v[38:39]\n"
"v_cvt_pk_bf16_f32 v42, v140, v141\n"
"v_cvt_pk_bf16_f32 v43, v142, v143\n"
"buffer_store_dwordx4 v[40:43], v5, s[48:51], s86 offen offset:0 sc1\n"
"v_pk_mul_f32 v[36:37], v[144:145], s[64:65] op_sel_hi:[1,0]\n"
"buffer_load_dwordx4 a[188:191], v6, s[48:51], s7 offen offset:2048 sc1\n"
"v_pk_mul_f32 v[38:39], v[146:147], s[64:65] op_sel_hi:[1,0]\n"
"v_exp_f32 v36, v36\n"
"v_exp_f32 v37, v37\n"
"v_pk_mul_f32 v[148:149], v[144:145], v[148:149]\n"
"v_exp_f32 v38, v38\n"
"v_exp_f32 v39, v39\n"
"v_pk_mul_f32 v[150:151], v[146:147], v[150:151]\n"
"v_pk_add_f32 v[36:37], v[36:37], s[64:65] op_sel:[0,1] op_sel_hi:[1,1]\n"
"v_pk_add_f32 v[38:39], v[38:39], s[64:65] op_sel:[0,1] op_sel_hi:[1,1]\n"
"v_rcp_f32 v36, v36\n"
"v_rcp_f32 v37, v37\n"
"v_rcp_f32 v38, v38\n"
"v_rcp_f32 v39, v39\n"
"v_pk_mul_f32 v[148:149], v[148:149], v[36:37]\n"
"v_pk_mul_f32 v[150:151], v[150:151], v[38:39]\n"
"v_cvt_pk_bf16_f32 v44, v148, v149\n"
"v_cvt_pk_bf16_f32 v45, v150, v151\n"
"v_pk_mul_f32 v[36:37], v[152:153], s[64:65] op_sel_hi:[1,0]\n"
"buffer_load_dwordx4 a[192:195], v6, s[48:51], s7 offen offset:3072 sc1\n"
"v_pk_mul_f32 v[38:39], v[154:155], s[64:65] op_sel_hi:[1,0]\n"
"v_exp_f32 v36, v36\n"
"v_exp_f32 v37, v37\n"
"v_pk_mul_f32 v[156:157], v[152:153], v[156:157]\n"
"v_exp_f32 v38, v38\n"
"v_exp_f32 v39, v39\n"
"v_pk_mul_f32 v[158:159], v[154:155], v[158:159]\n"
"v_pk_add_f32 v[36:37], v[36:37], s[64:65] op_sel:[0,1] op_sel_hi:[1,1]\n"
"v_pk_add_f32 v[38:39], v[38:39], s[64:65] op_sel:[0,1] op_sel_hi:[1,1]\n"
"v_rcp_f32 v36, v36\n"
"v_rcp_f32 v37, v37\n"
"v_rcp_f32 v38, v38\n"
"v_rcp_f32 v39, v39\n"
"v_pk_mul_f32 v[156:157], v[156:157], v[36:37]\n"
"v_pk_mul_f32 v[158:159], v[158:159], v[38:39]\n"
"v_cvt_pk_bf16_f32 v46, v156, v157\n"
"v_cvt_pk_bf16_f32 v47, v158, v159\n"
"buffer_store_dwordx4 v[44:47], v5, s[48:51], s86 offen offset:512 sc1\n"
"v_pk_mul_f32 v[36:37], v[160:161], s[64:65] op_sel_hi:[1,0]\n"
"s_add_u32 s7, s7, 1024\n"
"buffer_load_dwordx4 a[196:199], v6, s[48:51], s7 offen offset:3072 sc1\n"
"v_pk_mul_f32 v[38:39], v[162:163], s[64:65] op_sel_hi:[1,0]\n"
"v_exp_f32 v36, v36\n"
"v_exp_f32 v37, v37\n"
"v_pk_mul_f32 v[164:165], v[160:161], v[164:165]\n"
"v_exp_f32 v38, v38\n"
"v_exp_f32 v39, v39\n"
"v_pk_mul_f32 v[166:167], v[162:163], v[166:167]\n"
"v_pk_add_f32 v[36:37], v[36:37], s[64:65] op_sel:[0,1] op_sel_hi:[1,1]\n"
"v_pk_add_f32 v[38:39], v[38:39], s[64:65] op_sel:[0,1] op_sel_hi:[1,1]\n"
"v_rcp_f32 v36, v36\n"
"v_rcp_f32 v37, v37\n"
"v_rcp_f32 v38, v38\n"
"v_rcp_f32 v39, v39\n"
"v_pk_mul_f32 v[164:165], v[164:165], v[36:37]\n"
"v_pk_mul_f32 v[166:167], v[166:167], v[38:39]\n"
"v_cvt_pk_bf16_f32 v40, v164, v165\n"
"v_cvt_pk_bf16_f32 v41, v166, v167\n"
"v_pk_mul_f32 v[36:37], v[168:169], s[64:65] op_sel_hi:[1,0]\n"
"v_pk_mul_f32 v[38:39], v[170:171], s[64:65] op_sel_hi:[1,0]\n"
"s_add_u32 s7, s13, 0\n"
"buffer_load_dwordx4 a[200:203], v6, s[48:51], s7 offen offset:0 sc1\n"
"v_exp_f32 v36, v36\n"
"v_exp_f32 v37, v37\n"
"v_pk_mul_f32 v[172:173], v[168:169], v[172:173]\n"
"v_exp_f32 v38, v38\n"
"v_exp_f32 v39, v39\n"
"v_pk_mul_f32 v[174:175], v[170:171], v[174:175]\n"
"v_pk_add_f32 v[36:37], v[36:37], s[64:65] op_sel:[0,1] op_sel_hi:[1,1]\n"
"v_pk_add_f32 v[38:39], v[38:39], s[64:65] op_sel:[0,1] op_sel_hi:[1,1]\n"
"v_rcp_f32 v36, v36\n"
"v_rcp_f32 v37, v37\n"
"v_rcp_f32 v38, v38\n"
"v_rcp_f32 v39, v39\n"
"v_pk_mul_f32 v[172:173], v[172:173], v[36:37]\n"
"v_pk_mul_f32 v[174:175], v[174:175], v[38:39]\n"
"v_cvt_pk_bf16_f32 v42, v172, v173\n"
"v_cvt_pk_bf16_f32 v43, v174, v175\n"
"buffer_store_dwordx4 v[40:43], v5, s[48:51], s86 offen offset:1024 sc1\n"
"v_pk_mul_f32 v[36:37], v[176:177], s[64:65] op_sel_hi:[1,0]\n"
"buffer_load_dwordx4 a[204:207], v6, s[48:51], s7 offen offset:1024 sc1\n"
"v_pk_mul_f32 v[38:39], v[178:179], s[64:65] op_sel_hi:[1,0]\n"
"v_exp_f32 v36, v36\n"
"v_exp_f32 v37, v37\n"
"v_pk_mul_f32 v[180:181], v[176:177], v[180:181]\n"
"v_exp_f32 v38, v38\n"
"v_exp_f32 v39, v39\n"
"v_pk_mul_f32 v[182:183], v[178:179], v[182:183]\n"
"v_pk_add_f32 v[36:37], v[36:37], s[64:65] op_sel:[0,1] op_sel_hi:[1,1]\n"
"v_pk_add_f32 v[38:39], v[38:39], s[64:65] op_sel:[0,1] op_sel_hi:[1,1]\n"
"v_rcp_f32 v36, v36\n"
"v_rcp_f32 v37, v37\n"
"v_rcp_f32 v38, v38\n"
"v_rcp_f32 v39, v39\n"
"v_pk_mul_f32 v[180:181], v[180:181], v[36:37]\n"
"v_pk_mul_f32 v[182:183], v[182:183], v[38:39]\n"
"v_cvt_pk_bf16_f32 v44, v180, v181\n"
"v_cvt_pk_bf16_f32 v45, v182, v183\n"
"v_pk_mul_f32 v[36:37], v[184:185], s[64:65] op_sel_hi:[1,0]\n"
"v_pk_mul_f32 v[38:39], v[186:187], s[64:65] op_sel_hi:[1,0]\n"
"buffer_load_dwordx4 a[208:211], v6, s[48:51], s7 offen offset:2048 sc1\n"
"v_exp_f32 v36, v36\n"
"v_exp_f32 v37, v37\n"
"v_pk_mul_f32 v[188:189], v[184:185], v[188:189]\n"
"v_exp_f32 v38, v38\n"
"v_exp_f32 v39, v39\n"
"v_pk_mul_f32 v[190:191], v[186:187], v[190:191]\n"
"v_pk_add_f32 v[36:37], v[36:37], s[64:65] op_sel:[0,1] op_sel_hi:[1,1]\n"
"v_pk_add_f32 v[38:39], v[38:39], s[64:65] op_sel:[0,1] op_sel_hi:[1,1]\n"
"v_rcp_f32 v36, v36\n"
"v_rcp_f32 v37, v37\n"
"v_rcp_f32 v38, v38\n"
"v_rcp_f32 v39, v39\n"
"v_pk_mul_f32 v[188:189], v[188:189], v[36:37]\n"
"v_pk_mul_f32 v[190:191], v[190:191], v[38:39]\n"
"v_cvt_pk_bf16_f32 v46, v188, v189\n"
"v_cvt_pk_bf16_f32 v47, v190, v191\n"
"buffer_store_dwordx4 v[44:47], v5, s[48:51], s86 offen offset:1536 sc1\n"
"s_add_u32 s86, s86, 2048\n"
"s_waitcnt lgkmcnt(4)\n"
"v_mfma_f32_32x32x16_bf16 v[128:143], v[80:83], a[0:3], 0\n"
"ds_read_b64_tr_b16 v[96:97], v20 offset:4096\n"
"ds_read_b64_tr_b16 v[98:99], v20 offset:4608\n"
"buffer_load_dwordx4 a[212:215], v6, s[48:51], s7 offen offset:3072 sc1\n"
"v_mfma_f32_32x32x16_bf16 v[144:159], v[84:87], a[0:3], 0\n"
"ds_read_b64_tr_b16 v[100:101], v21 offset:4096\n"
"ds_read_b64_tr_b16 v[102:103], v21 offset:4608\n"
"buffer_load_dwordx4 v17, s[40:43], s70 offen offset:1024 lds\n"
"v_mul_f32 v36, 0xbfb8aa3b, v48\n"
"v_mul_f32 v37, 0xbfb8aa3b, v49\n"
"v_mul_f32 v38, 0xbfb8aa3b, v50\n"
"v_mul_f32 v39, 0xbfb8aa3b, v51\n"
"s_waitcnt lgkmcnt(4)\n"
"v_mfma_f32_32x32x16_bf16 v[160:175], v[88:91], a[0:3], 0\n"
"ds_read_b64_tr_b16 v[104:105], v22 offset:4096\n"
"ds_read_b64_tr_b16 v[106:107], v22 offset:4608\n"
"v_exp_f32 v36, v36\n"
"v_mul_f32 v52, v48, v52\n"
"v_exp_f32 v37, v37\n"
"v_mul_f32 v53, v49, v53\n"
"v_mfma_f32_32x32x16_bf16 v[176:191], v[92:95], a[0:3], 0\n"
"s_add_u32 s7, s7, 1024\n"
"ds_read_b64_tr_b16 v[108:109], v23 offset:4096\n"
"ds_read_b64_tr_b16 v[110:111], v23 offset:4608\n"
"buffer_load_dwordx4 a[216:219], v6, s[48:51], s7 offen offset:3072 sc1\n"
"v_exp_f32 v38, v38\n"
"s_waitcnt lgkmcnt(4)\n"
"v_mfma_f32_32x32x16_bf16 v[128:143], v[96:99], a[4:7], v[128:143]\n"
"ds_read_b64_tr_b16 v[80:81], v20 offset:8192\n"
"ds_read_b64_tr_b16 v[82:83], v20 offset:8704\n"
"v_mul_f32 v54, v50, v54\n"
"v_exp_f32 v39, v39\n"
"v_mul_f32 v55, v51, v55\n"
"v_add_f32 v36, 1.0, v36\n"
"v_add_f32 v37, 1.0, v37\n"
"v_add_f32 v38, 1.0, v38\n"
"v_mfma_f32_32x32x16_bf16 v[144:159], v[100:103], a[4:7], v[144:159]\n"
"ds_read_b64_tr_b16 v[84:85], v21 offset:8192\n"
"ds_read_b64_tr_b16 v[86:87], v21 offset:8704\n"
"ds_read_b64_tr_b16 v[88:89], v22 offset:8192\n"
"ds_read_b64_tr_b16 v[90:91], v22 offset:8704\n"
"buffer_load_dwordx4 v18, s[44:47], s70 offen offset:2048 lds\n"
"v_add_f32 v39, 1.0, v39\n"
"s_waitcnt lgkmcnt(6)\n"
"v_mfma_f32_32x32x16_bf16 v[160:175], v[104:107], a[4:7], v[160:175]\n"
"ds_read_b64_tr_b16 v[92:93], v23 offset:8192\n"
"ds_read_b64_tr_b16 v[94:95], v23 offset:8704\n"
"v_rcp_f32 v36, v36\n"
"v_rcp_f32 v37, v37\n"
"v_rcp_f32 v38, v38\n"
"v_mfma_f32_32x32x16_bf16 v[176:191], v[108:111], a[4:7], v[176:191]\n"
"s_add_u32 s7, s13, 0\n"
"s_add_u32 s7, s7, s1\n"
"ds_read_b64_tr_b16 v[96:97], v20 offset:12288\n"
"ds_read_b64_tr_b16 v[98:99], v20 offset:12800\n"
"buffer_load_dwordx4 a[220:223], v6, s[48:51], s7 offen offset:0 sc1\n"
"v_rcp_f32 v39, v39\n"
"s_waitcnt lgkmcnt(6)\n"
"v_mfma_f32_32x32x16_bf16 v[128:143], v[80:83], a[8:11], v[128:143]\n"
"ds_read_b64_tr_b16 v[100:101], v21 offset:12288\n"
"ds_read_b64_tr_b16 v[102:103], v21 offset:12800\n"
"v_mul_f32 v52, v52, v36\n"
"v_mul_f32 v53, v53, v37\n"
"v_mul_f32 v54, v54, v38\n"
"v_mul_f32 v55, v55, v39\n"
"v_cvt_pk_bf16_f32 v40, v52, v53\n"
"v_cvt_pk_bf16_f32 v41, v54, v55\n"
"v_mfma_f32_32x32x16_bf16 v[144:159], v[84:87], a[8:11], v[144:159]\n"
"ds_read_b64_tr_b16 v[104:105], v22 offset:12288\n"
"ds_read_b64_tr_b16 v[106:107], v22 offset:12800\n"
"buffer_load_dwordx4 v19, s[44:47], s70 offen offset:3072 lds\n"
"v_mul_f32 v36, 0xbfb8aa3b, v56\n"
"v_mul_f32 v37, 0xbfb8aa3b, v57\n"
"v_mul_f32 v38, 0xbfb8aa3b, v58\n"
"v_mul_f32 v39, 0xbfb8aa3b, v59\n"
"s_waitcnt lgkmcnt(6)\n"
"v_mfma_f32_32x32x16_bf16 v[160:175], v[88:91], a[8:11], v[160:175]\n"
"ds_read_b64_tr_b16 v[108:109], v23 offset:12288\n"
"ds_read_b64_tr_b16 v[110:111], v23 offset:12800\n"
"v_exp_f32 v36, v36\n"
"v_mul_f32 v60, v56, v60\n"
"v_exp_f32 v37, v37\n"
"v_mul_f32 v61, v57, v61\n"
"v_exp_f32 v38, v38\n"
"v_mfma_f32_32x32x16_bf16 v[176:191], v[92:95], a[8:11], v[176:191]\n"
"ds_read_b64_tr_b16 v[80:81], v20 offset:16384\n"
"ds_read_b64_tr_b16 v[82:83], v20 offset:16896\n"
"buffer_load_dwordx4 a[224:227], v6, s[48:51], s7 offen offset:1024 sc1\n"
"v_mul_f32 v62, v58, v62\n"
"v_exp_f32 v39, v39\n"
"s_waitcnt lgkmcnt(6)\n"
"v_mfma_f32_32x32x16_bf16 v[128:143], v[96:99], a[12:15], v[128:143]\n"
"ds_read_b64_tr_b16 v[84:85], v21 offset:16384\n"
"ds_read_b64_tr_b16 v[86:87], v21 offset:16896\n"
"v_mul_f32 v63, v59, v63\n"
"v_add_f32 v36, 1.0, v36\n"
"v_add_f32 v37, 1.0, v37\n"
"v_add_f32 v38, 1.0, v38\n"
"v_add_f32 v39, 1.0, v39\n"
"v_rcp_f32 v36, v36\n"
"v_mfma_f32_32x32x16_bf16 v[144:159], v[100:103], a[12:15], v[144:159]\n"
"s_add_u32 m0, s81, 0x4000\n"
"ds_read_b64_tr_b16 v[88:89], v22 offset:16384\n"
"ds_read_b64_tr_b16 v[90:91], v22 offset:16896\n"
"buffer_load_dwordx4 v16, s[40:43], s71 offen lds\n"
"v_rcp_f32 v37, v37\n"
"s_waitcnt lgkmcnt(6)\n"
"v_mfma_f32_32x32x16_bf16 v[160:175], v[104:107], a[12:15], v[160:175]\n"
"ds_read_b64_tr_b16 v[92:93], v23 offset:16384\n"
"ds_read_b64_tr_b16 v[94:95], v23 offset:16896\n"
"v_rcp_f32 v38, v38\n"
"v_rcp_f32 v39, v39\n"
"v_mul_f32 v60, v60, v36\n"
"v_mul_f32 v61, v61, v37\n"
"v_mfma_f32_32x32x16_bf16 v[176:191], v[108:111], a[12:15], v[176:191]\n"
"ds_read_b64_tr_b16 v[96:97], v20 offset:20480\n"
"ds_read_b64_tr_b16 v[98:99], v20 offset:20992\n"
"ds_read_b64_tr_b16 v[100:101], v21 offset:20480\n"
"ds_read_b64_tr_b16 v[102:103], v21 offset:20992\n"
"buffer_load_dwordx4 a[228:231], v6, s[48:51], s7 offen offset:2048 sc1\n"
"v_mul_f32 v62, v62, v38\n"
"s_waitcnt lgkmcnt(8)\n"
"v_mfma_f32_32x32x16_bf16 v[128:143], v[80:83], a[16:19], v[128:143]\n"
"ds_read_b64_tr_b16 v[104:105], v22 offset:20480\n"
"ds_read_b64_tr_b16 v[106:107], v22 offset:20992\n"
"v_mul_f32 v63, v63, v39\n"
"v_cvt_pk_bf16_f32 v42, v60, v61\n"
"v_cvt_pk_bf16_f32 v43, v62, v63\n"
"buffer_store_dwordx4 v[40:43], v5, s[48:51], s98 offen offset:0 sc1\n"
"v_mul_f32 v36, 0xbfb8aa3b, v64\n"
"v_mul_f32 v37, 0xbfb8aa3b, v65\n"
"v_mfma_f32_32x32x16_bf16 v[144:159], v[84:87], a[16:19], v[144:159]\n"
"ds_read_b64_tr_b16 v[108:109], v23 offset:20480\n"
"ds_read_b64_tr_b16 v[110:111], v23 offset:20992\n"
"buffer_load_dwordx4 v17, s[40:43], s71 offen offset:1024 lds\n"
"v_mul_f32 v38, 0xbfb8aa3b, v66\n"
"v_mul_f32 v39, 0xbfb8aa3b, v67\n"
"v_exp_f32 v36, v36\n"
"s_waitcnt lgkmcnt(8)\n"
"v_mfma_f32_32x32x16_bf16 v[160:175], v[88:91], a[16:19], v[160:175]\n"
"ds_read_b64_tr_b16 v[80:81], v20 offset:24576\n"
"ds_read_b64_tr_b16 v[82:83], v20 offset:25088\n"
"v_mul_f32 v68, v64, v68\n"
"v_exp_f32 v37, v37\n"
"v_mul_f32 v69, v65, v69\n"
"v_exp_f32 v38, v38\n"
"v_mul_f32 v70, v66, v70\n"
"v_mfma_f32_32x32x16_bf16 v[176:191], v[92:95], a[16:19], v[176:191]\n"
"ds_read_b64_tr_b16 v[84:85], v21 offset:24576\n"
"ds_read_b64_tr_b16 v[86:87], v21 offset:25088\n"
"buffer_load_dwordx4 a[232:235], v6, s[48:51], s7 offen offset:3072 sc1\n"
"v_exp_f32 v39, v39\n"
"v_mul_f32 v71, v67, v71\n"
"v_add_f32 v36, 1.0, v36\n"
"s_waitcnt lgkmcnt(8)\n"
"v_mfma_f32_32x32x16_bf16 v[128:143], v[96:99], a[20:23], v[128:143]\n"
"s_add_u32 s7, s7, 1024\n"
"ds_read_b64_tr_b16 v[88:89], v22 offset:24576\n"
"ds_read_b64_tr_b16 v[90:91], v22 offset:25088\n"
"buffer_load_dwordx4 a[236:239], v6, s[48:51], s7 offen offset:3072 sc1\n"
"v_mfma_f32_32x32x16_bf16 v[144:159], v[100:103], a[20:23], v[144:159]\n"
"ds_read_b64_tr_b16 v[92:93], v23 offset:24576\n"
"ds_read_b64_tr_b16 v[94:95], v23 offset:25088\n"
"buffer_load_dwordx4 v18, s[44:47], s71 offen offset:2048 lds\n"
"v_add_f32 v37, 1.0, v37\n"
"v_add_f32 v38, 1.0, v38\n"
"v_add_f32 v39, 1.0, v39\n"
"v_rcp_f32 v36, v36\n"
"s_waitcnt lgkmcnt(8)\n"
"v_mfma_f32_32x32x16_bf16 v[160:175], v[104:107], a[20:23], v[160:175]\n"
"ds_read_b64_tr_b16 v[96:97], v20 offset:28672\n"
"ds_read_b64_tr_b16 v[98:99], v20 offset:29184\n"
"v_rcp_f32 v37, v37\n"
"v_rcp_f32 v38, v38\n"
"v_rcp_f32 v39, v39\n"
"v_mfma_f32_32x32x16_bf16 v[176:191], v[108:111], a[20:23], v[176:191]\n"
"ds_read_b64_tr_b16 v[100:101], v21 offset:28672\n"
"ds_read_b64_tr_b16 v[102:103], v21 offset:29184\n"
"v_mul_f32 v68, v68, v36\n"
"v_mul_f32 v69, v69, v37\n"
"v_mul_f32 v70, v70, v38\n"
"v_mul_f32 v71, v71, v39\n"
"v_cvt_pk_bf16_f32 v44, v68, v69\n"
"v_cvt_pk_bf16_f32 v45, v70, v71\n"
"v_mul_f32 v36, 0xbfb8aa3b, v72\n"
"v_mul_f32 v37, 0xbfb8aa3b, v73\n"
"v_mul_f32 v38, 0xbfb8aa3b, v74\n"
"v_mul_f32 v39, 0xbfb8aa3b, v75\n"
"s_waitcnt lgkmcnt(8)\n"
"v_mfma_f32_32x32x16_bf16 v[128:143], v[80:83], a[24:27], v[128:143]\n"
"s_add_u32 s7, s4, 10240\n"
"ds_read_b64_tr_b16 v[104:105], v22 offset:28672\n"
"ds_read_b64_tr_b16 v[106:107], v22 offset:29184\n"
"buffer_load_dwordx4 a[240:243], v6, s[48:51], s7 offen offset:0 sc1\n"
"v_exp_f32 v36, v36\n"
"v_mfma_f32_32x32x16_bf16 v[144:159], v[84:87], a[24:27], v[144:159]\n"
"ds_read_b64_tr_b16 v[108:109], v23 offset:28672\n"
"ds_read_b64_tr_b16 v[110:111], v23 offset:29184\n"
"buffer_load_dwordx4 v19, s[44:47], s71 offen offset:3072 lds\n"
"v_mul_f32 v76, v72, v76\n"
"v_exp_f32 v37, v37\n"
"s_waitcnt lgkmcnt(8)\n"
"v_mfma_f32_32x32x16_bf16 v[160:175], v[88:91], a[24:27], v[160:175]\n"
"v_add_u32 v20, 0xa000, v20\n"
"v_add_u32 v21, 0xa000, v21\n"
"v_mul_f32 v77, v73, v77\n"
"v_exp_f32 v38, v38\n"
"v_mul_f32 v78, v74, v78\n"
"v_exp_f32 v39, v39\n"
"v_mul_f32 v79, v75, v79\n"
"v_mfma_f32_32x32x16_bf16 v[176:191], v[92:95], a[24:27], v[176:191]\n"
"v_add_u32 v22, 0xa000, v22\n"
"v_add_u32 v23, 0xa000, v23\n"
"buffer_load_dwordx4 a[244:247], v6, s[48:51], s7 offen offset:1024 sc1\n"
"v_add_f32 v36, 1.0, v36\n"
"v_add_f32 v37, 1.0, v37\n"
"v_add_f32 v38, 1.0, v38\n"
"v_add_f32 v39, 1.0, v39\n"
"s_waitcnt lgkmcnt(4)\n"
"v_mfma_f32_32x32x16_bf16 v[128:143], v[96:99], a[28:31], v[128:143]\n"
"s_waitcnt vmcnt(17) lgkmcnt(0)\n"
"s_barrier\n"
"ds_read_b64_tr_b16 v[80:81], v20 offset:0\n"
"ds_read_b64_tr_b16 v[82:83], v20 offset:512\n"
"v_mfma_f32_32x32x16_bf16 v[144:159], v[100:103], a[28:31], v[144:159]\n"
"ds_read_b64_tr_b16 v[84:85], v21 offset:0\n"
"ds_read_b64_tr_b16 v[86:87], v21 offset:512\n"
"buffer_store_dwordx2 v[2:3], v4, s[48:51], s87 offen offset:0 sc1\n"
"v_rcp_f32 v36, v36\n"
"v_rcp_f32 v37, v37\n"
"v_mfma_f32_32x32x16_bf16 v[160:175], v[104:107], a[28:31], v[160:175]\n"
"ds_read_b64_tr_b16 v[88:89], v22 offset:0\n"
"ds_read_b64_tr_b16 v[90:91], v22 offset:512\n"
"buffer_load_dwordx4 a[248:251], v6, s[48:51], s7 offen offset:2048 sc1\n"
"v_rcp_f32 v38, v38\n"
"v_mfma_f32_32x32x16_bf16 v[176:191], v[108:111], a[28:31], v[176:191]\n"
"s_add_u32 m0, s81, 0xa000\n"
"ds_read_b64_tr_b16 v[92:93], v23 offset:0\n"
"ds_read_b64_tr_b16 v[94:95], v23 offset:512\n"
"buffer_load_dwordx4 v16, s[40:43], s72 offen lds\n"
"s_waitcnt lgkmcnt(4)\n"
"v_mfma_f32_32x32x16_bf16 v[128:143], v[80:83], a[32:35], v[128:143]\n"
"ds_read_b64_tr_b16 v[96:97], v20 offset:4096\n"
"ds_read_b64_tr_b16 v[98:99], v20 offset:4608\n"
"v_rcp_f32 v39, v39\n"
"v_mul_f32 v76, v76, v36\n"
"v_mul_f32 v77, v77, v37\n"
"v_mul_f32 v78, v78, v38\n"
"v_mul_f32 v79, v79, v39\n"
"v_cvt_pk_bf16_f32 v46, v76, v77\n"
"v_mfma_f32_32x32x16_bf16 v[144:159], v[84:87], a[32:35], v[144:159]\n"
"ds_read_b64_tr_b16 v[100:101], v21 offset:4096\n"
"ds_read_b64_tr_b16 v[102:103], v21 offset:4608\n"
"buffer_load_dwordx4 v17, s[40:43], s72 offen offset:1024 lds\n"
"v_cvt_pk_bf16_f32 v47, v78, v79\n"
"buffer_store_dwordx4 v[44:47], v5, s[48:51], s98 offen offset:512 sc1\n"
"v_mul_f32 v36, 0xbfb8aa3b, v192\n"
"s_waitcnt lgkmcnt(4)\n"
"v_mfma_f32_32x32x16_bf16 v[160:175], v[88:91], a[32:35], v[160:175]\n"
"ds_read_b64_tr_b16 v[104:105], v22 offset:4096\n"
"ds_read_b64_tr_b16 v[106:107], v22 offset:4608\n"
"v_mul_f32 v37, 0xbfb8aa3b, v193\n"
"v_mul_f32 v38, 0xbfb8aa3b, v194\n"
"v_mul_f32 v39, 0xbfb8aa3b, v195\n"
"v_exp_f32 v36, v36\n"
"v_mul_f32 v196, v192, v196\n"
"v_exp_f32 v37, v37\n"
"v_mfma_f32_32x32x16_bf16 v[176:191], v[92:95], a[32:35], v[176:191]\n"
"ds_read_b64_tr_b16 v[108:109], v23 offset:4096\n"
"ds_read_b64_tr_b16 v[110:111], v23 offset:4608\n"
"buffer_load_dwordx4 a[252:255], v6, s[48:51], s7 offen offset:3072 sc1\n"
"v_mul_f32 v197, v193, v197\n"
"v_exp_f32 v38, v38\n"
"s_waitcnt lgkmcnt(4)\n"
"v_mfma_f32_32x32x16_bf16 v[128:143], v[96:99], a[36:39], v[128:143]\n"
"ds_read_b64_tr_b16 v[80:81], v20 offset:8192\n"
"ds_read_b64_tr_b16 v[82:83], v20 offset:8704\n"
"v_mul_f32 v198, v194, v198\n"
"v_exp_f32 v39, v39\n"
"v_mul_f32 v199, v195, v199\n"
"v_add_f32 v36, 1.0, v36\n"
"v_add_f32 v37, 1.0, v37\n"
"v_add_f32 v38, 1.0, v38\n"
"v_mfma_f32_32x32x16_bf16 v[144:159], v[100:103], a[36:39], v[144:159]\n"
"ds_read_b64_tr_b16 v[84:85], v21 offset:8192\n"
"ds_read_b64_tr_b16 v[86:87], v21 offset:8704\n"
"ds_read_b64_tr_b16 v[88:89], v22 offset:8192\n"
"ds_read_b64_tr_b16 v[90:91], v22 offset:8704\n"
"buffer_load_dwordx4 v18, s[44:47], s72 offen offset:2048 lds\n"
"v_add_f32 v39, 1.0, v39\n"
"s_waitcnt lgkmcnt(6)\n"
"v_mfma_f32_32x32x16_bf16 v[160:175], v[104:107], a[36:39], v[160:175]\n"
"ds_read_b64_tr_b16 v[92:93], v23 offset:8192\n"
"ds_read_b64_tr_b16 v[94:95], v23 offset:8704\n"
"v_rcp_f32 v36, v36\n"
"v_rcp_f32 v37, v37\n"
"v_rcp_f32 v38, v38\n"
"v_rcp_f32 v39, v39\n"
"v_mfma_f32_32x32x16_bf16 v[176:191], v[108:111], a[36:39], v[176:191]\n"
"s_add_u32 s7, s7, 1024\n"
"ds_read_b64_tr_b16 v[96:97], v20 offset:12288\n"
"ds_read_b64_tr_b16 v[98:99], v20 offset:12800\n"
"buffer_load_dwordx4 v[112:115], v6, s[48:51], s7 offen offset:3072 sc1\n"
"s_waitcnt lgkmcnt(6)\n"
"v_mfma_f32_32x32x16_bf16 v[128:143], v[80:83], a[40:43], v[128:143]\n"
"ds_read_b64_tr_b16 v[100:101], v21 offset:12288\n"
"ds_read_b64_tr_b16 v[102:103], v21 offset:12800\n"
"v_mul_f32 v196, v196, v36\n"
"v_mul_f32 v197, v197, v37\n"
"v_mul_f32 v198, v198, v38\n"
"v_mul_f32 v199, v199, v39\n"
"v_cvt_pk_bf16_f32 v40, v196, v197\n"
"v_cvt_pk_bf16_f32 v41, v198, v199\n"
"v_mul_f32 v36, 0xbfb8aa3b, v200\n"
"v_mfma_f32_32x32x16_bf16 v[144:159], v[84:87], a[40:43], v[144:159]\n"
"ds_read_b64_tr_b16 v[104:105], v22 offset:12288\n"
"ds_read_b64_tr_b16 v[106:107], v22 offset:12800\n"
"buffer_load_dwordx4 v19, s[44:47], s72 offen offset:3072 lds\n"
"v_mul_f32 v37, 0xbfb8aa3b, v201\n"
"v_mul_f32 v38, 0xbfb8aa3b, v202\n"
"v_mul_f32 v39, 0xbfb8aa3b, v203\n"
"s_waitcnt lgkmcnt(6)\n"
"v_mfma_f32_32x32x16_bf16 v[160:175], v[88:91], a[40:43], v[160:175]\n"
"ds_read_b64_tr_b16 v[108:109], v23 offset:12288\n"
"ds_read_b64_tr_b16 v[110:111], v23 offset:12800\n"
"v_exp_f32 v36, v36\n"
"v_mul_f32 v204, v200, v204\n"
"v_exp_f32 v37, v37\n"
"v_mul_f32 v205, v201, v205\n"
"v_exp_f32 v38, v38\n"
"v_mfma_f32_32x32x16_bf16 v[176:191], v[92:95], a[40:43], v[176:191]\n"
"s_add_u32 s7, s4, 10240\n"
"s_add_u32 s7, s7, s1\n"
"ds_read_b64_tr_b16 v[80:81], v20 offset:16384\n"
"ds_read_b64_tr_b16 v[82:83], v20 offset:16896\n"
"buffer_load_dwordx4 v[116:119], v6, s[48:51], s7 offen offset:0 sc1\n"
"v_mul_f32 v206, v202, v206\n"
"s_waitcnt lgkmcnt(6)\n"
"v_mfma_f32_32x32x16_bf16 v[128:143], v[96:99], a[44:47], v[128:143]\n"
"ds_read_b64_tr_b16 v[84:85], v21 offset:16384\n"
"ds_read_b64_tr_b16 v[86:87], v21 offset:16896\n"
"buffer_load_dwordx4 v[120:123], v6, s[48:51], s7 offen offset:1024 sc1\n"
"v_mfma_f32_32x32x16_bf16 v[144:159], v[100:103], a[44:47], v[144:159]\n"
"s_add_u32 m0, s81, 0xe000\n"
"ds_read_b64_tr_b16 v[88:89], v22 offset:16384\n"
"ds_read_b64_tr_b16 v[90:91], v22 offset:16896\n"
"buffer_load_dwordx4 v16, s[40:43], s73 offen lds\n"
"v_exp_f32 v39, v39\n"
"s_waitcnt lgkmcnt(6)\n"
"v_mfma_f32_32x32x16_bf16 v[160:175], v[104:107], a[44:47], v[160:175]\n"
"ds_read_b64_tr_b16 v[92:93], v23 offset:16384\n"
"ds_read_b64_tr_b16 v[94:95], v23 offset:16896\n"
"v_mul_f32 v207, v203, v207\n"
"v_add_f32 v36, 1.0, v36\n"
"v_add_f32 v37, 1.0, v37\n"
"v_add_f32 v38, 1.0, v38\n"
"v_add_f32 v39, 1.0, v39\n"
"v_rcp_f32 v36, v36\n"
"v_mfma_f32_32x32x16_bf16 v[176:191], v[108:111], a[44:47], v[176:191]\n"
"ds_read_b64_tr_b16 v[96:97], v20 offset:20480\n"
"ds_read_b64_tr_b16 v[98:99], v20 offset:20992\n"
"ds_read_b64_tr_b16 v[100:101], v21 offset:20480\n"
"ds_read_b64_tr_b16 v[102:103], v21 offset:20992\n"
"v_rcp_f32 v37, v37\n"
"v_rcp_f32 v38, v38\n"
"s_waitcnt lgkmcnt(8)\n"
"v_mfma_f32_32x32x16_bf16 v[128:143], v[80:83], a[48:51], v[128:143]\n"
"ds_read_b64_tr_b16 v[104:105], v22 offset:20480\n"
"ds_read_b64_tr_b16 v[106:107], v22 offset:20992\n"
"buffer_load_dwordx4 v[124:127], v6, s[48:51], s7 offen offset:2048 sc1\n"
"v_mfma_f32_32x32x16_bf16 v[144:159], v[84:87], a[48:51], v[144:159]\n"
"ds_read_b64_tr_b16 v[108:109], v23 offset:20480\n"
"ds_read_b64_tr_b16 v[110:111], v23 offset:20992\n"
"buffer_load_dwordx4 v17, s[40:43], s73 offen offset:1024 lds\n"
"v_rcp_f32 v39, v39\n"
"v_mul_f32 v204, v204, v36\n"
"v_mul_f32 v205, v205, v37\n"
"s_waitcnt lgkmcnt(8)\n"
"v_mfma_f32_32x32x16_bf16 v[160:175], v[88:91], a[48:51], v[160:175]\n"
"ds_read_b64_tr_b16 v[80:81], v20 offset:24576\n"
"ds_read_b64_tr_b16 v[82:83], v20 offset:25088\n"
"v_mul_f32 v206, v206, v38\n"
"v_mul_f32 v207, v207, v39\n"
"v_cvt_pk_bf16_f32 v42, v204, v205\n"
"v_cvt_pk_bf16_f32 v43, v206, v207\n"
"buffer_store_dwordx4 v[40:43], v5, s[48:51], s86 offen offset:0 sc1\n"
"v_mul_f32 v36, 0xbfb8aa3b, v208\n"
"v_mfma_f32_32x32x16_bf16 v[176:191], v[92:95], a[48:51], v[176:191]\n"
"s_add_u32 s7, s5, 0\n"
"s_add_u32 m0, s96, 0x0\n"
"ds_read_b64_tr_b16 v[84:85], v21 offset:24576\n"
"ds_read_b64_tr_b16 v[86:87], v21 offset:25088\n"
"buffer_load_dwordx4 v6, s[48:51], s7 offen sc1 lds\n"
"v_mul_f32 v37, 0xbfb8aa3b, v209\n"
"s_waitcnt lgkmcnt(8)\n"
"v_mfma_f32_32x32x16_bf16 v[128:143], v[96:99], a[52:55], v[128:143]\n"
"ds_read_b64_tr_b16 v[88:89], v22 offset:24576\n"
"ds_read_b64_tr_b16 v[90:91], v22 offset:25088\n"
"v_mul_f32 v38, 0xbfb8aa3b, v210\n"
"v_mul_f32 v39, 0xbfb8aa3b, v211\n"
"v_exp_f32 v36, v36\n"
"v_mul_f32 v212, v208, v212\n"
"v_exp_f32 v37, v37\n"
"v_mfma_f32_32x32x16_bf16 v[144:159], v[100:103], a[52:55], v[144:159]\n"
"s_add_u32 m0, s81, 0xe000\n"
"ds_read_b64_tr_b16 v[92:93], v23 offset:24576\n"
"ds_read_b64_tr_b16 v[94:95], v23 offset:25088\n"
"buffer_load_dwordx4 v18, s[44:47], s73 offen offset:2048 lds\n"
"v_mul_f32 v213, v209, v213\n"
"v_exp_f32 v38, v38\n"
"v_mul_f32 v214, v210, v214\n"
"s_waitcnt lgkmcnt(8)\n"
"v_mfma_f32_32x32x16_bf16 v[160:175], v[104:107], a[52:55], v[160:175]\n"
"ds_read_b64_tr_b16 v[96:97], v20 offset:28672\n"
"ds_read_b64_tr_b16 v[98:99], v20 offset:29184\n"
"v_exp_f32 v39, v39\n"
"v_mul_f32 v215, v211, v215\n"
"v_add_f32 v36, 1.0, v36\n"
"v_add_f32 v37, 1.0, v37\n"
"v_add_f32 v38, 1.0, v38\n"
"v_add_f32 v39, 1.0, v39\n"
"v_mfma_f32_32x32x16_bf16 v[176:191], v[108:111], a[52:55], v[176:191]\n"
"s_add_u32 m0, s96, 0x0\n"
"ds_read_b64_tr_b16 v[100:101], v21 offset:28672\n"
"ds_read_b64_tr_b16 v[102:103], v21 offset:29184\n"
"buffer_load_dwordx4 v6, s[48:51], s7 offen offset:1024 sc1 lds\n"
"s_waitcnt lgkmcnt(8)\n"
"v_mfma_f32_32x32x16_bf16 v[128:143], v[80:83], a[56:59], v[128:143]\n"
"ds_read_b64_tr_b16 v[104:105], v22 offset:28672\n"
"ds_read_b64_tr_b16 v[106:107], v22 offset:29184\n"
"v_rcp_f32 v36, v36\n"
"v_rcp_f32 v37, v37\n"
"v_rcp_f32 v38, v38\n"
"v_rcp_f32 v39, v39\n"
"v_mfma_f32_32x32x16_bf16 v[144:159], v[84:87], a[56:59], v[144:159]\n"
"s_add_u32 m0, s81, 0xe000\n"
"ds_read_b64_tr_b16 v[108:109], v23 offset:28672\n"
"ds_read_b64_tr_b16 v[110:111], v23 offset:29184\n"
"buffer_load_dwordx4 v19, s[44:47], s73 offen offset:3072 lds\n"
"v_mul_f32 v212, v212, v36\n"
"v_mul_f32 v213, v213, v37\n"
"v_mul_f32 v214, v214, v38\n"
"s_waitcnt lgkmcnt(8)\n"
"v_mfma_f32_32x32x16_bf16 v[160:175], v[88:91], a[56:59], v[160:175]\n"
"v_subrev_u32 v20, 0x14000, v20\n"
"v_subrev_u32 v21, 0x14000, v21\n"
"v_mul_f32 v215, v215, v39\n"
"v_cvt_pk_bf16_f32 v44, v212, v213\n"
"v_cvt_pk_bf16_f32 v45, v214, v215\n"
"v_mul_f32 v36, 0xbfb8aa3b, v216\n"
"v_mul_f32 v37, 0xbfb8aa3b, v217\n"
"v_mul_f32 v38, 0xbfb8aa3b, v218\n"
"v_mul_f32 v39, 0xbfb8aa3b, v219\n"
"v_mfma_f32_32x32x16_bf16 v[176:191], v[92:95], a[56:59], v[176:191]\n"
"s_add_u32 m0, s96, 0x0\n"
"v_subrev_u32 v22, 0x14000, v22\n"
"v_subrev_u32 v23, 0x14000, v23\n"
"buffer_load_dwordx4 v6, s[48:51], s7 offen offset:2048 sc1 lds\n"
"v_exp_f32 v36, v36\n"
"s_waitcnt lgkmcnt(4)\n"
"v_mfma_f32_32x32x16_bf16 v[128:143], v[96:99], a[60:63], v[128:143]\n"
"s_waitcnt vmcnt(21) lgkmcnt(0)\n"
"s_barrier\n"
"ds_read_b64_tr_b16 v[80:81], v20 offset:0\n"
"ds_read_b64_tr_b16 v[82:83], v20 offset:512\n"
"v_mfma_f32_32x32x16_bf16 v[144:159], v[100:103], a[60:63], v[144:159]\n"
"ds_read_b64_tr_b16 v[84:85], v21 offset:0\n"
"ds_read_b64_tr_b16 v[86:87], v21 offset:512\n"
"buffer_load_dwordx4 v6, s[48:51], s7 offen offset:3072 sc1 lds\n"
"v_mul_f32 v220, v216, v220\n"
"v_mfma_f32_32x32x16_bf16 v[160:175], v[104:107], a[60:63], v[160:175]\n"
"ds_read_b64_tr_b16 v[88:89], v22 offset:0\n"
"ds_read_b64_tr_b16 v[90:91], v22 offset:512\n"
"s_lshl_b32 s82, s77, 9\n"
"s_add_u32 s2, s82, 1280\n"
"s_mov_b32 s94, 4\n"
"s_mov_b32 s95, 0\n"
"v_lshlrev_b32 v33, 3, v1\n"
"v_cmp_gt_u32 vcc, 4, v1\n"
"v_mov_b32 v34, 0x40000000\n"
"s_nop 0\n"
"v_cndmask_b32 v33, v34, v33, vcc\n"
"buffer_load_dwordx2 v[8:9], v33, s[48:51], s2 offen sc1\n"
"v_mfma_f32_32x32x16_bf16 v[176:191], v[108:111], a[60:63], v[176:191]\n"
"s_add_u32 m0, s81, 0x14000\n"
"ds_read_b64_tr_b16 v[92:93], v23 offset:0\n"
"ds_read_b64_tr_b16 v[94:95], v23 offset:512\n"
"buffer_load_dwordx4 v16, s[40:43], s74 offen lds\n"
"v_exp_f32 v37, v37\n"
"s_waitcnt lgkmcnt(4)\n"
"v_mfma_f32_32x32x16_bf16 v[128:143], v[80:83], a[64:67], v[128:143]\n"
"ds_read_b64_tr_b16 v[96:97], v20 offset:4096\n"
"ds_read_b64_tr_b16 v[98:99], v20 offset:4608\n"
"v_mul_f32 v221, v217, v221\n"
"v_exp_f32 v38, v38\n"
"v_mul_f32 v222, v218, v222\n"
"v_exp_f32 v39, v39\n"
"v_mfma_f32_32x32x16_bf16 v[144:159], v[84:87], a[64:67], v[144:159]\n"
"ds_read_b64_tr_b16 v[100:101], v21 offset:4096\n"
"ds_read_b64_tr_b16 v[102:103], v21 offset:4608\n"
"buffer_load_dwordx4 v17, s[40:43], s74 offen offset:1024 lds\n"
"v_mul_f32 v223, v219, v223\n"
"v_add_f32 v36, 1.0, v36\n"
"v_add_f32 v37, 1.0, v37\n"
"v_add_f32 v38, 1.0, v38\n"
"s_waitcnt lgkmcnt(4)\n"
"v_mfma_f32_32x32x16_bf16 v[160:175], v[88:91], a[64:67], v[160:175]\n"
"ds_read_b64_tr_b16 v[104:105], v22 offset:4096\n"
"ds_read_b64_tr_b16 v[106:107], v22 offset:4608\n"
"v_add_f32 v39, 1.0, v39\n"
"v_rcp_f32 v36, v36\n"
"v_rcp_f32 v37, v37\n"
"v_rcp_f32 v38, v38\n"
"v_mfma_f32_32x32x16_bf16 v[176:191], v[92:95], a[64:67], v[176:191]\n"
"s_add_u32 s7, s7, 1024\n"
"s_add_u32 m0, s96, 0x400\n"
"ds_read_b64_tr_b16 v[108:109], v23 offset:4096\n"
"ds_read_b64_tr_b16 v[110:111], v23 offset:4608\n"
"buffer_load_dwordx4 v6, s[48:51], s7 offen offset:3072 sc1 lds\n"
"v_rcp_f32 v39, v39\n"
"s_waitcnt lgkmcnt(4)\n"
"v_mfma_f32_32x32x16_bf16 v[128:143], v[96:99], a[68:71], v[128:143]\n"
"s_add_u32 s7, s5, 0\n"
"s_add_u32 s7, s7, s1\n"
"s_add_u32 m0, s96, 0x1400\n"
"ds_read_b64_tr_b16 v[80:81], v20 offset:8192\n"
"ds_read_b64_tr_b16 v[82:83], v20 offset:8704\n"
"buffer_load_dwordx4 v6, s[48:51], s7 offen sc1 lds\n"
"v_mfma_f32_32x32x16_bf16 v[144:159], v[100:103], a[68:71], v[144:159]\n"
"s_add_u32 m0, s81, 0x14000\n"
"ds_read_b64_tr_b16 v[84:85], v21 offset:8192\n"
"ds_read_b64_tr_b16 v[86:87], v21 offset:8704\n"
"ds_read_b64_tr_b16 v[88:89], v22 offset:8192\n"
"ds_read_b64_tr_b16 v[90:91], v22 offset:8704\n"
"buffer_load_dwordx4 v18, s[44:47], s74 offen offset:2048 lds\n"
"v_mul_f32 v220, v220, v36\n"
"s_waitcnt lgkmcnt(6)\n"
"v_mfma_f32_32x32x16_bf16 v[160:175], v[104:107], a[68:71], v[160:175]\n"
"ds_read_b64_tr_b16 v[92:93], v23 offset:8192\n"
"ds_read_b64_tr_b16 v[94:95], v23 offset:8704\n"
"v_mul_f32 v221, v221, v37\n"
"v_mul_f32 v222, v222, v38\n"
"v_mul_f32 v223, v223, v39\n"
"v_cvt_pk_bf16_f32 v46, v220, v221\n"
"v_cvt_pk_bf16_f32 v47, v222, v223\n"
"buffer_store_dwordx4 v[44:47], v5, s[48:51], s86 offen offset:512 sc1\n"
"v_mfma_f32_32x32x16_bf16 v[176:191], v[108:111], a[68:71], v[176:191]\n"
"ds_read_b64_tr_b16 v[96:97], v20 offset:12288\n"
"ds_read_b64_tr_b16 v[98:99], v20 offset:12800\n"
"v_mul_f32 v36, 0xbfb8aa3b, v224\n"
"v_mul_f32 v37, 0xbfb8aa3b, v225\n"
"v_mul_f32 v38, 0xbfb8aa3b, v226\n"
"v_mul_f32 v39, 0xbfb8aa3b, v227\n"
"v_exp_f32 v36, v36\n"
"v_mul_f32 v228, v224, v228\n"
"v_exp_f32 v37, v37\n"
"v_mul_f32 v229, v225, v229\n"
"s_waitcnt lgkmcnt(6)\n"
"v_mfma_f32_32x32x16_bf16 v[128:143], v[80:83], a[72:75], v[128:143]\n"
"s_add_u32 m0, s96, 0x1400\n"
"ds_read_b64_tr_b16 v[100:101], v21 offset:12288\n"
"ds_read_b64_tr_b16 v[102:103], v21 offset:12800\n"
"buffer_load_dwordx4 v6, s[48:51], s7 offen offset:1024 sc1 lds\n"
"v_mfma_f32_32x32x16_bf16 v[144:159], v[84:87], a[72:75], v[144:159]\n"
"s_add_u32 m0, s81, 0x14000\n"
"ds_read_b64_tr_b16 v[104:105], v22 offset:12288\n"
"ds_read_b64_tr_b16 v[106:107], v22 offset:12800\n"
"buffer_load_dwordx4 v19, s[44:47], s74 offen offset:3072 lds\n"
"v_exp_f32 v38, v38\n"
"v_mul_f32 v230, v226, v230\n"
"v_exp_f32 v39, v39\n"
"s_waitcnt lgkmcnt(6)\n"
"v_mfma_f32_32x32x16_bf16 v[160:175], v[88:91], a[72:75], v[160:175]\n"
"ds_read_b64_tr_b16 v[108:109], v23 offset:12288\n"
"ds_read_b64_tr_b16 v[110:111], v23 offset:12800\n"
"v_mul_f32 v231, v227, v231\n"
"v_add_f32 v36, 1.0, v36\n"
"v_add_f32 v37, 1.0, v37\n"
"v_add_f32 v38, 1.0, v38\n"
"v_add_f32 v39, 1.0, v39\n"
"v_rcp_f32 v36, v36\n"
"v_mfma_f32_32x32x16_bf16 v[176:191], v[92:95], a[72:75], v[176:191]\n"
"s_add_u32 m0, s96, 0x1400\n"
"ds_read_b64_tr_b16 v[80:81], v20 offset:16384\n"
"ds_read_b64_tr_b16 v[82:83], v20 offset:16896\n"
"buffer_load_dwordx4 v6, s[48:51], s7 offen offset:2048 sc1 lds\n"
"s_waitcnt lgkmcnt(6)\n"
"v_mfma_f32_32x32x16_bf16 v[128:143], v[96:99], a[76:79], v[128:143]\n"
"ds_read_b64_tr_b16 v[84:85], v21 offset:16384\n"
"ds_read_b64_tr_b16 v[86:87], v21 offset:16896\n"
"v_rcp_f32 v37, v37\n"
"v_rcp_f32 v38, v38\n"
"v_rcp_f32 v39, v39\n"
"v_mul_f32 v228, v228, v36\n"
"v_mfma_f32_32x32x16_bf16 v[144:159], v[100:103], a[76:79], v[144:159]\n"
"s_add_u32 m0, s81, 0x18000\n"
"ds_read_b64_tr_b16 v[88:89], v22 offset:16384\n"
"ds_read_b64_tr_b16 v[90:91], v22 offset:16896\n"
"buffer_load_dwordx4 v16, s[40:43], s75 offen lds\n"
"v_mul_f32 v229, v229, v37\n"
"s_waitcnt lgkmcnt(6)\n"
"v_mfma_f32_32x32x16_bf16 v[160:175], v[104:107], a[76:79], v[160:175]\n"
"ds_read_b64_tr_b16 v[92:93], v23 offset:16384\n"
"ds_read_b64_tr_b16 v[94:95], v23 offset:16896\n"
"v_mul_f32 v230, v230, v38\n"
"v_mul_f32 v231, v231, v39\n"
"v_cvt_pk_bf16_f32 v40, v228, v229\n"
"v_cvt_pk_bf16_f32 v41, v230, v231\n"
"v_mul_f32 v36, 0xbfb8aa3b, v232\n"
"v_mul_f32 v37, 0xbfb8aa3b, v233\n"
"v_mul_f32 v38, 0xbfb8aa3b, v234\n"
"v_mfma_f32_32x32x16_bf16 v[176:191], v[108:111], a[76:79], v[176:191]\n"
"s_add_u32 m0, s96, 0x1400\n"
"ds_read_b64_tr_b16 v[96:97], v20 offset:20480\n"
"ds_read_b64_tr_b16 v[98:99], v20 offset:20992\n"
"ds_read_b64_tr_b16 v[100:101], v21 offset:20480\n"
"ds_read_b64_tr_b16 v[102:103], v21 offset:20992\n"
"buffer_load_dwordx4 v6, s[48:51], s7 offen offset:3072 sc1 lds\n"
"v_mul_f32 v39, 0xbfb8aa3b, v235\n"
"s_waitcnt lgkmcnt(8)\n"
"v_mfma_f32_32x32x16_bf16 v[128:143], v[80:83], a[80:83], v[128:143]\n"
"ds_read_b64_tr_b16 v[104:105], v22 offset:20480\n"
"ds_read_b64_tr_b16 v[106:107], v22 offset:20992\n"
"v_exp_f32 v36, v36\n"
"v_mul_f32 v236, v232, v236\n"
"v_exp_f32 v37, v37\n"
"v_mul_f32 v237, v233, v237\n"
"v_mfma_f32_32x32x16_bf16 v[144:159], v[84:87], a[80:83], v[144:159]\n"
"s_add_u32 m0, s81, 0x18000\n"
"ds_read_b64_tr_b16 v[108:109], v23 offset:20480\n"
"ds_read_b64_tr_b16 v[110:111], v23 offset:20992\n"
"buffer_load_dwordx4 v17, s[40:43], s75 offen offset:1024 lds\n"
"v_exp_f32 v38, v38\n"
"v_mul_f32 v238, v234, v238\n"
"v_exp_f32 v39, v39\n"
"s_waitcnt lgkmcnt(8)\n"
"v_mfma_f32_32x32x16_bf16 v[160:175], v[88:91], a[80:83], v[160:175]\n"
"ds_read_b64_tr_b16 v[80:81], v20 offset:24576\n"
"ds_read_b64_tr_b16 v[82:83], v20 offset:25088\n"
"v_mul_f32 v239, v235, v239\n"
"v_add_f32 v36, 1.0, v36\n"
"v_add_f32 v37, 1.0, v37\n"
"v_add_f32 v38, 1.0, v38\n"
"v_add_f32 v39, 1.0, v39\n"
"v_rcp_f32 v36, v36\n"
"v_mfma_f32_32x32x16_bf16 v[176:191], v[92:95], a[80:83], v[176:191]\n"
"s_add_u32 s7, s7, 1024\n"
"s_add_u32 m0, s96, 0x1800\n"
"ds_read_b64_tr_b16 v[84:85], v21 offset:24576\n"
"ds_read_b64_tr_b16 v[86:87], v21 offset:25088\n"
"buffer_load_dwordx4 v6, s[48:51], s7 offen offset:3072 sc1 lds\n"
"s_waitcnt lgkmcnt(8)\n"
"v_mfma_f32_32x32x16_bf16 v[128:143], v[96:99], a[84:87], v[128:143]\n"
"ds_read_b64_tr_b16 v[88:89], v22 offset:24576\n"
"ds_read_b64_tr_b16 v[90:91], v22 offset:25088\n"
"v_rcp_f32 v37, v37\n"
"v_rcp_f32 v38, v38\n"
"v_rcp_f32 v39, v39\n"
"v_mul_f32 v236, v236, v36\n"
"v_mul_f32 v237, v237, v37\n"
"v_mfma_f32_32x32x16_bf16 v[144:159], v[100:103], a[84:87], v[144:159]\n"
"s_add_u32 m0, s81, 0x18000\n"
"ds_read_b64_tr_b16 v[92:93], v23 offset:24576\n"
"ds_read_b64_tr_b16 v[94:95], v23 offset:25088\n"
"buffer_load_dwordx4 v18, s[44:47], s75 offen offset:2048 lds\n"
"v_mul_f32 v238, v238, v38\n"
"v_mul_f32 v239, v239, v39\n"
"v_cvt_pk_bf16_f32 v42, v236, v237\n"
"v_cvt_pk_bf16_f32 v43, v238, v239\n"
"s_waitcnt lgkmcnt(8)\n"
"v_mfma_f32_32x32x16_bf16 v[160:175], v[104:107], a[84:87], v[160:175]\n"
"ds_read_b64_tr_b16 v[96:97], v20 offset:28672\n"
"ds_read_b64_tr_b16 v[98:99], v20 offset:29184\n"
"buffer_store_dwordx4 v[40:43], v5, s[48:51], s86 offen offset:1024 sc1\n"
"v_mul_f32 v36, 0xbfb8aa3b, v240\n"
"v_mul_f32 v37, 0xbfb8aa3b, v241\n"
"v_mul_f32 v38, 0xbfb8aa3b, v242\n"
"v_mul_f32 v39, 0xbfb8aa3b, v243\n"
"v_mfma_f32_32x32x16_bf16 v[176:191], v[108:111], a[84:87], v[176:191]\n"
"s_add_u32 s7, s4, 20480\n"
"s_add_u32 m0, s96, 0x2800\n"
"ds_read_b64_tr_b16 v[100:101], v21 offset:28672\n"
"ds_read_b64_tr_b16 v[102:103], v21 offset:29184\n"
"buffer_load_dwordx4 v6, s[48:51], s7 offen sc1 lds\n"
"v_exp_f32 v36, v36\n"
"s_waitcnt lgkmcnt(8)\n"
"v_mfma_f32_32x32x16_bf16 v[128:143], v[80:83], a[88:91], v[128:143]\n"
"ds_read_b64_tr_b16 v[104:105], v22 offset:28672\n"
"ds_read_b64_tr_b16 v[106:107], v22 offset:29184\n"
"v_mul_f32 v244, v240, v244\n"
"v_exp_f32 v37, v37\n"
"v_mul_f32 v245, v241, v245\n"
"v_exp_f32 v38, v38\n"
"v_mfma_f32_32x32x16_bf16 v[144:159], v[84:87], a[88:91], v[144:159]\n"
"s_add_u32 m0, s81, 0x18000\n"
"ds_read_b64_tr_b16 v[108:109], v23 offset:28672\n"
"ds_read_b64_tr_b16 v[110:111], v23 offset:29184\n"
"buffer_load_dwordx4 v19, s[44:47], s75 offen offset:3072 lds\n"
"v_mul_f32 v246, v242, v246\n"
"v_exp_f32 v39, v39\n"
"v_mul_f32 v247, v243, v247\n"
"s_waitcnt lgkmcnt(8)\n"
"v_mfma_f32_32x32x16_bf16 v[160:175], v[88:91], a[88:91], v[160:175]\n"
"v_add_u32 v20, 0xa000, v20\n"
"v_add_u32 v21, 0xa000, v21\n"
"v_add_f32 v36, 1.0, v36\n"
"v_add_f32 v37, 1.0, v37\n"
"v_add_f32 v38, 1.0, v38\n"
"v_add_f32 v39, 1.0, v39\n"
"v_rcp_f32 v36, v36\n"
"v_rcp_f32 v37, v37\n"
"v_mfma_f32_32x32x16_bf16 v[176:191], v[92:95], a[88:91], v[176:191]\n"
"s_add_u32 m0, s96, 0x2800\n"
"v_add_u32 v22, 0xa000, v22\n"
"v_add_u32 v23, 0xa000, v23\n"
"buffer_load_dwordx4 v6, s[48:51], s7 offen offset:1024 sc1 lds\n"
"s_waitcnt lgkmcnt(4)\n"
"v_mfma_f32_32x32x16_bf16 v[128:143], v[96:99], a[92:95], v[128:143]\n"
"s_waitcnt vmcnt(21) lgkmcnt(0)\n"
"s_barrier\n"
"ds_read_b64_tr_b16 v[80:81], v20 offset:0\n"
"ds_read_b64_tr_b16 v[82:83], v20 offset:512\n"
"v_mfma_f32_32x32x16_bf16 v[144:159], v[100:103], a[92:95], v[144:159]\n"
"s_add_u32 s7, s4, 10240\n"
"s_add_u32 s7, s7, s1\n"
"ds_read_b64_tr_b16 v[84:85], v21 offset:0\n"
"ds_read_b64_tr_b16 v[86:87], v21 offset:512\n"
"buffer_load_dwordx4 v[48:51], v6, s[48:51], s7 offen offset:3072 sc1\n"
"v_rcp_f32 v38, v38\n"
"v_mfma_f32_32x32x16_bf16 v[160:175], v[104:107], a[92:95], v[160:175]\n"
"ds_read_b64_tr_b16 v[88:89], v22 offset:0\n"
"ds_read_b64_tr_b16 v[90:91], v22 offset:512\n"
"v_rcp_f32 v39, v39\n"
"v_mul_f32 v244, v244, v36\n"
"v_mul_f32 v245, v245, v37\n"
"v_mul_f32 v246, v246, v38\n"
"v_mul_f32 v247, v247, v39\n"
"v_cvt_pk_bf16_f32 v44, v244, v245\n"
"v_mfma_f32_32x32x16_bf16 v[176:191], v[108:111], a[92:95], v[176:191]\n"
"ds_read_b64_tr_b16 v[92:93], v23 offset:0\n"
"ds_read_b64_tr_b16 v[94:95], v23 offset:512\n"
"v_cvt_pk_bf16_f32 v45, v246, v247\n"
"v_mul_f32 v36, 0xbfb8aa3b, v248\n"
"v_mul_f32 v37, 0xbfb8aa3b, v249\n"
"v_mul_f32 v38, 0xbfb8aa3b, v250\n"
"v_mul_f32 v39, 0xbfb8aa3b, v251\n"
"v_exp_f32 v36, v36\n"
"v_mul_f32 v252, v248, v252\n"
"v_exp_f32 v37, v37\n"
"s_waitcnt lgkmcnt(4)\n"
"v_mfma_f32_32x32x16_bf16 v[128:143], v[80:83], a[96:99], v[128:143]\n"
"ds_read_b64_tr_b16 v[96:97], v20 offset:4096\n"
"ds_read_b64_tr_b16 v[98:99], v20 offset:4608\n"
"v_mul_f32 v253, v249, v253\n"
"v_exp_f32 v38, v38\n"
"v_mul_f32 v254, v250, v254\n"
"v_exp_f32 v39, v39\n"
"v_mul_f32 v255, v251, v255\n"
"v_mfma_f32_32x32x16_bf16 v[144:159], v[84:87], a[96:99], v[144:159]\n"
"s_add_u32 s7, s7, 1024\n"
"ds_read_b64_tr_b16 v[100:101], v21 offset:4096\n"
"ds_read_b64_tr_b16 v[102:103], v21 offset:4608\n"
"buffer_load_dwordx4 v[52:55], v6, s[48:51], s7 offen offset:3072 sc1\n"
"v_add_f32 v36, 1.0, v36\n"
"s_waitcnt lgkmcnt(4)\n"
"v_mfma_f32_32x32x16_bf16 v[160:175], v[88:91], a[96:99], v[160:175]\n"
"ds_read_b64_tr_b16 v[104:105], v22 offset:4096\n"
"ds_read_b64_tr_b16 v[106:107], v22 offset:4608\n"
"v_add_f32 v37, 1.0, v37\n"
"v_add_f32 v38, 1.0, v38\n"
"v_add_f32 v39, 1.0, v39\n"
"v_rcp_f32 v36, v36\n"
"v_rcp_f32 v37, v37\n"
"v_mfma_f32_32x32x16_bf16 v[176:191], v[92:95], a[96:99], v[176:191]\n"
"s_add_u32 s7, s13, 10240\n"
"ds_read_b64_tr_b16 v[108:109], v23 offset:4096\n"
"ds_read_b64_tr_b16 v[110:111], v23 offset:4608\n"
"buffer_load_dwordx4 v[56:59], v6, s[48:51], s7 offen offset:0 sc1\n"
"s_waitcnt lgkmcnt(4)\n"
"v_mfma_f32_32x32x16_bf16 v[128:143], v[96:99], a[100:103], v[128:143]\n"
"ds_read_b64_tr_b16 v[80:81], v20 offset:8192\n"
"ds_read_b64_tr_b16 v[82:83], v20 offset:8704\n"
"s_waitcnt vmcnt(21)\n"
"v_cmp_eq_u64 vcc, s[84:85], v[8:9]\n"
"s_and_b64 s[10:11], vcc, s[94:95]\n"
"s_cmp_eq_u64 s[10:11], s[94:95]\n"
"s_cbranch_scc0 L_ffn_g_slow\n"
"L_ffn_g_ok:\n"
"s_lshl_b32 s83, s76, 7\n"
"s_lshl_b32 s82, s77, 9\n"
"s_add_u32 s82, s82, s83\n"
"s_add_u32 s82, s82, 16384\n"
"v_and_b32 v34, 31, v1\n"
"v_lshlrev_b32 v34, 2, v34\n"
"buffer_load_dword v34, v34, s[48:51], s82 offen sc1\n"
"v_rcp_f32 v38, v38\n"
"v_mfma_f32_32x32x16_bf16 v[144:159], v[100:103], a[100:103], v[144:159]\n"
"ds_read_b64_tr_b16 v[84:85], v21 offset:8192\n"
"ds_read_b64_tr_b16 v[86:87], v21 offset:8704\n"
"ds_read_b64_tr_b16 v[88:89], v22 offset:8192\n"
"ds_read_b64_tr_b16 v[90:91], v22 offset:8704\n"
"v_rcp_f32 v39, v39\n"
"v_mul_f32 v252, v252, v36\n"
"s_waitcnt lgkmcnt(6)\n"
"v_mfma_f32_32x32x16_bf16 v[160:175], v[104:107], a[100:103], v[160:175]\n"
"ds_read_b64_tr_b16 v[92:93], v23 offset:8192\n"
"ds_read_b64_tr_b16 v[94:95], v23 offset:8704\n"
"s_add_u32 s82, s76, 0\n"
"s_and_b32 s82, s82, 3\n"
"s_mul_i32 s82, s82, 0x12800\n"
"v_add_u32 v16, s82, v6\n"
"s_add_u32 s82, s76, 1\n"
"v_mul_f32 v253, v253, v37\n"
"v_mfma_f32_32x32x16_bf16 v[176:191], v[108:111], a[100:103], v[176:191]\n"
"ds_read_b64_tr_b16 v[96:97], v20 offset:12288\n"
"ds_read_b64_tr_b16 v[98:99], v20 offset:12800\n"
"buffer_load_dwordx4 v[60:63], v6, s[48:51], s7 offen offset:1024 sc1\n"
"s_and_b32 s82, s82, 3\n"
"s_mul_i32 s82, s82, 0x12800\n"
"v_add_u32 v17, s82, v6\n"
"v_mul_f32 v254, v254, v38\n"
"s_waitcnt lgkmcnt(6)\n"
"v_mfma_f32_32x32x16_bf16 v[128:143], v[80:83], a[104:107], v[128:143]\n"
"ds_read_b64_tr_b16 v[100:101], v21 offset:12288\n"
"ds_read_b64_tr_b16 v[102:103], v21 offset:12800\n"
"s_add_u32 s82, s76, 2\n"
"s_and_b32 s82, s82, 3\n"
"s_mul_i32 s82, s82, 0x12800\n"
"v_add_u32 v18, s82, v6\n"
"s_add_u32 s82, s76, 3\n"
"v_mul_f32 v255, v255, v39\n"
"v_mfma_f32_32x32x16_bf16 v[144:159], v[84:87], a[104:107], v[144:159]\n"
"ds_read_b64_tr_b16 v[104:105], v22 offset:12288\n"
"ds_read_b64_tr_b16 v[106:107], v22 offset:12800\n"
"s_and_b32 s82, s82, 3\n"
"s_mul_i32 s82, s82, 0x12800\n"
"v_add_u32 v19, s82, v6\n"
"s_mul_i32 s82, s88, 303104\n"
"s_add_u32 s82, s82, 0x400000\n"
"s_mul_i32 s83, s76, 12288\n"
"s_add_u32 s56, s82, s83\n"
"s_waitcnt lgkmcnt(6)\n"
"v_mfma_f32_32x32x16_bf16 v[160:175], v[88:91], a[104:107], v[160:175]\n"
"ds_read_b64_tr_b16 v[108:109], v23 offset:12288\n"
"ds_read_b64_tr_b16 v[110:111], v23 offset:12800\n"
"s_bitcmp1_b32 s0, 1\n"
"s_cselect_b32 s83, 6144, 0\n"
"s_add_u32 s62, s56, 6144\n"
"s_sub_u32 s62, s62, s83\n"
"s_add_u32 s56, s56, s83\n"
"s_lshl_b32 s83, s76, 11\n"
"v_cvt_pk_bf16_f32 v46, v252, v253\n"
"v_mfma_f32_32x32x16_bf16 v[176:191], v[92:95], a[104:107], v[176:191]\n"
"ds_read_b64_tr_b16 v[80:81], v20 offset:16384\n"
"ds_read_b64_tr_b16 v[82:83], v20 offset:16896\n"
"buffer_load_dwordx4 v[64:67], v6, s[48:51], s7 offen offset:2048 sc1\n"
"s_add_u32 s83, s83, s82\n"
"s_add_u32 s57, s83, 49152\n"
"s_mul_i32 s83, s76, 5120\n"
"v_cvt_pk_bf16_f32 v47, v254, v255\n"
"s_waitcnt lgkmcnt(6)\n"
"v_mfma_f32_32x32x16_bf16 v[128:143], v[96:99], a[108:111], v[128:143]\n"
"ds_read_b64_tr_b16 v[84:85], v21 offset:16384\n"
"ds_read_b64_tr_b16 v[86:87], v21 offset:16896\n"
"s_add_u32 s83, s83, s82\n"
"s_add_u32 s58, s83, 55296\n"
"s_bitcmp1_b32 s0, 0\n"
"s_cselect_b32 s83, 1024, 0\n"
"s_lshl_b32 s63, s83, 1\n"
"v_mfma_f32_32x32x16_bf16 v[144:159], v[100:103], a[108:111], v[144:159]\n"
"ds_read_b64_tr_b16 v[88:89], v22 offset:16384\n"
"ds_read_b64_tr_b16 v[90:91], v22 offset:16896\n"
"buffer_load_dwordx4 v[68:71], v6, s[48:51], s7 offen offset:3072 sc1\n"
"s_sub_u32 s63, 1024, s63\n"
"s_add_u32 s56, s56, s83\n"
"s_add_u32 s62, s62, s83\n"
"buffer_store_dwordx4 v[44:47], v5, s[48:51], s86 offen offset:1536 sc1\n"
"s_waitcnt lgkmcnt(6)\n"
"v_mfma_f32_32x32x16_bf16 v[160:175], v[104:107], a[108:111], v[160:175]\n"
"ds_read_b64_tr_b16 v[92:93], v23 offset:16384\n"
"ds_read_b64_tr_b16 v[94:95], v23 offset:16896\n"
"s_add_u32 s57, s57, s83\n"
"s_add_u32 s66, s58, 5120\n"
"s_lshl_b32 s83, s0, 10\n"
"s_add_u32 s58, s58, s83\n"
"s_cmp_eq_u32 s76, 3\n"
"s_cselect_b32 s57, 0x40000000, s57\n"
"v_mfma_f32_32x32x16_bf16 v[176:191], v[108:111], a[108:111], v[176:191]\n"
"ds_read_b64_tr_b16 v[96:97], v20 offset:20480\n"
"ds_read_b64_tr_b16 v[98:99], v20 offset:20992\n"
"ds_read_b64_tr_b16 v[100:101], v21 offset:20480\n"
"ds_read_b64_tr_b16 v[102:103], v21 offset:20992\n"
"v_add_u32 v27, s96, v6\n"
"v_lshlrev_b32 v32, 3, v1\n"
"s_add_u32 s86, s86, 2048\n"
"s_waitcnt lgkmcnt(8)\n"
"v_mfma_f32_32x32x16_bf16 v[128:143], v[80:83], a[112:115], v[128:143]\n"
"ds_read_b64_tr_b16 v[104:105], v22 offset:20480\n"
"ds_read_b64_tr_b16 v[106:107], v22 offset:20992\n"
"s_lshr_b32 s82, s88, 1\n"
"s_lshl_b32 s82, s82, 9\n"
"s_add_u32 s60, s82, 1024\n"
"s_lshl_b32 s97, s76, 3\n"
"buffer_load_dwordx2 v[8:9], v32, s[48:51], s60 offen sc1\n"
"v_mfma_f32_32x32x16_bf16 v[144:159], v[84:87], a[112:115], v[144:159]\n"
"s_add_u32 s7, s7, 1024\n"
"ds_read_b64_tr_b16 v[108:109], v23 offset:20480\n"
"ds_read_b64_tr_b16 v[110:111], v23 offset:20992\n"
"buffer_load_dwordx4 v[72:75], v6, s[48:51], s7 offen offset:3072 sc1\n"
"s_waitcnt lgkmcnt(8)\n"
"v_mfma_f32_32x32x16_bf16 v[160:175], v[88:91], a[112:115], v[160:175]\n"
"ds_read_b64_tr_b16 v[80:81], v20 offset:24576\n"
"ds_read_b64_tr_b16 v[82:83], v20 offset:25088\n"
"v_mfma_f32_32x32x16_bf16 v[176:191], v[92:95], a[112:115], v[176:191]\n"
"ds_read_b64_tr_b16 v[84:85], v21 offset:24576\n"
"ds_read_b64_tr_b16 v[86:87], v21 offset:25088\n"
"s_waitcnt lgkmcnt(8)\n"
"v_mfma_f32_32x32x16_bf16 v[128:143], v[96:99], a[116:119], v[128:143]\n"
"ds_read_b64_tr_b16 v[88:89], v22 offset:24576\n"
"ds_read_b64_tr_b16 v[90:91], v22 offset:25088\n"
"v_mfma_f32_32x32x16_bf16 v[144:159], v[100:103], a[116:119], v[144:159]\n"
"s_add_u32 s7, s13, 10240\n"
"s_add_u32 s7, s7, s1\n"
"ds_read_b64_tr_b16 v[92:93], v23 offset:24576\n"
"ds_read_b64_tr_b16 v[94:95], v23 offset:25088\n"
"buffer_load_dwordx4 v[76:79], v6, s[48:51], s7 offen offset:0 sc1\n"
"s_waitcnt lgkmcnt(8)\n"
"v_mfma_f32_32x32x16_bf16 v[160:175], v[104:107], a[116:119], v[160:175]\n"
"ds_read_b64_tr_b16 v[96:97], v20 offset:28672\n"
"ds_read_b64_tr_b16 v[98:99], v20 offset:29184\n"
"v_mfma_f32_32x32x16_bf16 v[176:191], v[108:111], a[116:119], v[176:191]\n"
"ds_read_b64_tr_b16 v[100:101], v21 offset:28672\n"
"ds_read_b64_tr_b16 v[102:103], v21 offset:29184\n"
"s_waitcnt lgkmcnt(8)\n"
"v_mfma_f32_32x32x16_bf16 v[128:143], v[80:83], a[120:123], v[128:143]\n"
"ds_read_b64_tr_b16 v[104:105], v22 offset:28672\n"
"ds_read_b64_tr_b16 v[106:107], v22 offset:29184\n"
"v_mfma_f32_32x32x16_bf16 v[144:159], v[84:87], a[120:123], v[144:159]\n"
"ds_read_b64_tr_b16 v[108:109], v23 offset:28672\n"
"ds_read_b64_tr_b16 v[110:111], v23 offset:29184\n"
"s_waitcnt lgkmcnt(8)\n"
"v_mfma_f32_32x32x16_bf16 v[160:175], v[88:91], a[120:123], v[160:175]\n"
"v_add_u32 v20, 0xa000, v20\n"
"v_add_u32 v21, 0xa000, v21\n"
"v_mfma_f32_32x32x16_bf16 v[176:191], v[92:95], a[120:123], v[176:191]\n"
"v_add_u32 v22, 0xa000, v22\n"
"v_add_u32 v23, 0xa000, v23\n"
"s_waitcnt lgkmcnt(4)\n"
"v_mfma_f32_32x32x16_bf16 v[128:143], v[96:99], a[124:127], v[128:143]\n"
"s_waitcnt vmcnt(12) lgkmcnt(0)\n"
"s_barrier\n"
"ds_read_b64_tr_b16 v[80:81], v20 offset:0\n"
"ds_read_b64_tr_b16 v[82:83], v20 offset:512\n"
"v_mfma_f32_32x32x16_bf16 v[144:159], v[100:103], a[124:127], v[144:159]\n"
"ds_read_b64_tr_b16 v[84:85], v21 offset:0\n"
"ds_read_b64_tr_b16 v[86:87], v21 offset:512\n"
"v_mfma_f32_32x32x16_bf16 v[160:175], v[104:107], a[124:127], v[160:175]\n"
"ds_read_b64_tr_b16 v[88:89], v22 offset:0\n"
"ds_read_b64_tr_b16 v[90:91], v22 offset:512\n"
"v_mfma_f32_32x32x16_bf16 v[176:191], v[108:111], a[124:127], v[176:191]\n"
"ds_read_b64_tr_b16 v[92:93], v23 offset:0\n"
"ds_read_b64_tr_b16 v[94:95], v23 offset:512\n"
"s_waitcnt lgkmcnt(4)\n"
"v_mfma_f32_32x32x16_bf16 v[128:143], v[80:83], a[128:131], v[128:143]\n"
"ds_read_b64_tr_b16 v[96:97], v20 offset:4096\n"
"ds_read_b64_tr_b16 v[98:99], v20 offset:4608\n"
"v_mfma_f32_32x32x16_bf16 v[144:159], v[84:87], a[128:131], v[144:159]\n"
"ds_read_b64_tr_b16 v[100:101], v21 offset:4096\n"
"ds_read_b64_tr_b16 v[102:103], v21 offset:4608\n"
"s_waitcnt lgkmcnt(4)\n"
"v_mfma_f32_32x32x16_bf16 v[160:175], v[88:91], a[128:131], v[160:175]\n"
"ds_read_b64_tr_b16 v[104:105], v22 offset:4096\n"
"ds_read_b64_tr_b16 v[106:107], v22 offset:4608\n"
"v_mfma_f32_32x32x16_bf16 v[176:191], v[92:95], a[128:131], v[176:191]\n"
"ds_read_b64_tr_b16 v[108:109], v23 offset:4096\n"
"ds_read_b64_tr_b16 v[110:111], v23 offset:4608\n"
"s_waitcnt lgkmcnt(4)\n"
"v_mfma_f32_32x32x16_bf16 v[128:143], v[96:99], a[132:135], v[128:143]\n"
"ds_read_b64_tr_b16 v[80:81], v20 offset:8192\n"
"ds_read_b64_tr_b16 v[82:83], v20 offset:8704\n"
"v_mfma_f32_32x32x16_bf16 v[144:159], v[100:103], a[132:135], v[144:159]\n"
"ds_read_b64_tr_b16 v[84:85], v21 offset:8192\n"
"ds_read_b64_tr_b16 v[86:87], v21 offset:8704\n"
"ds_read_b64_tr_b16 v[88:89], v22 offset:8192\n"
"ds_read_b64_tr_b16 v[90:91], v22 offset:8704\n"
"s_waitcnt lgkmcnt(6)\n"
"v_mfma_f32_32x32x16_bf16 v[160:175], v[104:107], a[132:135], v[160:175]\n"
"ds_read_b64_tr_b16 v[92:93], v23 offset:8192\n"
"ds_read_b64_tr_b16 v[94:95], v23 offset:8704\n"
"v_mfma_f32_32x32x16_bf16 v[176:191], v[108:111], a[132:135], v[176:191]\n"
"ds_read_b64_tr_b16 v[96:97], v20 offset:12288\n"
"ds_read_b64_tr_b16 v[98:99], v20 offset:12800\n"
"s_waitcnt lgkmcnt(6)\n"
"v_mfma_f32_32x32x16_bf16 v[128:143], v[80:83], a[136:139], v[128:143]\n"
"ds_read_b64_tr_b16 v[100:101], v21 offset:12288\n"
"ds_read_b64_tr_b16 v[102:103], v21 offset:12800\n"
"v_mfma_f32_32x32x16_bf16 v[144:159], v[84:87], a[136:139], v[144:159]\n"
"ds_read_b64_tr_b16 v[104:105], v22 offset:12288\n"
"ds_read_b64_tr_b16 v[106:107], v22 offset:12800\n"
"s_waitcnt lgkmcnt(6)\n"
"v_mfma_f32_32x32x16_bf16 v[160:175], v[88:91], a[136:139], v[160:175]\n"
"ds_read_b64_tr_b16 v[108:109], v23 offset:12288\n"
"ds_read_b64_tr_b16 v[110:111], v23 offset:12800\n"
"s_lshl_b32 s94, 0x11, s97\n"
"s_lshl_b32 s95, 0x4, s97\n"
"s_waitcnt vmcnt(2)\n"
"v_cmp_eq_u64 vcc, s[84:85], v[8:9]\n"
"s_and_b64 s[10:11], vcc, s[94:95]\n"
"s_cmp_eq_u64 s[10:11], s[94:95]\n"
"s_cbranch_scc0 L_ffn_e_slow\n"
"L_ffn_e_ok:\n"
"v_mfma_f32_32x32x16_bf16 v[176:191], v[92:95], a[136:139], v[176:191]\n"
"s_add_u32 s59, s56, 0\n"
"ds_read_b64_tr_b16 v[80:81], v20 offset:16384\n"
"ds_read_b64_tr_b16 v[82:83], v20 offset:16896\n"
"buffer_load_dwordx4 v[192:195], v16, s[48:51], s59 offen sc1\n"
"s_waitcnt lgkmcnt(6)\n"
"v_mfma_f32_32x32x16_bf16 v[128:143], v[96:99], a[140:143], v[128:143]\n"
"ds_read_b64_tr_b16 v[84:85], v21 offset:16384\n"
"ds_read_b64_tr_b16 v[86:87], v21 offset:16896\n"
"v_mfma_f32_32x32x16_bf16 v[144:159], v[100:103], a[140:143], v[144:159]\n"
"ds_read_b64_tr_b16 v[88:89], v22 offset:16384\n"
"ds_read_b64_tr_b16 v[90:91], v22 offset:16896\n"
"buffer_load_dwordx4 v[196:199], v17, s[48:51], s59 offen sc1\n"
"s_waitcnt lgkmcnt(6)\n"
"v_mfma_f32_32x32x16_bf16 v[160:175], v[104:107], a[140:143], v[160:175]\n"
"ds_read_b64_tr_b16 v[92:93], v23 offset:16384\n"
"ds_read_b64_tr_b16 v[94:95], v23 offset:16896\n"
"v_mfma_f32_32x32x16_bf16 v[176:191], v[108:111], a[140:143], v[176:191]\n"
"ds_read_b64_tr_b16 v[96:97], v20 offset:20480\n"
"ds_read_b64_tr_b16 v[98:99], v20 offset:20992\n"
"ds_read_b64_tr_b16 v[100:101], v21 offset:20480\n"
"ds_read_b64_tr_b16 v[102:103], v21 offset:20992\n"
"s_waitcnt lgkmcnt(8)\n"
"v_mfma_f32_32x32x16_bf16 v[128:143], v[80:83], a[144:147], v[128:143]\n"
"ds_read_b64_tr_b16 v[104:105], v22 offset:20480\n"
"ds_read_b64_tr_b16 v[106:107], v22 offset:20992\n"
"buffer_load_dwordx4 v[200:203], v18, s[48:51], s59 offen sc1\n"
"v_mfma_f32_32x32x16_bf16 v[144:159], v[84:87], a[144:147], v[144:159]\n"
"ds_read_b64_tr_b16 v[108:109], v23 offset:20480\n"
"ds_read_b64_tr_b16 v[110:111], v23 offset:20992\n"
"s_waitcnt lgkmcnt(8)\n"
"v_mfma_f32_32x32x16_bf16 v[160:175], v[88:91], a[144:147], v[160:175]\n"
"ds_read_b64_tr_b16 v[80:81], v20 offset:24576\n"
"ds_read_b64_tr_b16 v[82:83], v20 offset:25088\n"
"buffer_load_dwordx4 v[204:207], v19, s[48:51], s59 offen sc1\n"
"v_mfma_f32_32x32x16_bf16 v[176:191], v[92:95], a[144:147], v[176:191]\n"
"ds_read_b64_tr_b16 v[84:85], v21 offset:24576\n"
"ds_read_b64_tr_b16 v[86:87], v21 offset:25088\n"
"s_waitcnt lgkmcnt(8)\n"
"v_mfma_f32_32x32x16_bf16 v[128:143], v[96:99], a[148:151], v[128:143]\n"
"ds_read_b64_tr_b16 v[88:89], v22 offset:24576\n"
"ds_read_b64_tr_b16 v[90:91], v22 offset:25088\n"
"v_mfma_f32_32x32x16_bf16 v[144:159], v[100:103], a[148:151], v[144:159]\n"
"s_add_u32 s59, s56, 0\n"
"s_add_u32 s59, s59, s63\n"
"ds_read_b64_tr_b16 v[92:93], v23 offset:24576\n"
"ds_read_b64_tr_b16 v[94:95], v23 offset:25088\n"
"buffer_load_dwordx4 v[208:211], v16, s[48:51], s59 offen sc1\n"
"s_waitcnt lgkmcnt(8)\n"
"v_mfma_f32_32x32x16_bf16 v[160:175], v[104:107], a[148:151], v[160:175]\n"
"ds_read_b64_tr_b16 v[96:97], v20 offset:28672\n"
"ds_read_b64_tr_b16 v[98:99], v20 offset:29184\n"
"v_mfma_f32_32x32x16_bf16 v[176:191], v[108:111], a[148:151], v[176:191]\n"
"ds_read_b64_tr_b16 v[100:101], v21 offset:28672\n"
"ds_read_b64_tr_b16 v[102:103], v21 offset:29184\n"
"s_waitcnt lgkmcnt(8)\n"
"v_mfma_f32_32x32x16_bf16 v[128:143], v[80:83], a[152:155], v[128:143]\n"
"ds_read_b64_tr_b16 v[104:105], v22 offset:28672\n"
"ds_read_b64_tr_b16 v[106:107], v22 offset:29184\n"
"buffer_load_dwordx4 v[212:215], v17, s[48:51], s59 offen sc1\n"
"v_mfma_f32_32x32x16_bf16 v[144:159], v[84:87], a[152:155], v[144:159]\n"
"ds_read_b64_tr_b16 v[108:109], v23 offset:28672\n"
"ds_read_b64_tr_b16 v[110:111], v23 offset:29184\n"
"s_waitcnt lgkmcnt(8)\n"
"v_mfma_f32_32x32x16_bf16 v[160:175], v[88:91], a[152:155], v[160:175]\n"
"buffer_load_dwordx4 v[216:219], v18, s[48:51], s59 offen sc1\n"
"v_mfma_f32_32x32x16_bf16 v[176:191], v[92:95], a[152:155], v[176:191]\n"
"s_waitcnt lgkmcnt(4)\n"
"v_mfma_f32_32x32x16_bf16 v[128:143], v[96:99], a[156:159], v[128:143]\n"
"v_mfma_f32_32x32x16_bf16 v[144:159], v[100:103], a[156:159], v[144:159]\n"
"buffer_load_dwordx4 v[220:223], v19, s[48:51], s59 offen sc1\n"
"s_waitcnt lgkmcnt(0)\n"
"v_mfma_f32_32x32x16_bf16 v[160:175], v[104:107], a[156:159], v[160:175]\n"
"v_mfma_f32_32x32x16_bf16 v[176:191], v[108:111], a[156:159], v[176:191]\n"
"s_barrier\n"
"buffer_store_dwordx2 v[2:3], v4, s[48:51], s87 offen offset:8 sc1\n"
"buffer_store_dwordx2 v[2:3], v4, s[48:51], s87 offen offset:24 sc1\n"
"s_nop 3\n"
"v_pk_mul_f32 v[36:37], v[128:129], s[64:65] op_sel_hi:[1,0]\n"
"s_add_u32 s59, s62, 0\n"
"buffer_load_dwordx4 a[0:3], v16, s[48:51], s59 offen sc1\n"
"v_pk_mul_f32 v[38:39], v[130:131], s[64:65] op_sel_hi:[1,0]\n"
"v_exp_f32 v36, v36\n"
"v_exp_f32 v37, v37\n"
"v_pk_mul_f32 v[132:133], v[128:129], v[132:133]\n"
"v_exp_f32 v38, v38\n"
"v_exp_f32 v39, v39\n"
"v_pk_mul_f32 v[134:135], v[130:131], v[134:135]\n"
"v_pk_add_f32 v[36:37], v[36:37], s[64:65] op_sel:[0,1] op_sel_hi:[1,1]\n"
"buffer_load_dwordx4 a[4:7], v17, s[48:51], s59 offen sc1\n"
"v_pk_add_f32 v[38:39], v[38:39], s[64:65] op_sel:[0,1] op_sel_hi:[1,1]\n"
"v_pk_mul_f32 v[132:133], v[132:133], v[34:35] op_sel_hi:[1,0]\n"
"v_pk_mul_f32 v[134:135], v[134:135], v[34:35] op_sel_hi:[1,0]\n"
"v_rcp_f32 v36, v36\n"
"v_rcp_f32 v37, v37\n"
"v_rcp_f32 v38, v38\n"
"v_rcp_f32 v39, v39\n"
"v_pk_mul_f32 v[132:133], v[132:133], v[36:37]\n"
"buffer_load_dwordx4 a[8:11], v18, s[48:51], s59 offen sc1\n"
"v_pk_mul_f32 v[134:135], v[134:135], v[38:39]\n"
"v_cvt_pk_bf16_f32 v40, v132, v133\n"
"v_cvt_pk_bf16_f32 v41, v134, v135\n"
"v_pk_mul_f32 v[36:37], v[136:137], s[64:65] op_sel_hi:[1,0]\n"
"v_pk_mul_f32 v[38:39], v[138:139], s[64:65] op_sel_hi:[1,0]\n"
"v_exp_f32 v36, v36\n"
"v_exp_f32 v37, v37\n"
"v_pk_mul_f32 v[140:141], v[136:137], v[140:141]\n"
"buffer_load_dwordx4 a[12:15], v19, s[48:51], s59 offen sc1\n"
"v_exp_f32 v38, v38\n"
"v_exp_f32 v39, v39\n"
"v_pk_mul_f32 v[142:143], v[138:139], v[142:143]\n"
"v_pk_add_f32 v[36:37], v[36:37], s[64:65] op_sel:[0,1] op_sel_hi:[1,1]\n"
"v_pk_add_f32 v[38:39], v[38:39], s[64:65] op_sel:[0,1] op_sel_hi:[1,1]\n"
"v_pk_mul_f32 v[140:141], v[140:141], v[34:35] op_sel_hi:[1,0]\n"
"v_pk_mul_f32 v[142:143], v[142:143], v[34:35] op_sel_hi:[1,0]\n"
"v_rcp_f32 v36, v36\n"
"s_add_u32 s59, s62, 0\n"
"s_add_u32 s59, s59, s63\n"
"buffer_load_dwordx4 a[16:19], v16, s[48:51], s59 offen sc1\n"
"v_rcp_f32 v37, v37\n"
"v_rcp_f32 v38, v38\n"
"v_rcp_f32 v39, v39\n"
"v_pk_mul_f32 v[140:141], v[140:141], v[36:37]\n"
"v_pk_mul_f32 v[142:143], v[142:143], v[38:39]\n"
"v_cvt_pk_bf16_f32 v42, v140, v141\n"
"v_cvt_pk_bf16_f32 v43, v142, v143\n"
"buffer_store_dwordx4 v[40:43], v5, s[48:51], s86 offen offset:0 sc1\n"
"buffer_load_dwordx4 a[20:23], v17, s[48:51], s59 offen sc1\n"
"v_pk_mul_f32 v[36:37], v[144:145], s[64:65] op_sel_hi:[1,0]\n"
"v_pk_mul_f32 v[38:39], v[146:147], s[64:65] op_sel_hi:[1,0]\n"
"v_exp_f32 v36, v36\n"
"v_exp_f32 v37, v37\n"
"v_pk_mul_f32 v[148:149], v[144:145], v[148:149]\n"
"v_exp_f32 v38, v38\n"
"v_exp_f32 v39, v39\n"
"v_pk_mul_f32 v[150:151], v[146:147], v[150:151]\n"
"buffer_load_dwordx4 a[24:27], v18, s[48:51], s59 offen sc1\n"
"v_pk_add_f32 v[36:37], v[36:37], s[64:65] op_sel:[0,1] op_sel_hi:[1,1]\n"
"v_pk_add_f32 v[38:39], v[38:39], s[64:65] op_sel:[0,1] op_sel_hi:[1,1]\n"
"v_pk_mul_f32 v[148:149], v[148:149], v[34:35] op_sel_hi:[1,0]\n"
"v_pk_mul_f32 v[150:151], v[150:151], v[34:35] op_sel_hi:[1,0]\n"
"v_rcp_f32 v36, v36\n"
"v_rcp_f32 v37, v37\n"
"v_rcp_f32 v38, v38\n"
"v_rcp_f32 v39, v39\n"
"v_pk_mul_f32 v[148:149], v[148:149], v[36:37]\n"
"buffer_load_dwordx4 a[28:31], v19, s[48:51], s59 offen sc1\n"
"v_pk_mul_f32 v[150:151], v[150:151], v[38:39]\n"
"v_cvt_pk_bf16_f32 v44, v148, v149\n"
"v_cvt_pk_bf16_f32 v45, v150, v151\n"
"v_pk_mul_f32 v[36:37], v[152:153], s[64:65] op_sel_hi:[1,0]\n"
"v_pk_mul_f32 v[38:39], v[154:155], s[64:65] op_sel_hi:[1,0]\n"
"v_exp_f32 v36, v36\n"
"v_exp_f32 v37, v37\n"
"v_pk_mul_f32 v[156:157], v[152:153], v[156:157]\n"
"buffer_load_dwordx4 v[240:243], v6, s[48:51], s7 offen offset:1024 sc1\n"
"v_exp_f32 v38, v38\n"
"v_exp_f32 v39, v39\n"
"v_pk_mul_f32 v[158:159], v[154:155], v[158:159]\n"
"v_pk_add_f32 v[36:37], v[36:37], s[64:65] op_sel:[0,1] op_sel_hi:[1,1]\n"
"v_pk_add_f32 v[38:39], v[38:39], s[64:65] op_sel:[0,1] op_sel_hi:[1,1]\n"
"v_pk_mul_f32 v[156:157], v[156:157], v[34:35] op_sel_hi:[1,0]\n"
"v_pk_mul_f32 v[158:159], v[158:159], v[34:35] op_sel_hi:[1,0]\n"
"v_rcp_f32 v36, v36\n"
"buffer_load_dwordx4 v[244:247], v6, s[48:51], s7 offen offset:2048 sc1\n"
"v_rcp_f32 v37, v37\n"
"v_rcp_f32 v38, v38\n"
"v_rcp_f32 v39, v39\n"
"v_pk_mul_f32 v[156:157], v[156:157], v[36:37]\n"
"v_pk_mul_f32 v[158:159], v[158:159], v[38:39]\n"
"v_cvt_pk_bf16_f32 v46, v156, v157\n"
"v_cvt_pk_bf16_f32 v47, v158, v159\n"
"buffer_store_dwordx4 v[44:47], v5, s[48:51], s86 offen offset:512 sc1\n"
"buffer_load_dwordx4 v[248:251], v6, s[48:51], s7 offen offset:3072 sc1\n"
"v_pk_mul_f32 v[36:37], v[160:161], s[64:65] op_sel_hi:[1,0]\n"
"v_pk_mul_f32 v[38:39], v[162:163], s[64:65] op_sel_hi:[1,0]\n"
"v_exp_f32 v36, v36\n"
"v_exp_f32 v37, v37\n"
"v_pk_mul_f32 v[164:165], v[160:161], v[164:165]\n"
"v_exp_f32 v38, v38\n"
"v_exp_f32 v39, v39\n"
"v_pk_mul_f32 v[166:167], v[162:163], v[166:167]\n"
"s_add_u32 s7, s7, 1024\n"
"buffer_load_dwordx4 v[252:255], v6, s[48:51], s7 offen offset:3072 sc1\n"
"v_pk_add_f32 v[36:37], v[36:37], s[64:65] op_sel:[0,1] op_sel_hi:[1,1]\n"
"v_pk_add_f32 v[38:39], v[38:39], s[64:65] op_sel:[0,1] op_sel_hi:[1,1]\n"
"v_pk_mul_f32 v[164:165], v[164:165], v[34:35] op_sel_hi:[1,0]\n"
"v_pk_mul_f32 v[166:167], v[166:167], v[34:35] op_sel_hi:[1,0]\n"
"v_rcp_f32 v36, v36\n"
"v_rcp_f32 v37, v37\n"
"v_rcp_f32 v38, v38\n"
"v_rcp_f32 v39, v39\n"
"v_pk_mul_f32 v[164:165], v[164:165], v[36:37]\n"
"v_pk_mul_f32 v[166:167], v[166:167], v[38:39]\n"
"v_cvt_pk_bf16_f32 v40, v164, v165\n"
"v_cvt_pk_bf16_f32 v41, v166, v167\n"
"v_pk_mul_f32 v[36:37], v[168:169], s[64:65] op_sel_hi:[1,0]\n"
"v_pk_mul_f32 v[38:39], v[170:171], s[64:65] op_sel_hi:[1,0]\n"
"v_exp_f32 v36, v36\n"
"v_exp_f32 v37, v37\n"
"v_pk_mul_f32 v[172:173], v[168:169], v[172:173]\n"
"v_exp_f32 v38, v38\n"
"v_exp_f32 v39, v39\n"
"v_pk_mul_f32 v[174:175], v[170:171], v[174:175]\n"
"v_pk_add_f32 v[36:37], v[36:37], s[64:65] op_sel:[0,1] op_sel_hi:[1,1]\n"
"v_pk_add_f32 v[38:39], v[38:39], s[64:65] op_sel:[0,1] op_sel_hi:[1,1]\n"
"v_pk_mul_f32 v[172:173], v[172:173], v[34:35] op_sel_hi:[1,0]\n"
"v_pk_mul_f32 v[174:175], v[174:175], v[34:35] op_sel_hi:[1,0]\n"
"v_rcp_f32 v36, v36\n"
"v_rcp_f32 v37, v37\n"
"v_rcp_f32 v38, v38\n"
"v_rcp_f32 v39, v39\n"
"v_pk_mul_f32 v[172:173], v[172:173], v[36:37]\n"
"v_pk_mul_f32 v[174:175], v[174:175], v[38:39]\n"
"v_cvt_pk_bf16_f32 v42, v172, v173\n"
"v_cvt_pk_bf16_f32 v43, v174, v175\n"
"buffer_store_dwordx4 v[40:43], v5, s[48:51], s86 offen offset:1024 sc1\n"
"v_pk_mul_f32 v[36:37], v[176:177], s[64:65] op_sel_hi:[1,0]\n"
"v_pk_mul_f32 v[38:39], v[178:179], s[64:65] op_sel_hi:[1,0]\n"
"v_exp_f32 v36, v36\n"
"v_exp_f32 v37, v37\n"
"v_pk_mul_f32 v[180:181], v[176:177], v[180:181]\n"
"v_exp_f32 v38, v38\n"
"v_exp_f32 v39, v39\n"
"v_pk_mul_f32 v[182:183], v[178:179], v[182:183]\n"
"v_pk_add_f32 v[36:37], v[36:37], s[64:65] op_sel:[0,1] op_sel_hi:[1,1]\n"
"v_pk_add_f32 v[38:39], v[38:39], s[64:65] op_sel:[0,1] op_sel_hi:[1,1]\n"
"v_pk_mul_f32 v[180:181], v[180:181], v[34:35] op_sel_hi:[1,0]\n"
"v_pk_mul_f32 v[182:183], v[182:183], v[34:35] op_sel_hi:[1,0]\n"
"v_rcp_f32 v36, v36\n"
"v_rcp_f32 v37, v37\n"
"v_rcp_f32 v38, v38\n"
"v_rcp_f32 v39, v39\n"
"v_pk_mul_f32 v[180:181], v[180:181], v[36:37]\n"
"v_pk_mul_f32 v[182:183], v[182:183], v[38:39]\n"
"v_cvt_pk_bf16_f32 v44, v180, v181\n"
"v_cvt_pk_bf16_f32 v45, v182, v183\n"
"v_pk_mul_f32 v[36:37], v[184:185], s[64:65] op_sel_hi:[1,0]\n"
"v_pk_mul_f32 v[38:39], v[186:187], s[64:65] op_sel_hi:[1,0]\n"
"v_exp_f32 v36, v36\n"
"v_exp_f32 v37, v37\n"
"v_pk_mul_f32 v[188:189], v[184:185], v[188:189]\n"
"v_exp_f32 v38, v38\n"
"v_exp_f32 v39, v39\n"
"v_pk_mul_f32 v[190:191], v[186:187], v[190:191]\n"
"v_pk_add_f32 v[36:37], v[36:37], s[64:65] op_sel:[0,1] op_sel_hi:[1,1]\n"
"v_pk_add_f32 v[38:39], v[38:39], s[64:65] op_sel:[0,1] op_sel_hi:[1,1]\n"
"v_pk_mul_f32 v[188:189], v[188:189], v[34:35] op_sel_hi:[1,0]\n"
"v_pk_mul_f32 v[190:191], v[190:191], v[34:35] op_sel_hi:[1,0]\n"
"v_rcp_f32 v36, v36\n"
"v_rcp_f32 v37, v37\n"
"v_rcp_f32 v38, v38\n"
"v_rcp_f32 v39, v39\n"
"v_pk_mul_f32 v[188:189], v[188:189], v[36:37]\n"
"v_pk_mul_f32 v[190:191], v[190:191], v[38:39]\n"
"v_cvt_pk_bf16_f32 v46, v188, v189\n"
"v_cvt_pk_bf16_f32 v47, v190, v191\n"
"buffer_store_dwordx4 v[44:47], v5, s[48:51], s86 offen offset:1536 sc1\n"
"s_add_u32 s7, s4, 20480\n"
"buffer_load_dwordx4 a[80:83], v6, s[48:51], s7 offen offset:2048 sc1\n"
"buffer_load_dwordx4 a[84:87], v6, s[48:51], s7 offen offset:3072 sc1\n"
"s_add_u32 s7, s7, 1024\n"
"buffer_load_dwordx4 a[88:91], v6, s[48:51], s7 offen offset:3072 sc1\n"
"s_add_u32 s7, s4, 20480\n"
"s_add_u32 s7, s7, s1\n"
"buffer_load_dwordx4 a[92:95], v6, s[48:51], s7 offen offset:0 sc1\n"
"s_waitcnt vmcnt(29)\n"
"v_mfma_f32_16x16x32_bf16 v[128:131], a[160:163], v[192:195], 0\n"
"buffer_load_dwordx2 v[8:9], v32, s[48:51], s60 offen sc1\n"
"buffer_load_dwordx4 a[96:99], v6, s[48:51], s7 offen offset:1024 sc1\n"
"v_mfma_f32_16x16x32_bf16 v[144:147], a[164:167], v[192:195], 0\n"
"v_mfma_f32_16x16x32_bf16 v[160:163], a[168:171], v[192:195], 0\n"
"v_mfma_f32_16x16x32_bf16 v[176:179], a[172:175], v[192:195], 0\n"
"buffer_load_dwordx4 a[100:103], v6, s[48:51], s7 offen offset:2048 sc1\n"
"v_mfma_f32_16x16x32_bf16 v[224:227], a[176:179], v[192:195], 0\n"
"s_waitcnt vmcnt(31)\n"
"v_mfma_f32_16x16x32_bf16 v[132:135], a[160:163], v[196:199], 0\n"
"v_mfma_f32_16x16x32_bf16 v[148:151], a[164:167], v[196:199], 0\n"
"buffer_load_dwordx4 a[104:107], v6, s[48:51], s7 offen offset:3072 sc1\n"
"v_mfma_f32_16x16x32_bf16 v[164:167], a[168:171], v[196:199], 0\n"
"v_mfma_f32_16x16x32_bf16 v[180:183], a[172:175], v[196:199], 0\n"
"v_mfma_f32_16x16x32_bf16 v[228:231], a[176:179], v[196:199], 0\n"
"s_waitcnt vmcnt(31)\n"
"v_mfma_f32_16x16x32_bf16 v[136:139], a[160:163], v[200:203], 0\n"
"s_add_u32 s7, s7, 1024\n"
"buffer_load_dwordx4 a[108:111], v6, s[48:51], s7 offen offset:3072 sc1\n"
"v_mfma_f32_16x16x32_bf16 v[152:155], a[164:167], v[200:203], 0\n"
"v_mfma_f32_16x16x32_bf16 v[168:171], a[168:171], v[200:203], 0\n"
"v_mfma_f32_16x16x32_bf16 v[184:187], a[172:175], v[200:203], 0\n"
"s_add_u32 s7, s13, 20480\n"
"buffer_load_dwordx4 a[112:115], v6, s[48:51], s7 offen offset:0 sc1\n"
"v_mfma_f32_16x16x32_bf16 v[232:235], a[176:179], v[200:203], 0\n"
"s_waitcnt vmcnt(32)\n"
"v_mfma_f32_16x16x32_bf16 v[140:143], a[160:163], v[204:207], 0\n"
"v_mfma_f32_16x16x32_bf16 v[156:159], a[164:167], v[204:207], 0\n"
"buffer_load_dwordx4 a[116:119], v6, s[48:51], s7 offen offset:1024 sc1\n"
"v_mfma_f32_16x16x32_bf16 v[172:175], a[168:171], v[204:207], 0\n"
"v_mfma_f32_16x16x32_bf16 v[188:191], a[172:175], v[204:207], 0\n"
"v_mfma_f32_16x16x32_bf16 v[236:239], a[176:179], v[204:207], 0\n"
"s_waitcnt vmcnt(32)\n"
"v_mfma_f32_16x16x32_bf16 v[128:131], a[180:183], v[208:211], v[128:131]\n"
"buffer_load_dwordx4 a[120:123], v6, s[48:51], s7 offen offset:2048 sc1\n"
"v_mfma_f32_16x16x32_bf16 v[144:147], a[184:187], v[208:211], v[144:147]\n"
"v_mfma_f32_16x16x32_bf16 v[160:163], a[188:191], v[208:211], v[160:163]\n"
"v_mfma_f32_16x16x32_bf16 v[176:179], a[192:195], v[208:211], v[176:179]\n"
"buffer_load_dwordx4 a[124:127], v6, s[48:51], s7 offen offset:3072 sc1\n"
"v_mfma_f32_16x16x32_bf16 v[224:227], a[196:199], v[208:211], v[224:227]\n"
"s_waitcnt vmcnt(33)\n"
"v_mfma_f32_16x16x32_bf16 v[132:135], a[180:183], v[212:215], v[132:135]\n"
"v_mfma_f32_16x16x32_bf16 v[148:151], a[184:187], v[212:215], v[148:151]\n"
"s_add_u32 s7, s7, 1024\n"
"buffer_load_dwordx4 a[128:131], v6, s[48:51], s7 offen offset:3072 sc1\n"
"v_mfma_f32_16x16x32_bf16 v[164:167], a[188:191], v[212:215], v[164:167]\n"
"v_mfma_f32_16x16x32_bf16 v[180:183], a[192:195], v[212:215], v[180:183]\n"
"v_mfma_f32_16x16x32_bf16 v[228:231], a[196:199], v[212:215], v[228:231]\n"
"s_add_u32 s7, s13, 20480\n"
"s_add_u32 s7, s7, s1\n"
"buffer_load_dwordx4 a[132:135], v6, s[48:51], s7 offen offset:0 sc1\n"
"s_waitcnt vmcnt(34)\n"
"v_mfma_f32_16x16x32_bf16 v[136:139], a[180:183], v[216:219], v[136:139]\n"
"s_lshl_b32 s94, 0xaa, s97\n"
"s_lshl_b32 s95, 0x0, s97\n"
"s_waitcnt vmcnt(10)\n"
"v_cmp_eq_u64 vcc, s[84:85], v[8:9]\n"
"s_and_b64 s[10:11], vcc, s[94:95]\n"
"s_cmp_eq_u64 s[10:11], s[94:95]\n"
"s_cbranch_scc0 L_ffn_f_slow\n"
"L_ffn_f_ok:\n"
"s_barrier\n"
"buffer_store_dwordx2 v[2:3], v4, s[48:51], s87 offen offset:16 sc1\n"
"v_mfma_f32_16x16x32_bf16 v[152:155], a[184:187], v[216:219], v[152:155]\n"
"v_mfma_f32_16x16x32_bf16 v[168:171], a[188:191], v[216:219], v[168:171]\n"
"v_mfma_f32_16x16x32_bf16 v[184:187], a[192:195], v[216:219], v[184:187]\n"
"s_add_u32 s59, s56, 2048\n"
"buffer_load_dwordx4 v[80:83], v16, s[48:51], s59 offen sc1\n"
"v_mfma_f32_16x16x32_bf16 v[232:235], a[196:199], v[216:219], v[232:235]\n"
"v_mfma_f32_16x16x32_bf16 v[140:143], a[180:183], v[220:223], v[140:143]\n"
"v_mfma_f32_16x16x32_bf16 v[156:159], a[184:187], v[220:223], v[156:159]\n"
"buffer_load_dwordx4 v[84:87], v17, s[48:51], s59 offen sc1\n"
"v_mfma_f32_16x16x32_bf16 v[172:175], a[188:191], v[220:223], v[172:175]\n"
"v_mfma_f32_16x16x32_bf16 v[188:191], a[192:195], v[220:223], v[188:191]\n"
"v_mfma_f32_16x16x32_bf16 v[236:239], a[196:199], v[220:223], v[236:239]\n"
"v_mfma_f32_16x16x32_bf16 v[128:131], a[200:203], a[0:3], v[128:131]\n"
"buffer_load_dwordx4 v[88:91], v18, s[48:51], s59 offen sc1\n"
"v_mfma_f32_16x16x32_bf16 v[144:147], a[204:207], a[0:3], v[144:147]\n"
"s_lshl_b32 s82, s76, 14\n"
"v_mfma_f32_16x16x32_bf16 v[160:163], a[208:211], a[0:3], v[160:163]\n"
"s_lshl_b32 s83, s88, 8\n"
"v_mfma_f32_16x16x32_bf16 v[176:179], a[212:215], a[0:3], v[176:179]\n"
"buffer_load_dwordx4 v[92:95], v19, s[48:51], s59 offen sc1\n"
"v_mfma_f32_16x16x32_bf16 v[224:227], a[216:219], a[0:3], v[224:227]\n"
"s_add_u32 s82, s82, s83\n"
"v_mfma_f32_16x16x32_bf16 v[132:135], a[200:203], a[4:7], v[132:135]\n"
"s_add_u32 s82, s82, 16384\n"
"v_mfma_f32_16x16x32_bf16 v[148:151], a[204:207], a[4:7], v[148:151]\n"
"s_add_u32 s59, s56, 2048\n"
"s_add_u32 s59, s59, s63\n"
"buffer_load_dwordx4 v[96:99], v16, s[48:51], s59 offen sc1\n"
"v_mfma_f32_16x16x32_bf16 v[164:167], a[208:211], a[4:7], v[164:167]\n"
"v_and_b32 v12, 15, v1\n"
"v_mfma_f32_16x16x32_bf16 v[180:183], a[212:215], a[4:7], v[180:183]\n"
"v_lshlrev_b32 v12, 2, v12\n"
"v_mfma_f32_16x16x32_bf16 v[228:231], a[216:219], a[4:7], v[228:231]\n"
"s_add_u32 s83, s76, 0\n"
"s_and_b32 s83, s83, 3\n"
"v_mfma_f32_16x16x32_bf16 v[136:139], a[200:203], a[8:11], v[136:139]\n"
"buffer_load_dwordx4 v[100:103], v17, s[48:51], s59 offen sc1\n"
"v_mfma_f32_16x16x32_bf16 v[152:155], a[204:207], a[8:11], v[152:155]\n"
"s_lshl_b32 s83, s83, 6\n"
"v_mfma_f32_16x16x32_bf16 v[168:171], a[208:211], a[8:11], v[168:171]\n"
"s_add_u32 s83, s83, s82\n"
"v_mfma_f32_16x16x32_bf16 v[184:187], a[212:215], a[8:11], v[184:187]\n"
"buffer_load_dwordx4 v[104:107], v18, s[48:51], s59 offen sc1\n"
"v_mfma_f32_16x16x32_bf16 v[232:235], a[216:219], a[8:11], v[232:235]\n"
"v_mfma_f32_16x16x32_bf16 v[140:143], a[200:203], a[12:15], v[140:143]\n"
"buffer_load_dword v21, v12, s[48:51], s83 offen sc1\n"
"v_mfma_f32_16x16x32_bf16 v[156:159], a[204:207], a[12:15], v[156:159]\n"
"buffer_load_dwordx4 v[108:111], v19, s[48:51], s59 offen sc1\n"
"v_mfma_f32_16x16x32_bf16 v[172:175], a[208:211], a[12:15], v[172:175]\n"
"s_add_u32 s83, s76, 1\n"
"v_mfma_f32_16x16x32_bf16 v[188:191], a[212:215], a[12:15], v[188:191]\n"
"s_and_b32 s83, s83, 3\n"
"v_mfma_f32_16x16x32_bf16 v[236:239], a[216:219], a[12:15], v[236:239]\n"
"s_lshl_b32 s83, s83, 6\n"
"v_mfma_f32_16x16x32_bf16 v[128:131], a[220:223], a[16:19], v[128:131]\n"
"s_add_u32 s59, s62, 2048\n"
"buffer_load_dwordx4 v[192:195], v16, s[48:51], s59 offen sc1\n"
"v_mfma_f32_16x16x32_bf16 v[144:147], a[224:227], a[16:19], v[144:147]\n"
"s_add_u32 s83, s83, s82\n"
"v_mfma_f32_16x16x32_bf16 v[160:163], a[228:231], a[16:19], v[160:163]\n"
"buffer_load_dword v22, v12, s[48:51], s83 offen sc1\n"
"v_mfma_f32_16x16x32_bf16 v[176:179], a[232:235], a[16:19], v[176:179]\n"
"buffer_load_dwordx4 v[196:199], v17, s[48:51], s59 offen sc1\n"
"v_mfma_f32_16x16x32_bf16 v[224:227], a[236:239], a[16:19], v[224:227]\n"
"v_mfma_f32_16x16x32_bf16 v[132:135], a[220:223], a[20:23], v[132:135]\n"
"s_add_u32 s83, s76, 2\n"
"v_mfma_f32_16x16x32_bf16 v[148:151], a[224:227], a[20:23], v[148:151]\n"
"buffer_load_dwordx4 v[200:203], v18, s[48:51], s59 offen sc1\n"
"v_mfma_f32_16x16x32_bf16 v[164:167], a[228:231], a[20:23], v[164:167]\n"
"s_and_b32 s83, s83, 3\n"
"v_mfma_f32_16x16x32_bf16 v[180:183], a[232:235], a[20:23], v[180:183]\n"
"s_lshl_b32 s83, s83, 6\n"
"v_mfma_f32_16x16x32_bf16 v[228:231], a[236:239], a[20:23], v[228:231]\n"
"s_add_u32 s83, s83, s82\n"
"v_mfma_f32_16x16x32_bf16 v[136:139], a[220:223], a[24:27], v[136:139]\n"
"buffer_load_dwordx4 v[204:207], v19, s[48:51], s59 offen sc1\n"
"v_mfma_f32_16x16x32_bf16 v[152:155], a[224:227], a[24:27], v[152:155]\n"
"buffer_load_dword v23, v12, s[48:51], s83 offen sc1\n"
"v_mfma_f32_16x16x32_bf16 v[168:171], a[228:231], a[24:27], v[168:171]\n"
"s_add_u32 s83, s76, 3\n"
"v_mfma_f32_16x16x32_bf16 v[184:187], a[232:235], a[24:27], v[184:187]\n"
"s_add_u32 s59, s62, 2048\n"
"s_add_u32 s59, s59, s63\n"
"buffer_load_dwordx4 v[208:211], v16, s[48:51], s59 offen sc1\n"
"v_mfma_f32_16x16x32_bf16 v[232:235], a[236:239], a[24:27], v[232:235]\n"
"s_and_b32 s83, s83, 3\n"
"v_mfma_f32_16x16x32_bf16 v[140:143], a[220:223], a[28:31], v[140:143]\n"
"s_lshl_b32 s83, s83, 6\n"
"v_mfma_f32_16x16x32_bf16 v[156:159], a[224:227], a[28:31], v[156:159]\n"
"buffer_load_dwordx4 v[212:215], v17, s[48:51], s59 offen sc1\n"
"v_mfma_f32_16x16x32_bf16 v[172:175], a[228:231], a[28:31], v[172:175]\n"
"s_add_u32 s83, s83, s82\n"
"v_mfma_f32_16x16x32_bf16 v[188:191], a[232:235], a[28:31], v[188:191]\n"
"v_mfma_f32_16x16x32_bf16 v[236:239], a[236:239], a[28:31], v[236:239]\n"
"buffer_load_dword v24, v12, s[48:51], s83 offen sc1\n"
"s_waitcnt vmcnt(17)\n"
"v_mfma_f32_16x16x32_bf16 v[128:131], a[240:243], v[80:83], v[128:131]\n"
"buffer_load_dwordx4 v[216:219], v18, s[48:51], s59 offen sc1\n"
"v_mfma_f32_16x16x32_bf16 v[144:147], a[244:247], v[80:83], v[144:147]\n"
"v_mfma_f32_16x16x32_bf16 v[160:163], a[248:251], v[80:83], v[160:163]\n"
"v_mfma_f32_16x16x32_bf16 v[176:179], a[252:255], v[80:83], v[176:179]\n"
"buffer_load_dwordx4 v[220:223], v19, s[48:51], s59 offen sc1\n"
"v_mfma_f32_16x16x32_bf16 v[224:227], v[112:115], v[80:83], v[224:227]\n"
"s_waitcnt vmcnt(18)\n"
"v_mfma_f32_16x16x32_bf16 v[132:135], a[240:243], v[84:87], v[132:135]\n"
"v_mfma_f32_16x16x32_bf16 v[148:151], a[244:247], v[84:87], v[148:151]\n"
"s_add_u32 s59, s57, 0\n"
"buffer_load_dwordx4 a[0:3], v16, s[48:51], s59 offen sc1\n"
"v_mfma_f32_16x16x32_bf16 v[164:167], a[248:251], v[84:87], v[164:167]\n"
"v_mfma_f32_16x16x32_bf16 v[180:183], a[252:255], v[84:87], v[180:183]\n"
"v_mfma_f32_16x16x32_bf16 v[228:231], v[112:115], v[84:87], v[228:231]\n"
"buffer_load_dwordx4 a[4:7], v17, s[48:51], s59 offen sc1\n"
"s_waitcnt vmcnt(19)\n"
"v_mfma_f32_16x16x32_bf16 v[136:139], a[240:243], v[88:91], v[136:139]\n"
"v_mfma_f32_16x16x32_bf16 v[152:155], a[244:247], v[88:91], v[152:155]\n"
"v_mfma_f32_16x16x32_bf16 v[168:171], a[248:251], v[88:91], v[168:171]\n"
"v_mfma_f32_16x16x32_bf16 v[184:187], a[252:255], v[88:91], v[184:187]\n"
"buffer_load_dwordx4 a[8:11], v18, s[48:51], s59 offen sc1\n"
"v_mfma_f32_16x16x32_bf16 v[232:235], v[112:115], v[88:91], v[232:235]\n"
"s_waitcnt vmcnt(19)\n"
"v_mfma_f32_16x16x32_bf16 v[140:143], a[240:243], v[92:95], v[140:143]\n"
"buffer_load_dwordx2 v[8:9], v32, s[48:51], s60 offen sc1\n"
"v_mfma_f32_16x16x32_bf16 v[156:159], a[244:247], v[92:95], v[156:159]\n"
"buffer_load_dwordx4 a[12:15], v19, s[48:51], s59 offen sc1\n"
"v_mfma_f32_16x16x32_bf16 v[172:175], a[248:251], v[92:95], v[172:175]\n"
"v_mfma_f32_16x16x32_bf16 v[188:191], a[252:255], v[92:95], v[188:191]\n"
"v_mfma_f32_16x16x32_bf16 v[236:239], v[112:115], v[92:95], v[236:239]\n"
"s_add_u32 s59, s57, 0\n"
"s_add_u32 s59, s59, s63\n"
"buffer_load_dwordx4 a[16:19], v16, s[48:51], s59 offen sc1\n"
"s_waitcnt vmcnt(21)\n"
"v_mfma_f32_16x16x32_bf16 v[128:131], v[116:119], v[96:99], v[128:131]\n"
"v_mfma_f32_16x16x32_bf16 v[144:147], v[120:123], v[96:99], v[144:147]\n"
"v_mfma_f32_16x16x32_bf16 v[160:163], v[124:127], v[96:99], v[160:163]\n"
"v_mfma_f32_16x16x32_bf16 v[176:179], v[48:51], v[96:99], v[176:179]\n"
"buffer_load_dwordx4 a[20:23], v17, s[48:51], s59 offen sc1\n"
"v_mfma_f32_16x16x32_bf16 v[224:227], v[52:55], v[96:99], v[224:227]\n"
"s_waitcnt vmcnt(21)\n"
"v_mfma_f32_16x16x32_bf16 v[132:135], v[116:119], v[100:103], v[132:135]\n"
"v_mfma_f32_16x16x32_bf16 v[148:151], v[120:123], v[100:103], v[148:151]\n"
"buffer_load_dwordx4 a[24:27], v18, s[48:51], s59 offen sc1\n"
"v_mfma_f32_16x16x32_bf16 v[164:167], v[124:127], v[100:103], v[164:167]\n"
"v_mfma_f32_16x16x32_bf16 v[180:183], v[48:51], v[100:103], v[180:183]\n"
"v_mfma_f32_16x16x32_bf16 v[228:231], v[52:55], v[100:103], v[228:231]\n"
"buffer_load_dwordx4 a[28:31], v19, s[48:51], s59 offen sc1\n"
"s_waitcnt vmcnt(22)\n"
"v_mfma_f32_16x16x32_bf16 v[136:139], v[116:119], v[104:107], v[136:139]\n"
"v_mfma_f32_16x16x32_bf16 v[152:155], v[120:123], v[104:107], v[152:155]\n"
"v_mfma_f32_16x16x32_bf16 v[168:171], v[124:127], v[104:107], v[168:171]\n"
"v_mfma_f32_16x16x32_bf16 v[184:187], v[48:51], v[104:107], v[184:187]\n"
"buffer_load_dwordx4 a[136:139], v6, s[48:51], s7 offen offset:1024 sc1\n"
"v_mfma_f32_16x16x32_bf16 v[232:235], v[52:55], v[104:107], v[232:235]\n"
"s_waitcnt vmcnt(21)\n"
"v_mfma_f32_16x16x32_bf16 v[140:143], v[116:119], v[108:111], v[140:143]\n"
"v_mfma_f32_16x16x32_bf16 v[156:159], v[120:123], v[108:111], v[156:159]\n"
"buffer_load_dwordx4 a[140:143], v6, s[48:51], s7 offen offset:2048 sc1\n"
"v_mfma_f32_16x16x32_bf16 v[172:175], v[124:127], v[108:111], v[172:175]\n"
"v_mfma_f32_16x16x32_bf16 v[188:191], v[48:51], v[108:111], v[188:191]\n"
"v_mfma_f32_16x16x32_bf16 v[236:239], v[52:55], v[108:111], v[236:239]\n"
"buffer_load_dwordx4 a[144:147], v6, s[48:51], s7 offen offset:3072 sc1\n"
"s_waitcnt vmcnt(22)\n"
"v_mfma_f32_16x16x32_bf16 v[128:131], v[56:59], v[192:195], v[128:131]\n"
"v_mfma_f32_16x16x32_bf16 v[144:147], v[60:63], v[192:195], v[144:147]\n"
"v_mfma_f32_16x16x32_bf16 v[160:163], v[64:67], v[192:195], v[160:163]\n"
"v_mfma_f32_16x16x32_bf16 v[176:179], v[68:71], v[192:195], v[176:179]\n"
"s_add_u32 s7, s7, 1024\n"
"buffer_load_dwordx4 a[148:151], v6, s[48:51], s7 offen offset:3072 sc1\n"
"v_mfma_f32_16x16x32_bf16 v[224:227], v[72:75], v[192:195], v[224:227]\n"
"s_waitcnt vmcnt(21)\n"
"v_mfma_f32_16x16x32_bf16 v[132:135], v[56:59], v[196:199], v[132:135]\n"
"s_lshl_b32 s94, 0x44, s97\n"
"s_lshl_b32 s95, 0x0, s97\n"
"s_waitcnt vmcnt(9)\n"
"v_cmp_eq_u64 vcc, s[84:85], v[8:9]\n"
"s_and_b64 s[10:11], vcc, s[94:95]\n"
"s_cmp_eq_u64 s[10:11], s[94:95]\n"
"s_cbranch_scc0 L_ffn_m_slow\n"
"L_ffn_m_ok:\n"
"v_mfma_f32_16x16x32_bf16 v[148:151], v[60:63], v[196:199], v[148:151]\n"
"s_add_u32 s59, s56, 4096\n"
"buffer_load_dwordx4 v[80:83], v16, s[48:51], s59 offen sc1\n"
"v_mfma_f32_16x16x32_bf16 v[164:167], v[64:67], v[196:199], v[164:167]\n"
"v_mfma_f32_16x16x32_bf16 v[180:183], v[68:71], v[196:199], v[180:183]\n"
"v_mfma_f32_16x16x32_bf16 v[228:231], v[72:75], v[196:199], v[228:231]\n"
"buffer_load_dwordx4 v[84:87], v17, s[48:51], s59 offen sc1\n"
"v_mfma_f32_16x16x32_bf16 v[136:139], v[56:59], v[200:203], v[136:139]\n"
"v_mfma_f32_16x16x32_bf16 v[152:155], v[60:63], v[200:203], v[152:155]\n"
"v_mfma_f32_16x16x32_bf16 v[168:171], v[64:67], v[200:203], v[168:171]\n"
"v_mfma_f32_16x16x32_bf16 v[184:187], v[68:71], v[200:203], v[184:187]\n"
"buffer_load_dwordx4 v[88:91], v18, s[48:51], s59 offen sc1\n"
"v_mfma_f32_16x16x32_bf16 v[232:235], v[72:75], v[200:203], v[232:235]\n"
"v_mfma_f32_16x16x32_bf16 v[140:143], v[56:59], v[204:207], v[140:143]\n"
"ds_read_b128 a[32:35], v27 offset:0\n"
"v_mfma_f32_16x16x32_bf16 v[156:159], v[60:63], v[204:207], v[156:159]\n"
"buffer_load_dwordx4 v[92:95], v19, s[48:51], s59 offen sc1\n"
"ds_read_b128 a[36:39], v27 offset:1024\n"
"v_mfma_f32_16x16x32_bf16 v[172:175], v[64:67], v[204:207], v[172:175]\n"
"ds_read_b128 a[40:43], v27 offset:2048\n"
"v_mfma_f32_16x16x32_bf16 v[188:191], v[68:71], v[204:207], v[188:191]\n"
"ds_read_b128 a[44:47], v27 offset:3072\n"
"v_mfma_f32_16x16x32_bf16 v[236:239], v[72:75], v[204:207], v[236:239]\n"
"s_add_u32 s59, s56, 4096\n"
"s_add_u32 s59, s59, s63\n"
"buffer_load_dwordx4 v[96:99], v16, s[48:51], s59 offen sc1\n"
"ds_read_b128 a[48:51], v27 offset:4096\n"
"v_mfma_f32_16x16x32_bf16 v[128:131], v[76:79], v[208:211], v[128:131]\n"
"v_mfma_f32_16x16x32_bf16 v[144:147], v[240:243], v[208:211], v[144:147]\n"
"v_mfma_f32_16x16x32_bf16 v[160:163], v[244:247], v[208:211], v[160:163]\n"
"v_mfma_f32_16x16x32_bf16 v[176:179], v[248:251], v[208:211], v[176:179]\n"
"buffer_load_dwordx4 v[100:103], v17, s[48:51], s59 offen sc1\n"
"v_mfma_f32_16x16x32_bf16 v[224:227], v[252:255], v[208:211], v[224:227]\n"
"v_mfma_f32_16x16x32_bf16 v[132:135], v[76:79], v[212:215], v[132:135]\n"
"v_mfma_f32_16x16x32_bf16 v[148:151], v[240:243], v[212:215], v[148:151]\n"
"buffer_load_dwordx4 v[104:107], v18, s[48:51], s59 offen sc1\n"
"v_mfma_f32_16x16x32_bf16 v[164:167], v[244:247], v[212:215], v[164:167]\n"
"v_mfma_f32_16x16x32_bf16 v[180:183], v[248:251], v[212:215], v[180:183]\n"
"v_mfma_f32_16x16x32_bf16 v[228:231], v[252:255], v[212:215], v[228:231]\n"
"v_mfma_f32_16x16x32_bf16 v[136:139], v[76:79], v[216:219], v[136:139]\n"
"buffer_load_dwordx4 v[108:111], v19, s[48:51], s59 offen sc1\n"
"v_mfma_f32_16x16x32_bf16 v[152:155], v[240:243], v[216:219], v[152:155]\n"
"v_mfma_f32_16x16x32_bf16 v[168:171], v[244:247], v[216:219], v[168:171]\n"
"v_mfma_f32_16x16x32_bf16 v[184:187], v[248:251], v[216:219], v[184:187]\n"
"s_add_u32 s59, s62, 4096\n"
"buffer_load_dwordx4 v[192:195], v16, s[48:51], s59 offen sc1\n"
"v_mfma_f32_16x16x32_bf16 v[232:235], v[252:255], v[216:219], v[232:235]\n"
"v_mfma_f32_16x16x32_bf16 v[140:143], v[76:79], v[220:223], v[140:143]\n"
"ds_read_b128 a[52:55], v27 offset:5120\n"
"v_mfma_f32_16x16x32_bf16 v[156:159], v[240:243], v[220:223], v[156:159]\n"
"buffer_load_dwordx4 v[196:199], v17, s[48:51], s59 offen sc1\n"
"ds_read_b128 a[56:59], v27 offset:6144\n"
"v_mfma_f32_16x16x32_bf16 v[172:175], v[244:247], v[220:223], v[172:175]\n"
"ds_read_b128 a[60:63], v27 offset:7168\n"
"v_mfma_f32_16x16x32_bf16 v[188:191], v[248:251], v[220:223], v[188:191]\n"
"ds_read_b128 a[64:67], v27 offset:8192\n"
"v_mfma_f32_16x16x32_bf16 v[236:239], v[252:255], v[220:223], v[236:239]\n"
"ds_read_b128 a[68:71], v27 offset:9216\n"
"s_waitcnt lgkmcnt(9)\n"
"v_mfma_f32_16x16x32_bf16 v[128:131], a[32:35], a[0:3], v[128:131]\n"
"buffer_load_dwordx4 v[200:203], v18, s[48:51], s59 offen sc1\n"
"s_waitcnt lgkmcnt(8)\n"
"v_mfma_f32_16x16x32_bf16 v[144:147], a[36:39], a[0:3], v[144:147]\n"
"s_waitcnt lgkmcnt(7)\n"
"v_mfma_f32_16x16x32_bf16 v[160:163], a[40:43], a[0:3], v[160:163]\n"
"s_waitcnt lgkmcnt(6)\n"
"v_mfma_f32_16x16x32_bf16 v[176:179], a[44:47], a[0:3], v[176:179]\n"
"buffer_load_dwordx4 v[204:207], v19, s[48:51], s59 offen sc1\n"
"s_waitcnt lgkmcnt(5)\n"
"v_mfma_f32_16x16x32_bf16 v[224:227], a[48:51], a[0:3], v[224:227]\n"
"v_mfma_f32_16x16x32_bf16 v[132:135], a[32:35], a[4:7], v[132:135]\n"
"v_mfma_f32_16x16x32_bf16 v[148:151], a[36:39], a[4:7], v[148:151]\n"
"s_add_u32 s59, s62, 4096\n"
"s_add_u32 s59, s59, s63\n"
"buffer_load_dwordx4 v[208:211], v16, s[48:51], s59 offen sc1\n"
"v_mfma_f32_16x16x32_bf16 v[164:167], a[40:43], a[4:7], v[164:167]\n"
"v_mfma_f32_16x16x32_bf16 v[180:183], a[44:47], a[4:7], v[180:183]\n"
"v_mfma_f32_16x16x32_bf16 v[228:231], a[48:51], a[4:7], v[228:231]\n"
"v_mfma_f32_16x16x32_bf16 v[136:139], a[32:35], a[8:11], v[136:139]\n"
"buffer_load_dwordx4 v[212:215], v17, s[48:51], s59 offen sc1\n"
"v_mfma_f32_16x16x32_bf16 v[152:155], a[36:39], a[8:11], v[152:155]\n"
"v_mfma_f32_16x16x32_bf16 v[168:171], a[40:43], a[8:11], v[168:171]\n"
"v_mfma_f32_16x16x32_bf16 v[184:187], a[44:47], a[8:11], v[184:187]\n"
"buffer_load_dwordx4 v[216:219], v18, s[48:51], s59 offen sc1\n"
"v_mfma_f32_16x16x32_bf16 v[232:235], a[48:51], a[8:11], v[232:235]\n"
"s_waitcnt vmcnt(23)\n"
"v_mfma_f32_16x16x32_bf16 v[140:143], a[32:35], a[12:15], v[140:143]\n"
"buffer_load_dwordx2 v[8:9], v32, s[48:51], s60 offen sc1\n"
"ds_read_b128 a[72:75], v27 offset:10240\n"
"v_mfma_f32_16x16x32_bf16 v[156:159], a[36:39], a[12:15], v[156:159]\n"
"buffer_load_dwordx4 v[220:223], v19, s[48:51], s59 offen sc1\n"
"ds_read_b128 a[76:79], v27 offset:11264\n"
"v_mfma_f32_16x16x32_bf16 v[172:175], a[40:43], a[12:15], v[172:175]\n"
"v_mfma_f32_16x16x32_bf16 v[188:191], a[44:47], a[12:15], v[188:191]\n"
"v_mfma_f32_16x16x32_bf16 v[236:239], a[48:51], a[12:15], v[236:239]\n"
"s_waitcnt vmcnt(24) lgkmcnt(6)\n"
"v_mfma_f32_16x16x32_bf16 v[128:131], a[52:55], a[16:19], v[128:131]\n"
"s_add_u32 s7, s6, 0\n"
"buffer_load_dwordx4 a[152:155], v6, s[48:51], s7 offen offset:0 sc1\n"
"s_waitcnt lgkmcnt(5)\n"
"v_mfma_f32_16x16x32_bf16 v[144:147], a[56:59], a[16:19], v[144:147]\n"
"s_waitcnt lgkmcnt(4)\n"
"v_mfma_f32_16x16x32_bf16 v[160:163], a[60:63], a[16:19], v[160:163]\n"
"s_waitcnt lgkmcnt(3)\n"
"v_mfma_f32_16x16x32_bf16 v[176:179], a[64:67], a[16:19], v[176:179]\n"
"buffer_load_dwordx4 a[156:159], v6, s[48:51], s7 offen offset:1024 sc1\n"
"s_waitcnt lgkmcnt(2)\n"
"v_mfma_f32_16x16x32_bf16 v[224:227], a[68:71], a[16:19], v[224:227]\n"
"s_waitcnt vmcnt(25)\n"
"v_mfma_f32_16x16x32_bf16 v[132:135], a[52:55], a[20:23], v[132:135]\n"
"v_mfma_f32_16x16x32_bf16 v[148:151], a[56:59], a[20:23], v[148:151]\n"
"buffer_load_dwordx4 a[160:163], v6, s[48:51], s7 offen offset:2048 sc1\n"
"v_mfma_f32_16x16x32_bf16 v[164:167], a[60:63], a[20:23], v[164:167]\n"
"v_mfma_f32_16x16x32_bf16 v[180:183], a[64:67], a[20:23], v[180:183]\n"
"v_mul_f32 v128, v21, v128\n"
"v_mul_f32 v129, v21, v129\n"
"v_mfma_f32_16x16x32_bf16 v[228:231], a[68:71], a[20:23], v[228:231]\n"
"v_mul_f32 v130, v21, v130\n"
"v_mul_f32 v131, v21, v131\n"
"v_mul_f32 v144, v21, v144\n"
"v_mul_f32 v145, v21, v145\n"
"s_waitcnt vmcnt(25)\n"
"v_mfma_f32_16x16x32_bf16 v[136:139], a[52:55], a[24:27], v[136:139]\n"
"buffer_load_dwordx4 a[164:167], v6, s[48:51], s7 offen offset:3072 sc1\n"
"v_mul_f32 v146, v21, v146\n"
"v_mul_f32 v147, v21, v147\n"
"v_mul_f32 v160, v21, v160\n"
"v_mul_f32 v161, v21, v161\n"
"v_mfma_f32_16x16x32_bf16 v[152:155], a[56:59], a[24:27], v[152:155]\n"
"v_mul_f32 v162, v21, v162\n"
"v_mul_f32 v163, v21, v163\n"
"v_mul_f32 v176, v21, v176\n"
"v_mul_f32 v177, v21, v177\n"
"v_mfma_f32_16x16x32_bf16 v[168:171], a[60:63], a[24:27], v[168:171]\n"
"v_mul_f32 v178, v21, v178\n"
"v_mul_f32 v179, v21, v179\n"
"v_mul_f32 v224, v21, v224\n"
"v_mul_f32 v225, v21, v225\n"
"v_mfma_f32_16x16x32_bf16 v[184:187], a[64:67], a[24:27], v[184:187]\n"
"s_add_u32 s7, s7, 1024\n"
"buffer_load_dwordx4 a[168:171], v6, s[48:51], s7 offen offset:3072 sc1\n"
"v_mul_f32 v226, v21, v226\n"
"v_mul_f32 v227, v21, v227\n"
"v_mul_f32 v132, v22, v132\n"
"v_mul_f32 v133, v22, v133\n"
"v_mfma_f32_16x16x32_bf16 v[232:235], a[68:71], a[24:27], v[232:235]\n"
"v_mul_f32 v134, v22, v134\n"
"v_mul_f32 v135, v22, v135\n"
"v_mul_f32 v148, v22, v148\n"
"v_mul_f32 v149, v22, v149\n"
"s_waitcnt vmcnt(26)\n"
"v_mfma_f32_16x16x32_bf16 v[140:143], a[52:55], a[28:31], v[140:143]\n"
"v_mul_f32 v150, v22, v150\n"
"v_mul_f32 v151, v22, v151\n"
"v_mul_f32 v164, v22, v164\n"
"v_mul_f32 v165, v22, v165\n"
"v_mfma_f32_16x16x32_bf16 v[156:159], a[56:59], a[28:31], v[156:159]\n"
"s_add_u32 s7, s6, 5120\n"
"s_cmp_ge_u32 s7, s12\n"
"s_cselect_b32 s91, 25600, 0\n"
"s_sub_u32 s7, s7, s91\n"
"buffer_load_dwordx4 a[172:175], v6, s[48:51], s7 offen offset:0 sc1\n"
"v_mul_f32 v166, v22, v166\n"
"v_mul_f32 v167, v22, v167\n"
"v_mul_f32 v180, v22, v180\n"
"v_mul_f32 v181, v22, v181\n"
"v_mfma_f32_16x16x32_bf16 v[172:175], a[60:63], a[28:31], v[172:175]\n"
"v_mul_f32 v182, v22, v182\n"
"v_mul_f32 v183, v22, v183\n"
"v_mul_f32 v228, v22, v228\n"
"v_mul_f32 v229, v22, v229\n"
"v_mfma_f32_16x16x32_bf16 v[188:191], a[64:67], a[28:31], v[188:191]\n"
"v_mul_f32 v230, v22, v230\n"
"v_mul_f32 v231, v22, v231\n"
"v_mul_f32 v136, v23, v136\n"
"v_mul_f32 v137, v23, v137\n"
"v_mfma_f32_16x16x32_bf16 v[236:239], a[68:71], a[28:31], v[236:239]\n"
"v_mul_f32 v138, v23, v138\n"
"v_mul_f32 v139, v23, v139\n"
"v_mul_f32 v152, v23, v152\n"
"v_mul_f32 v153, v23, v153\n"
"s_waitcnt vmcnt(22) lgkmcnt(1)\n"
"v_mfma_f32_16x16x32_bf16 v[128:131], a[72:75], v[80:83], v[128:131]\n"
"buffer_load_dwordx4 a[176:179], v6, s[48:51], s7 offen offset:1024 sc1\n"
"v_mul_f32 v154, v23, v154\n"
"v_mul_f32 v155, v23, v155\n"
"v_mul_f32 v168, v23, v168\n"
"v_mul_f32 v169, v23, v169\n"
"s_waitcnt lgkmcnt(0)\n"
"v_mfma_f32_16x16x32_bf16 v[144:147], a[76:79], v[80:83], v[144:147]\n"
"v_mul_f32 v170, v23, v170\n"
"v_mul_f32 v171, v23, v171\n"
"v_mul_f32 v184, v23, v184\n"
"v_mul_f32 v185, v23, v185\n"
"v_mfma_f32_16x16x32_bf16 v[160:163], a[80:83], v[80:83], v[160:163]\n"
"v_mul_f32 v186, v23, v186\n"
"v_mul_f32 v187, v23, v187\n"
"v_mul_f32 v232, v23, v232\n"
"v_mul_f32 v233, v23, v233\n"
"v_mfma_f32_16x16x32_bf16 v[176:179], a[84:87], v[80:83], v[176:179]\n"
"buffer_load_dwordx4 a[180:183], v6, s[48:51], s7 offen offset:2048 sc1\n"
"v_mul_f32 v234, v23, v234\n"
"v_mul_f32 v235, v23, v235\n"
"v_mul_f32 v140, v24, v140\n"
"v_mul_f32 v141, v24, v141\n"
"v_mfma_f32_16x16x32_bf16 v[224:227], a[88:91], v[80:83], v[224:227]\n"
"v_mul_f32 v142, v24, v142\n"
"v_mul_f32 v143, v24, v143\n"
"v_mul_f32 v156, v24, v156\n"
"v_mul_f32 v157, v24, v157\n"
"s_waitcnt vmcnt(23)\n"
"v_mfma_f32_16x16x32_bf16 v[132:135], a[72:75], v[84:87], v[132:135]\n"
"s_lshl_b32 s94, 0x0, s97\n"
"s_lshl_b32 s95, 0x22, s97\n"
"s_waitcnt vmcnt(9)\n"
"v_cmp_eq_u64 vcc, s[84:85], v[8:9]\n"
"s_and_b64 s[10:11], vcc, s[94:95]\n"
"s_cmp_eq_u64 s[10:11], s[94:95]\n"
"s_cbranch_scc0 L_ffn_o_slow\n"
"L_ffn_o_ok:\n"
"v_mul_f32 v158, v24, v158\n"
"v_mul_f32 v159, v24, v159\n"
"v_mul_f32 v172, v24, v172\n"
"v_mul_f32 v173, v24, v173\n"
"v_mfma_f32_16x16x32_bf16 v[148:151], a[76:79], v[84:87], v[148:151]\n"
"s_add_u32 s59, s58, 0\n"
"buffer_load_dwordx4 a[0:3], v16, s[48:51], s59 offen sc1\n"
"v_mul_f32 v174, v24, v174\n"
"v_mul_f32 v175, v24, v175\n"
"v_mul_f32 v188, v24, v188\n"
"v_mul_f32 v189, v24, v189\n"
"v_mfma_f32_16x16x32_bf16 v[164:167], a[80:83], v[84:87], v[164:167]\n"
"v_mul_f32 v190, v24, v190\n"
"v_mul_f32 v191, v24, v191\n"
"v_mul_f32 v236, v24, v236\n"
"v_mul_f32 v237, v24, v237\n"
"v_mfma_f32_16x16x32_bf16 v[180:183], a[84:87], v[84:87], v[180:183]\n"
"v_mul_f32 v238, v24, v238\n"
"v_mul_f32 v239, v24, v239\n"
"v_mfma_f32_16x16x32_bf16 v[228:231], a[88:91], v[84:87], v[228:231]\n"
"v_mfma_f32_16x16x32_bf16 v[136:139], a[72:75], v[88:91], v[136:139]\n"
"buffer_load_dwordx4 a[4:7], v17, s[48:51], s59 offen sc1\n"
"v_mfma_f32_16x16x32_bf16 v[152:155], a[76:79], v[88:91], v[152:155]\n"
"v_mfma_f32_16x16x32_bf16 v[168:171], a[80:83], v[88:91], v[168:171]\n"
"v_mfma_f32_16x16x32_bf16 v[184:187], a[84:87], v[88:91], v[184:187]\n"
"buffer_load_dwordx4 a[8:11], v18, s[48:51], s59 offen sc1\n"
"v_mfma_f32_16x16x32_bf16 v[232:235], a[88:91], v[88:91], v[232:235]\n"
"v_mfma_f32_16x16x32_bf16 v[140:143], a[72:75], v[92:95], v[140:143]\n"
"v_mfma_f32_16x16x32_bf16 v[156:159], a[76:79], v[92:95], v[156:159]\n"
"buffer_load_dwordx4 a[12:15], v19, s[48:51], s59 offen sc1\n"
"v_mfma_f32_16x16x32_bf16 v[172:175], a[80:83], v[92:95], v[172:175]\n"
"v_mfma_f32_16x16x32_bf16 v[188:191], a[84:87], v[92:95], v[188:191]\n"
"v_mfma_f32_16x16x32_bf16 v[236:239], a[88:91], v[92:95], v[236:239]\n"
"v_mfma_f32_16x16x32_bf16 v[128:131], a[92:95], v[96:99], v[128:131]\n"
"s_add_u32 s59, s58, 1024\n"
"s_cmp_ge_u32 s59, s66\n"
"s_cselect_b32 s91, 5120, 0\n"
"s_sub_u32 s59, s59, s91\n"
"buffer_load_dwordx4 a[16:19], v16, s[48:51], s59 offen sc1\n"
"v_mfma_f32_16x16x32_bf16 v[144:147], a[96:99], v[96:99], v[144:147]\n"
"v_mfma_f32_16x16x32_bf16 v[160:163], a[100:103], v[96:99], v[160:163]\n"
"v_mfma_f32_16x16x32_bf16 v[176:179], a[104:107], v[96:99], v[176:179]\n"
"buffer_load_dwordx4 a[184:187], v6, s[48:51], s7 offen offset:3072 sc1\n"
"v_mfma_f32_16x16x32_bf16 v[224:227], a[108:111], v[96:99], v[224:227]\n"
"v_mfma_f32_16x16x32_bf16 v[132:135], a[92:95], v[100:103], v[132:135]\n"
"v_mfma_f32_16x16x32_bf16 v[148:151], a[96:99], v[100:103], v[148:151]\n"
"s_add_u32 s7, s7, 1024\n"
"buffer_load_dwordx4 a[188:191], v6, s[48:51], s7 offen offset:3072 sc1\n"
"v_mfma_f32_16x16x32_bf16 v[164:167], a[100:103], v[100:103], v[164:167]\n"
"v_mfma_f32_16x16x32_bf16 v[180:183], a[104:107], v[100:103], v[180:183]\n"
"v_mfma_f32_16x16x32_bf16 v[228:231], a[108:111], v[100:103], v[228:231]\n"
"v_mfma_f32_16x16x32_bf16 v[136:139], a[92:95], v[104:107], v[136:139]\n"
"buffer_load_dwordx4 a[20:23], v17, s[48:51], s59 offen sc1\n"
"v_mfma_f32_16x16x32_bf16 v[152:155], a[96:99], v[104:107], v[152:155]\n"
"v_mfma_f32_16x16x32_bf16 v[168:171], a[100:103], v[104:107], v[168:171]\n"
"v_mfma_f32_16x16x32_bf16 v[184:187], a[104:107], v[104:107], v[184:187]\n"
"buffer_load_dwordx4 a[24:27], v18, s[48:51], s59 offen sc1\n"
"v_mfma_f32_16x16x32_bf16 v[232:235], a[108:111], v[104:107], v[232:235]\n"
"v_mfma_f32_16x16x32_bf16 v[140:143], a[92:95], v[108:111], v[140:143]\n"
"v_mfma_f32_16x16x32_bf16 v[156:159], a[96:99], v[108:111], v[156:159]\n"
"buffer_load_dwordx4 a[28:31], v19, s[48:51], s59 offen sc1\n"
"v_mfma_f32_16x16x32_bf16 v[172:175], a[100:103], v[108:111], v[172:175]\n"
"v_mfma_f32_16x16x32_bf16 v[188:191], a[104:107], v[108:111], v[188:191]\n"
"v_mfma_f32_16x16x32_bf16 v[236:239], a[108:111], v[108:111], v[236:239]\n"
"v_mfma_f32_16x16x32_bf16 v[128:131], a[112:115], v[192:195], v[128:131]\n"
"s_add_u32 s7, s6, 10240\n"
"s_cmp_ge_u32 s7, s12\n"
"s_cselect_b32 s91, 25600, 0\n"
"s_sub_u32 s7, s7, s91\n"
"buffer_load_dwordx4 a[192:195], v6, s[48:51], s7 offen offset:0 sc1\n"
"v_mfma_f32_16x16x32_bf16 v[144:147], a[116:119], v[192:195], v[144:147]\n"
"v_mfma_f32_16x16x32_bf16 v[160:163], a[120:123], v[192:195], v[160:163]\n"
"v_mfma_f32_16x16x32_bf16 v[176:179], a[124:127], v[192:195], v[176:179]\n"
"s_add_u32 s59, s58, 2048\n"
"s_cmp_ge_u32 s59, s66\n"
"s_cselect_b32 s91, 5120, 0\n"
"s_sub_u32 s59, s59, s91\n"
"buffer_load_dwordx4 v[80:83], v16, s[48:51], s59 offen sc1\n"
"v_mfma_f32_16x16x32_bf16 v[224:227], a[128:131], v[192:195], v[224:227]\n"
"v_mfma_f32_16x16x32_bf16 v[132:135], a[112:115], v[196:199], v[132:135]\n"
"v_mfma_f32_16x16x32_bf16 v[148:151], a[116:119], v[196:199], v[148:151]\n"
"buffer_load_dwordx4 a[196:199], v6, s[48:51], s7 offen offset:1024 sc1\n"
"v_mfma_f32_16x16x32_bf16 v[164:167], a[120:123], v[196:199], v[164:167]\n"
"v_mfma_f32_16x16x32_bf16 v[180:183], a[124:127], v[196:199], v[180:183]\n"
"v_mfma_f32_16x16x32_bf16 v[228:231], a[128:131], v[196:199], v[228:231]\n"
"v_mfma_f32_16x16x32_bf16 v[136:139], a[112:115], v[200:203], v[136:139]\n"
"buffer_load_dwordx4 a[200:203], v6, s[48:51], s7 offen offset:2048 sc1\n"
"v_mfma_f32_16x16x32_bf16 v[152:155], a[116:119], v[200:203], v[152:155]\n"
"v_mfma_f32_16x16x32_bf16 v[168:171], a[120:123], v[200:203], v[168:171]\n"
"v_mfma_f32_16x16x32_bf16 v[184:187], a[124:127], v[200:203], v[184:187]\n"
"buffer_load_dwordx4 a[204:207], v6, s[48:51], s7 offen offset:3072 sc1\n"
"v_mfma_f32_16x16x32_bf16 v[232:235], a[128:131], v[200:203], v[232:235]\n"
"v_mfma_f32_16x16x32_bf16 v[140:143], a[112:115], v[204:207], v[140:143]\n"
"v_mfma_f32_16x16x32_bf16 v[156:159], a[116:119], v[204:207], v[156:159]\n"
"s_add_u32 s7, s7, 1024\n"
"buffer_load_dwordx4 a[208:211], v6, s[48:51], s7 offen offset:3072 sc1\n"
"v_mfma_f32_16x16x32_bf16 v[172:175], a[120:123], v[204:207], v[172:175]\n"
"v_mfma_f32_16x16x32_bf16 v[188:191], a[124:127], v[204:207], v[188:191]\n"
"v_mfma_f32_16x16x32_bf16 v[236:239], a[128:131], v[204:207], v[236:239]\n"
"v_mfma_f32_16x16x32_bf16 v[128:131], a[132:135], v[208:211], v[128:131]\n"
"buffer_load_dwordx4 v[84:87], v17, s[48:51], s59 offen sc1\n"
"v_mfma_f32_16x16x32_bf16 v[144:147], a[136:139], v[208:211], v[144:147]\n"
"v_mfma_f32_16x16x32_bf16 v[160:163], a[140:143], v[208:211], v[160:163]\n"
"v_mfma_f32_16x16x32_bf16 v[176:179], a[144:147], v[208:211], v[176:179]\n"
"buffer_load_dwordx4 v[88:91], v18, s[48:51], s59 offen sc1\n"
"v_mfma_f32_16x16x32_bf16 v[224:227], a[148:151], v[208:211], v[224:227]\n"
"v_mfma_f32_16x16x32_bf16 v[132:135], a[132:135], v[212:215], v[132:135]\n"
"v_mfma_f32_16x16x32_bf16 v[148:151], a[136:139], v[212:215], v[148:151]\n"
"buffer_load_dwordx4 v[92:95], v19, s[48:51], s59 offen sc1\n"
"v_mfma_f32_16x16x32_bf16 v[164:167], a[140:143], v[212:215], v[164:167]\n"
"v_mfma_f32_16x16x32_bf16 v[180:183], a[144:147], v[212:215], v[180:183]\n"
"v_mfma_f32_16x16x32_bf16 v[228:231], a[148:151], v[212:215], v[228:231]\n"
"v_mfma_f32_16x16x32_bf16 v[136:139], a[132:135], v[216:219], v[136:139]\n"
"s_add_u32 s7, s6, 15360\n"
"s_cmp_ge_u32 s7, s12\n"
"s_cselect_b32 s91, 25600, 0\n"
"s_sub_u32 s7, s7, s91\n"
"buffer_load_dwordx4 a[212:215], v6, s[48:51], s7 offen offset:0 sc1\n"
"v_mfma_f32_16x16x32_bf16 v[152:155], a[136:139], v[216:219], v[152:155]\n"
"v_mfma_f32_16x16x32_bf16 v[168:171], a[140:143], v[216:219], v[168:171]\n"
"v_mfma_f32_16x16x32_bf16 v[184:187], a[144:147], v[216:219], v[184:187]\n"
"s_add_u32 s59, s58, 3072\n"
"s_cmp_ge_u32 s59, s66\n"
"s_cselect_b32 s91, 5120, 0\n"
"s_sub_u32 s59, s59, s91\n"
"buffer_load_dwordx4 v[96:99], v16, s[48:51], s59 offen sc1\n"
"v_mfma_f32_16x16x32_bf16 v[232:235], a[148:151], v[216:219], v[232:235]\n"
"s_waitcnt vmcnt(29)\n"
"v_mfma_f32_16x16x32_bf16 v[140:143], a[132:135], v[220:223], v[140:143]\n"
"v_mfma_f32_16x16x32_bf16 v[156:159], a[136:139], v[220:223], v[156:159]\n"
"buffer_load_dwordx4 a[216:219], v6, s[48:51], s7 offen offset:1024 sc1\n"
"v_mfma_f32_16x16x32_bf16 v[172:175], a[140:143], v[220:223], v[172:175]\n"
"v_mfma_f32_16x16x32_bf16 v[188:191], a[144:147], v[220:223], v[188:191]\n"
"v_mfma_f32_16x16x32_bf16 v[236:239], a[148:151], v[220:223], v[236:239]\n"
"s_waitcnt vmcnt(21)\n"
"v_mfma_f32_16x16x32_bf16 v[128:131], a[152:155], a[0:3], v[128:131]\n"
"buffer_load_dwordx4 a[220:223], v6, s[48:51], s7 offen offset:2048 sc1\n"
"v_mfma_f32_16x16x32_bf16 v[144:147], a[156:159], a[0:3], v[144:147]\n"
"v_mfma_f32_16x16x32_bf16 v[160:163], a[160:163], a[0:3], v[160:163]\n"
"v_mfma_f32_16x16x32_bf16 v[176:179], a[164:167], a[0:3], v[176:179]\n"
"buffer_load_dwordx4 a[224:227], v6, s[48:51], s7 offen offset:3072 sc1\n"
"v_mfma_f32_16x16x32_bf16 v[224:227], a[168:171], a[0:3], v[224:227]\n"
"s_waitcnt vmcnt(22)\n"
"v_mfma_f32_16x16x32_bf16 v[132:135], a[152:155], a[4:7], v[132:135]\n"
"v_mfma_f32_16x16x32_bf16 v[148:151], a[156:159], a[4:7], v[148:151]\n"
"s_add_u32 s7, s7, 1024\n"
"buffer_load_dwordx4 a[228:231], v6, s[48:51], s7 offen offset:3072 sc1\n"
"v_mfma_f32_16x16x32_bf16 v[164:167], a[160:163], a[4:7], v[164:167]\n"
"v_mfma_f32_16x16x32_bf16 v[180:183], a[164:167], a[4:7], v[180:183]\n"
"v_mfma_f32_16x16x32_bf16 v[228:231], a[168:171], a[4:7], v[228:231]\n"
"s_waitcnt vmcnt(22)\n"
"v_mfma_f32_16x16x32_bf16 v[136:139], a[152:155], a[8:11], v[136:139]\n"
"buffer_load_dwordx4 v[100:103], v17, s[48:51], s59 offen sc1\n"
"v_mfma_f32_16x16x32_bf16 v[152:155], a[156:159], a[8:11], v[152:155]\n"
"v_mfma_f32_16x16x32_bf16 v[168:171], a[160:163], a[8:11], v[168:171]\n"
"v_mfma_f32_16x16x32_bf16 v[184:187], a[164:167], a[8:11], v[184:187]\n"
"buffer_load_dwordx4 v[104:107], v18, s[48:51], s59 offen sc1\n"
"v_mfma_f32_16x16x32_bf16 v[232:235], a[168:171], a[8:11], v[232:235]\n"
"s_waitcnt vmcnt(23)\n"
"v_mfma_f32_16x16x32_bf16 v[140:143], a[152:155], a[12:15], v[140:143]\n"
"v_mfma_f32_16x16x32_bf16 v[156:159], a[156:159], a[12:15], v[156:159]\n"
"buffer_load_dwordx4 v[108:111], v19, s[48:51], s59 offen sc1\n"
"v_mfma_f32_16x16x32_bf16 v[172:175], a[160:163], a[12:15], v[172:175]\n"
"v_mfma_f32_16x16x32_bf16 v[188:191], a[164:167], a[12:15], v[188:191]\n"
"v_mfma_f32_16x16x32_bf16 v[236:239], a[168:171], a[12:15], v[236:239]\n"
"s_waitcnt vmcnt(23)\n"
"v_mfma_f32_16x16x32_bf16 v[128:131], a[172:175], a[16:19], v[128:131]\n"
"s_add_u32 s7, s6, 20480\n"
"s_cmp_ge_u32 s7, s12\n"
"s_cselect_b32 s91, 25600, 0\n"
"s_sub_u32 s7, s7, s91\n"
"buffer_load_dwordx4 a[232:235], v6, s[48:51], s7 offen offset:0 sc1\n"
"v_mfma_f32_16x16x32_bf16 v[144:147], a[176:179], a[16:19], v[144:147]\n"
"s_add_u32 s82, s76, 1\n"
"v_mfma_f32_16x16x32_bf16 v[160:163], a[180:183], a[16:19], v[160:163]\n"
"s_and_b32 s82, s82, 3\n"
"s_waitcnt vmcnt(23)\n"
"v_mfma_f32_16x16x32_bf16 v[176:179], a[184:187], a[16:19], v[176:179]\n"
"s_add_u32 s59, s58, 4096\n"
"s_cmp_ge_u32 s59, s66\n"
"s_cselect_b32 s91, 5120, 0\n"
"s_sub_u32 s59, s59, s91\n"
"buffer_load_dwordx4 v[196:199], v17, s[48:51], s59 offen sc1\n"
"s_waitcnt vmcnt(23)\n"
"v_mfma_f32_16x16x32_bf16 v[224:227], a[188:191], a[16:19], v[224:227]\n"
"s_waitcnt vmcnt(22)\n"
"v_mfma_f32_16x16x32_bf16 v[132:135], a[172:175], a[20:23], v[132:135]\n"
"s_mul_i32 s82, s82, 15360\n"
"v_mfma_f32_16x16x32_bf16 v[148:151], a[176:179], a[20:23], v[148:151]\n"
"buffer_load_dwordx4 a[236:239], v6, s[48:51], s7 offen offset:1024 sc1\n"
"v_mfma_f32_16x16x32_bf16 v[164:167], a[180:183], a[20:23], v[164:167]\n"
"v_add_u32 v36, s82, v6\n"
"v_mfma_f32_16x16x32_bf16 v[180:183], a[184:187], a[20:23], v[180:183]\n"
"s_add_u32 s82, s76, 2\n"
"v_mfma_f32_16x16x32_bf16 v[228:231], a[188:191], a[20:23], v[228:231]\n"
"s_waitcnt vmcnt(22)\n"
"v_mfma_f32_16x16x32_bf16 v[136:139], a[172:175], a[24:27], v[136:139]\n"
"buffer_load_dwordx4 a[240:243], v6, s[48:51], s7 offen offset:2048 sc1\n"
"s_and_b32 s82, s82, 3\n"
"v_mfma_f32_16x16x32_bf16 v[152:155], a[176:179], a[24:27], v[152:155]\n"
"v_mfma_f32_16x16x32_bf16 v[168:171], a[180:183], a[24:27], v[168:171]\n"
"s_mul_i32 s82, s82, 15360\n"
"v_mfma_f32_16x16x32_bf16 v[184:187], a[184:187], a[24:27], v[184:187]\n"
"buffer_load_dwordx4 a[244:247], v6, s[48:51], s7 offen offset:3072 sc1\n"
"v_mfma_f32_16x16x32_bf16 v[232:235], a[188:191], a[24:27], v[232:235]\n"
"v_add_u32 v37, s82, v6\n"
"s_waitcnt vmcnt(23)\n"
"v_mfma_f32_16x16x32_bf16 v[140:143], a[172:175], a[28:31], v[140:143]\n"
"s_add_u32 s82, s76, 3\n"
"v_mfma_f32_16x16x32_bf16 v[156:159], a[176:179], a[28:31], v[156:159]\n"
"s_add_u32 s7, s7, 1024\n"
"buffer_load_dwordx4 a[248:251], v6, s[48:51], s7 offen offset:3072 sc1\n"
"v_mfma_f32_16x16x32_bf16 v[172:175], a[180:183], a[28:31], v[172:175]\n"
"v_mfma_f32_16x16x32_bf16 v[188:191], a[184:187], a[28:31], v[188:191]\n"
"s_and_b32 s82, s82, 3\n"
"v_mfma_f32_16x16x32_bf16 v[236:239], a[188:191], a[28:31], v[236:239]\n"
"buffer_load_dwordx4 v[200:203], v18, s[48:51], s59 offen sc1\n"
"s_waitcnt vmcnt(23)\n"
"v_mfma_f32_16x16x32_bf16 v[128:131], a[192:195], v[80:83], v[128:131]\n"
"s_mul_i32 s82, s82, 15360\n"
"s_waitcnt vmcnt(22)\n"
"v_mfma_f32_16x16x32_bf16 v[144:147], a[196:199], v[80:83], v[144:147]\n"
"v_add_u32 v38, s82, v6\n"
"s_waitcnt vmcnt(21)\n"
"v_mfma_f32_16x16x32_bf16 v[160:163], a[200:203], v[80:83], v[160:163]\n"
"s_waitcnt vmcnt(20)\n"
"v_mfma_f32_16x16x32_bf16 v[176:179], a[204:207], v[80:83], v[176:179]\n"
"buffer_load_dwordx4 v[204:207], v19, s[48:51], s59 offen sc1\n"
"s_mul_i32 s82, s76, 15360\n"
"s_waitcnt vmcnt(20)\n"
"v_mfma_f32_16x16x32_bf16 v[224:227], a[208:211], v[80:83], v[224:227]\n"
"s_waitcnt vmcnt(19)\n"
"v_mfma_f32_16x16x32_bf16 v[132:135], a[192:195], v[84:87], v[132:135]\n"
"v_add_u32 v26, s82, v6\n"
"v_mfma_f32_16x16x32_bf16 v[148:151], a[196:199], v[84:87], v[148:151]\n"
"buffer_load_dwordx4 v[192:195], v16, s[48:51], s59 offen sc1\n"
"v_mfma_f32_16x16x32_bf16 v[164:167], a[200:203], v[84:87], v[164:167]\n"
"s_lshl_b32 s82, s88, 2\n"
"v_mfma_f32_16x16x32_bf16 v[180:183], a[204:207], v[84:87], v[180:183]\n"
"s_add_u32 s82, s82, s76\n"
"v_mfma_f32_16x16x32_bf16 v[228:231], a[208:211], v[84:87], v[228:231]\n"
"s_waitcnt vmcnt(19)\n"
"v_mfma_f32_16x16x32_bf16 v[136:139], a[192:195], v[88:91], v[136:139]\n"
"s_mul_i32 s82, s82, 20480\n"
"v_mfma_f32_16x16x32_bf16 v[152:155], a[196:199], v[88:91], v[152:155]\n"
"s_mul_i32 s83, s89, 160\n"
"v_mfma_f32_16x16x32_bf16 v[168:171], a[200:203], v[88:91], v[168:171]\n"
"v_mfma_f32_16x16x32_bf16 v[184:187], a[204:207], v[88:91], v[184:187]\n"
"s_add_u32 s61, s82, s83\n"
"v_mfma_f32_16x16x32_bf16 v[232:235], a[208:211], v[88:91], v[232:235]\n"
"v_and_b32 v12, 15, v1\n"
"s_waitcnt vmcnt(18)\n"
"v_mfma_f32_16x16x32_bf16 v[140:143], a[192:195], v[92:95], v[140:143]\n"
"v_mul_u32_u24 v12, 1280, v12\n"
"v_mfma_f32_16x16x32_bf16 v[156:159], a[196:199], v[92:95], v[156:159]\n"
"v_mfma_f32_16x16x32_bf16 v[172:175], a[200:203], v[92:95], v[172:175]\n"
"v_lshrrev_b32 v13, 4, v1\n"
"v_mfma_f32_16x16x32_bf16 v[188:191], a[204:207], v[92:95], v[188:191]\n"
"v_lshl_add_u32 v25, v13, 3, v12\n"
"v_mfma_f32_16x16x32_bf16 v[236:239], a[208:211], v[92:95], v[236:239]\n"
"v_bfe_u32 v13, v1, 4, 1\n"
"s_waitcnt vmcnt(16)\n"
"v_mfma_f32_16x16x32_bf16 v[128:131], a[212:215], v[96:99], v[128:131]\n"
"s_waitcnt vmcnt(15)\n"
"v_mfma_f32_16x16x32_bf16 v[144:147], a[216:219], v[96:99], v[144:147]\n"
"v_lshl_add_u32 v39, v13, 5, v12\n"
"s_waitcnt vmcnt(14)\n"
"v_mfma_f32_16x16x32_bf16 v[160:163], a[220:223], v[96:99], v[160:163]\n"
"v_lshrrev_b32 v13, 5, v1\n"
"s_waitcnt vmcnt(13)\n"
"v_mfma_f32_16x16x32_bf16 v[176:179], a[224:227], v[96:99], v[176:179]\n"
"v_lshl_add_u32 v39, v13, 4, v39\n"
"s_waitcnt vmcnt(12)\n"
"v_mfma_f32_16x16x32_bf16 v[224:227], a[228:231], v[96:99], v[224:227]\n"
"s_waitcnt vmcnt(11)\n"
"v_mfma_f32_16x16x32_bf16 v[132:135], a[212:215], v[100:103], v[132:135]\n"
"s_mov_b32 s36, s32\n"
"v_mfma_f32_16x16x32_bf16 v[148:151], a[216:219], v[100:103], v[148:151]\n"
"s_and_b32 s37, s33, 0xffff\n"
"v_mfma_f32_16x16x32_bf16 v[164:167], a[220:223], v[100:103], v[164:167]\n"
"s_mov_b32 s38, 0x280000\n"
"v_mfma_f32_16x16x32_bf16 v[180:183], a[224:227], v[100:103], v[180:183]\n"
"v_mfma_f32_16x16x32_bf16 v[228:231], a[228:231], v[100:103], v[228:231]\n"
"s_mov_b32 s39, 0x20000\n"
"s_waitcnt vmcnt(10)\n"
"v_mfma_f32_16x16x32_bf16 v[136:139], a[212:215], v[104:107], v[136:139]\n"
"v_mfma_f32_16x16x32_bf16 v[152:155], a[216:219], v[104:107], v[152:155]\n"
"v_mfma_f32_16x16x32_bf16 v[168:171], a[220:223], v[104:107], v[168:171]\n"
"v_mfma_f32_16x16x32_bf16 v[184:187], a[224:227], v[104:107], v[184:187]\n"
"v_mfma_f32_16x16x32_bf16 v[232:235], a[228:231], v[104:107], v[232:235]\n"
"s_waitcnt vmcnt(9)\n"
"v_mfma_f32_16x16x32_bf16 v[140:143], a[212:215], v[108:111], v[140:143]\n"
"v_mfma_f32_16x16x32_bf16 v[156:159], a[216:219], v[108:111], v[156:159]\n"
"v_mfma_f32_16x16x32_bf16 v[172:175], a[220:223], v[108:111], v[172:175]\n"
"v_mfma_f32_16x16x32_bf16 v[188:191], a[224:227], v[108:111], v[188:191]\n"
"v_mfma_f32_16x16x32_bf16 v[236:239], a[228:231], v[108:111], v[236:239]\n"
"s_waitcnt vmcnt(7)\n"
"v_mfma_f32_16x16x32_bf16 v[132:135], a[232:235], v[196:199], v[132:135]\n"
"s_waitcnt vmcnt(6)\n"
"v_mfma_f32_16x16x32_bf16 v[148:151], a[236:239], v[196:199], v[148:151]\n"
"s_waitcnt vmcnt(5)\n"
"v_mfma_f32_16x16x32_bf16 v[164:167], a[240:243], v[196:199], v[164:167]\n"
"s_waitcnt vmcnt(4)\n"
"v_mfma_f32_16x16x32_bf16 v[180:183], a[244:247], v[196:199], v[180:183]\n"
"s_waitcnt vmcnt(3)\n"
"v_mfma_f32_16x16x32_bf16 v[228:231], a[248:251], v[196:199], v[228:231]\n"
"s_waitcnt vmcnt(2)\n"
"v_mfma_f32_16x16x32_bf16 v[136:139], a[232:235], v[200:203], v[136:139]\n"
"ds_write_b128 v36, v[132:135] offset:0\n"
"v_mfma_f32_16x16x32_bf16 v[152:155], a[236:239], v[200:203], v[152:155]\n"
"ds_write_b128 v36, v[148:151] offset:1024\n"
"v_mfma_f32_16x16x32_bf16 v[168:171], a[240:243], v[200:203], v[168:171]\n"
"ds_write_b128 v36, v[164:167] offset:2048\n"
"v_mfma_f32_16x16x32_bf16 v[184:187], a[244:247], v[200:203], v[184:187]\n"
"ds_write_b128 v36, v[180:183] offset:3072\n"
"v_mfma_f32_16x16x32_bf16 v[232:235], a[248:251], v[200:203], v[232:235]\n"
"ds_write_b128 v36, v[228:231] offset:4096\n"
"s_waitcnt vmcnt(1)\n"
"v_mfma_f32_16x16x32_bf16 v[140:143], a[232:235], v[204:207], v[140:143]\n"
"ds_write_b128 v37, v[136:139] offset:5120\n"
"v_mfma_f32_16x16x32_bf16 v[156:159], a[236:239], v[204:207], v[156:159]\n"
"ds_write_b128 v37, v[152:155] offset:6144\n"
"v_mfma_f32_16x16x32_bf16 v[172:175], a[240:243], v[204:207], v[172:175]\n"
"ds_write_b128 v37, v[168:171] offset:7168\n"
"v_mfma_f32_16x16x32_bf16 v[188:191], a[244:247], v[204:207], v[188:191]\n"
"ds_write_b128 v37, v[184:187] offset:8192\n"
"v_mfma_f32_16x16x32_bf16 v[236:239], a[248:251], v[204:207], v[236:239]\n"
"ds_write_b128 v37, v[232:235] offset:9216\n"
"s_waitcnt vmcnt(0)\n"
"v_mfma_f32_16x16x32_bf16 v[128:131], a[232:235], v[192:195], v[128:131]\n"
"ds_write_b128 v38, v[140:143] offset:10240\n"
"v_mfma_f32_16x16x32_bf16 v[144:147], a[236:239], v[192:195], v[144:147]\n"
"ds_write_b128 v38, v[156:159] offset:11264\n"
"v_mfma_f32_16x16x32_bf16 v[160:163], a[240:243], v[192:195], v[160:163]\n"
"ds_write_b128 v38, v[172:175] offset:12288\n"
"v_mfma_f32_16x16x32_bf16 v[176:179], a[244:247], v[192:195], v[176:179]\n"
"ds_write_b128 v38, v[188:191] offset:13312\n"
"v_mfma_f32_16x16x32_bf16 v[224:227], a[248:251], v[192:195], v[224:227]\n"
"ds_write_b128 v38, v[236:239] offset:14336\n"
"s_waitcnt lgkmcnt(0)\n"
"s_barrier\n"
"ds_read_b128 v[48:51], v26 offset:0\n"
"ds_read_b128 v[52:55], v26 offset:5120\n"
"ds_read_b128 v[56:59], v26 offset:10240\n"
"ds_read_b128 v[60:63], v26 offset:1024\n"
"ds_read_b128 v[64:67], v26 offset:6144\n"
"ds_read_b128 v[68:71], v26 offset:11264\n"
"ds_read_b128 v[72:75], v26 offset:2048\n"
"ds_read_b128 v[76:79], v26 offset:7168\n"
"ds_read_b128 v[80:83], v26 offset:12288\n"
"ds_read_b128 v[84:87], v26 offset:3072\n"
"ds_read_b128 v[88:91], v26 offset:8192\n"
"ds_read_b128 v[92:95], v26 offset:13312\n"
"ds_read_b128 v[96:99], v26 offset:4096\n"
"ds_read_b128 v[100:103], v26 offset:9216\n"
"ds_read_b128 v[104:107], v26 offset:14336\n"
"s_waitcnt lgkmcnt(14)\n"
"v_pk_add_f32 v[48:49], v[48:49], v[128:129]\n"
"v_pk_add_f32 v[50:51], v[50:51], v[130:131]\n"
"s_waitcnt lgkmcnt(12)\n"
"v_pk_add_f32 v[52:53], v[52:53], v[56:57]\n"
"v_pk_add_f32 v[54:55], v[54:55], v[58:59]\n"
"v_pk_add_f32 v[48:49], v[48:49], v[52:53]\n"
"v_pk_add_f32 v[50:51], v[50:51], v[54:55]\n"
"v_cvt_pk_bf16_f32 v112, v48, v49\n"
"v_cvt_pk_bf16_f32 v113, v50, v51\n"
"buffer_store_dwordx2 v[112:113], v25, s[36:39], s61 offen offset:0\n"
"s_waitcnt lgkmcnt(11)\n"
"v_pk_add_f32 v[60:61], v[60:61], v[144:145]\n"
"v_pk_add_f32 v[62:63], v[62:63], v[146:147]\n"
"s_waitcnt lgkmcnt(9)\n"
"v_pk_add_f32 v[64:65], v[64:65], v[68:69]\n"
"v_pk_add_f32 v[66:67], v[66:67], v[70:71]\n"
"v_pk_add_f32 v[60:61], v[60:61], v[64:65]\n"
"v_pk_add_f32 v[62:63], v[62:63], v[66:67]\n"
"v_cvt_pk_bf16_f32 v114, v60, v61\n"
"v_cvt_pk_bf16_f32 v115, v62, v63\n"
"buffer_store_dwordx2 v[114:115], v25, s[36:39], s61 offen offset:32\n"
"s_waitcnt lgkmcnt(8)\n"
"v_pk_add_f32 v[72:73], v[72:73], v[160:161]\n"
"v_pk_add_f32 v[74:75], v[74:75], v[162:163]\n"
"s_waitcnt lgkmcnt(6)\n"
"v_pk_add_f32 v[76:77], v[76:77], v[80:81]\n"
"v_pk_add_f32 v[78:79], v[78:79], v[82:83]\n"
"v_pk_add_f32 v[72:73], v[72:73], v[76:77]\n"
"v_pk_add_f32 v[74:75], v[74:75], v[78:79]\n"
"v_cvt_pk_bf16_f32 v116, v72, v73\n"
"v_cvt_pk_bf16_f32 v117, v74, v75\n"
"buffer_store_dwordx2 v[116:117], v25, s[36:39], s61 offen offset:64\n"
"s_waitcnt lgkmcnt(5)\n"
"v_pk_add_f32 v[84:85], v[84:85], v[176:177]\n"
"v_pk_add_f32 v[86:87], v[86:87], v[178:179]\n"
"s_waitcnt lgkmcnt(3)\n"
"v_pk_add_f32 v[88:89], v[88:89], v[92:93]\n"
"v_pk_add_f32 v[90:91], v[90:91], v[94:95]\n"
"v_pk_add_f32 v[84:85], v[84:85], v[88:89]\n"
"v_pk_add_f32 v[86:87], v[86:87], v[90:91]\n"
"v_cvt_pk_bf16_f32 v118, v84, v85\n"
"v_cvt_pk_bf16_f32 v119, v86, v87\n"
"buffer_store_dwordx2 v[118:119], v25, s[36:39], s61 offen offset:96\n"
"s_waitcnt lgkmcnt(2)\n"
"v_pk_add_f32 v[96:97], v[96:97], v[224:225]\n"
"v_pk_add_f32 v[98:99], v[98:99], v[226:227]\n"
"s_waitcnt lgkmcnt(0)\n"
"v_pk_add_f32 v[100:101], v[100:101], v[104:105]\n"
"v_pk_add_f32 v[102:103], v[102:103], v[106:107]\n"
"v_pk_add_f32 v[96:97], v[96:97], v[100:101]\n"
"v_pk_add_f32 v[98:99], v[98:99], v[102:103]\n"
"v_cvt_pk_bf16_f32 v120, v96, v97\n"
"v_cvt_pk_bf16_f32 v121, v98, v99\n"
"buffer_store_dwordx2 v[120:121], v25, s[36:39], s61 offen offset:128\n"
"s_or_b32 s83, s100, s101\n"
"s_cmp_lg_u32 s83, 0\n"
"s_cbranch_scc1 L_ffn_end\n"
"s_mov_b32 s94, -1\n"
"s_mov_b32 s95, 0x1f\n"
"s_mov_b32 s2, 11264\n"
"buffer_load_dwordx4 v[8:11], v6, s[48:51], s2 offen sc1\n"
"s_waitcnt vmcnt(0)\n"
"v_cmp_eq_u64 vcc, s[84:85], v[8:9]\n"
"v_cmp_eq_u64 s[10:11], s[84:85], v[10:11]\n"
"s_and_b64 vcc, vcc, s[10:11]\n"
"s_and_b64 s[10:11], vcc, s[94:95]\n"
"s_cmp_eq_u64 s[10:11], s[94:95]\n"
"s_cbranch_scc0 L_ffn_z_slow\n"
"L_ffn_z_ok:\n"
"buffer_store_dwordx2 v[2:3], v4, s[48:51], 0 offen sc1\n"
"L_ffn_end:\n"
"s_endpgm\n"
"L_att_b_slow:\n"
"s_mov_b32 s3, 0\n"
"L_att_b_loop:\n"
"s_sleep 6\n"
"buffer_load_dwordx4 v[16:19], v6, s[48:51], s2 offen sc1\n"
"s_waitcnt vmcnt(0)\n"
"v_cmp_eq_u64 vcc, s[84:85], v[16:17]\n"
"v_cmp_eq_u64 s[10:11], s[84:85], v[18:19]\n"
"s_and_b64 vcc, vcc, s[10:11]\n"
"s_and_b64 s[10:11], vcc, s[94:95]\n"
"s_cmp_eq_u64 s[10:11], s[94:95]\n"
"s_cbranch_scc1 L_att_b_ok\n"
"s_add_u32 s3, s3, 1\n"
"s_cmp_lt_u32 s3, 0x40000\n"
"s_cbranch_scc1 L_att_b_loop\n"
"s_branch L_att_b_ok\n"
"L_att_p_slow:\n"
"s_mov_b32 s3, 0\n"
"L_att_p_loop:\n"
"s_sleep 6\n"
"buffer_load_dwordx2 v[8:9], v43, s[48:51], s2 offen sc1\n"
"s_waitcnt vmcnt(0)\n"
"v_cmp_eq_u64 vcc, s[84:85], v[8:9]\n"
"s_and_b64 s[10:11], vcc, s[94:95]\n"
"s_cmp_eq_u64 s[10:11], s[94:95]\n"
"s_cbranch_scc1 L_att_p_ok\n"
"s_add_u32 s3, s3, 1\n"
"s_cmp_lt_u32 s3, 0x40000\n"
"s_cbranch_scc1 L_att_p_loop\n"
"s_branch L_att_p_ok\n"
"L_att_e_slow:\n"
"s_mov_b32 s3, 0\n"
"L_att_e_loop:\n"
"s_sleep 6\n"
"buffer_load_dwordx2 v[8:9], v32, s[48:51], s60 offen sc1\n"
"s_waitcnt vmcnt(0)\n"
"v_cmp_eq_u64 vcc, s[84:85], v[8:9]\n"
"s_and_b64 s[10:11], vcc, s[94:95]\n"
"s_cmp_eq_u64 s[10:11], s[94:95]\n"
"s_cbranch_scc1 L_att_e_ok\n"
"s_add_u32 s3, s3, 1\n"
"s_cmp_lt_u32 s3, 0x40000\n"
"s_cbranch_scc1 L_att_e_loop\n"
"s_branch L_att_e_ok\n"
"L_att_f_slow:\n"
"s_mov_b32 s3, 0\n"
"L_att_f_loop:\n"
"s_sleep 6\n"
"buffer_load_dwordx2 v[8:9], v32, s[48:51], s60 offen sc1\n"
"s_waitcnt vmcnt(0)\n"
"v_cmp_eq_u64 vcc, s[84:85], v[8:9]\n"
"s_and_b64 s[10:11], vcc, s[94:95]\n"
"s_cmp_eq_u64 s[10:11], s[94:95]\n"
"s_cbranch_scc1 L_att_f_ok\n"
"s_add_u32 s3, s3, 1\n"
"s_cmp_lt_u32 s3, 0x40000\n"
"s_cbranch_scc1 L_att_f_loop\n"
"s_branch L_att_f_ok\n"
"L_att_m_slow:\n"
"s_mov_b32 s3, 0\n"
"L_att_m_loop:\n"
"s_sleep 6\n"
"buffer_load_dwordx2 v[8:9], v32, s[48:51], s60 offen sc1\n"
"s_waitcnt vmcnt(0)\n"
"v_cmp_eq_u64 vcc, s[84:85], v[8:9]\n"
"s_and_b64 s[10:11], vcc, s[94:95]\n"
"s_cmp_eq_u64 s[10:11], s[94:95]\n"
"s_cbranch_scc1 L_att_m_ok\n"
"s_add_u32 s3, s3, 1\n"
"s_cmp_lt_u32 s3, 0x40000\n"
"s_cbranch_scc1 L_att_m_loop\n"
"s_branch L_att_m_ok\n"
"L_att_o_slow:\n"
"s_mov_b32 s3, 0\n"
"L_att_o_loop:\n"
"s_sleep 6\n"
"buffer_load_dwordx2 v[8:9], v32, s[48:51], s60 offen sc1\n"
"s_waitcnt vmcnt(0)\n"
"v_cmp_eq_u64 vcc, s[84:85], v[8:9]\n"
"s_and_b64 s[10:11], vcc, s[94:95]\n"
"s_cmp_eq_u64 s[10:11], s[94:95]\n"
"s_cbranch_scc1 L_att_o_ok\n"
"s_add_u32 s3, s3, 1\n"
"s_cmp_lt_u32 s3, 0x40000\n"
"s_cbranch_scc1 L_att_o_loop\n"
"s_branch L_att_o_ok\n"
"L_ffn_b_slow:\n"
"s_mov_b32 s3, 0\n"
"L_ffn_b_loop:\n"
"s_sleep 6\n"
"buffer_load_dwordx4 v[8:11], v6, s[48:51], s2 offen sc1\n"
"s_waitcnt vmcnt(0)\n"
"v_cmp_eq_u64 vcc, s[84:85], v[8:9]\n"
"v_cmp_eq_u64 s[10:11], s[84:85], v[10:11]\n"
"s_and_b64 vcc, vcc, s[10:11]\n"
"s_and_b64 s[10:11], vcc, s[94:95]\n"
"s_cmp_eq_u64 s[10:11], s[94:95]\n"
"s_cbranch_scc1 L_ffn_b_ok\n"
"s_add_u32 s3, s3, 1\n"
"s_cmp_lt_u32 s3, 0x40000\n"
"s_cbranch_scc1 L_ffn_b_loop\n"
"s_branch L_ffn_b_ok\n"
"L_ffn_g_slow:\n"
"s_mov_b32 s3, 0\n"
"L_ffn_g_loop:\n"
"s_sleep 6\n"
"buffer_load_dwordx2 v[8:9], v33, s[48:51], s2 offen sc1\n"
"s_waitcnt vmcnt(0)\n"
"v_cmp_eq_u64 vcc, s[84:85], v[8:9]\n"
"s_and_b64 s[10:11], vcc, s[94:95]\n"
"s_cmp_eq_u64 s[10:11], s[94:95]\n"
"s_cbranch_scc1 L_ffn_g_ok\n"
"s_add_u32 s3, s3, 1\n"
"s_cmp_lt_u32 s3, 0x40000\n"
"s_cbranch_scc1 L_ffn_g_loop\n"
"s_branch L_ffn_g_ok\n"
"L_ffn_e_slow:\n"
"s_mov_b32 s3, 0\n"
"L_ffn_e_loop:\n"
"s_sleep 6\n"
"buffer_load_dwordx2 v[8:9], v32, s[48:51], s60 offen sc1\n"
"s_waitcnt vmcnt(0)\n"
"v_cmp_eq_u64 vcc, s[84:85], v[8:9]\n"
"s_and_b64 s[10:11], vcc, s[94:95]\n"
"s_cmp_eq_u64 s[10:11], s[94:95]\n"
"s_cbranch_scc1 L_ffn_e_ok\n"
"s_add_u32 s3, s3, 1\n"
"s_cmp_lt_u32 s3, 0x40000\n"
"s_cbranch_scc1 L_ffn_e_loop\n"
"s_branch L_ffn_e_ok\n"
"L_ffn_f_slow:\n"
"s_mov_b32 s3, 0\n"
"L_ffn_f_loop:\n"
"s_sleep 6\n"
"buffer_load_dwordx2 v[8:9], v32, s[48:51], s60 offen sc1\n"
"s_waitcnt vmcnt(0)\n"
"v_cmp_eq_u64 vcc, s[84:85], v[8:9]\n"
"s_and_b64 s[10:11], vcc, s[94:95]\n"
"s_cmp_eq_u64 s[10:11], s[94:95]\n"
"s_cbranch_scc1 L_ffn_f_ok\n"
"s_add_u32 s3, s3, 1\n"
"s_cmp_lt_u32 s3, 0x40000\n"
"s_cbranch_scc1 L_ffn_f_loop\n"
"s_branch L_ffn_f_ok\n"
"L_ffn_m_slow:\n"
"s_mov_b32 s3, 0\n"
"L_ffn_m_loop:\n"
"s_sleep 6\n"
"buffer_load_dwordx2 v[8:9], v32, s[48:51], s60 offen sc1\n"
"s_waitcnt vmcnt(0)\n"
"v_cmp_eq_u64 vcc, s[84:85], v[8:9]\n"
"s_and_b64 s[10:11], vcc, s[94:95]\n"
"s_cmp_eq_u64 s[10:11], s[94:95]\n"
"s_cbranch_scc1 L_ffn_m_ok\n"
"s_add_u32 s3, s3, 1\n"
"s_cmp_lt_u32 s3, 0x40000\n"
"s_cbranch_scc1 L_ffn_m_loop\n"
"s_branch L_ffn_m_ok\n"
"L_ffn_o_slow:\n"
"s_mov_b32 s3, 0\n"
"L_ffn_o_loop:\n"
"s_sleep 6\n"
"buffer_load_dwordx2 v[8:9], v32, s[48:51], s60 offen sc1\n"
"s_waitcnt vmcnt(0)\n"
"v_cmp_eq_u64 vcc, s[84:85], v[8:9]\n"
"s_and_b64 s[10:11], vcc, s[94:95]\n"
"s_cmp_eq_u64 s[10:11], s[94:95]\n"
"s_cbranch_scc1 L_ffn_o_ok\n"
"s_add_u32 s3, s3, 1\n"
"s_cmp_lt_u32 s3, 0x40000\n"
"s_cbranch_scc1 L_ffn_o_loop\n"
"s_branch L_ffn_o_ok\n"
"L_ffn_z_slow:\n"
"s_mov_b32 s3, 0\n"
"L_ffn_z_loop:\n"
"s_sleep 6\n"
"buffer_load_dwordx4 v[8:11], v6, s[48:51], s2 offen sc1\n"
"s_waitcnt vmcnt(0)\n"
"v_cmp_eq_u64 vcc, s[84:85], v[8:9]\n"
"v_cmp_eq_u64 s[10:11], s[84:85], v[10:11]\n"
"s_and_b64 vcc, vcc, s[10:11]\n"
"s_and_b64 s[10:11], vcc, s[94:95]\n"
"s_cmp_eq_u64 s[10:11], s[94:95]\n"
"s_cbranch_scc1 L_ffn_z_ok\n"
"s_add_u32 s3, s3, 1\n"
"s_cmp_lt_u32 s3, 0x40000\n"
"s_cbranch_scc1 L_ffn_z_loop\n"
"s_branch L_ffn_z_ok\n"
"L_end:\n"
  :: "s"(__builtin_amdgcn_kernarg_segment_ptr()), "s"(__builtin_amdgcn_workgroup_id_x()), "v"(__builtin_amdgcn_workitem_id_x())
  : "v1", "v2", "v3", "v4", "v5", "v6", "v7", "v8", "v9", "v10", "v11", "v12", "v13", "v14", "v15", "v16", "v17", "v18", "v19", "v20", "v21", "v22", "v23", "v24", "v25", "v26", "v27", "v28", "v29", "v30", "v31", "v32", "v33", "v34", "v35", "v36", "v37", "v38", "v39", "v40", "v41", "v42", "v43", "v44", "v45", "v46", "v47", "v48", "v49", "v50", "v51", "v52", "v53", "v54", "v55", "v56", "v57", "v58", "v59", "v60", "v61", "v62", "v63", "v64", "v65", "v66", "v67", "v68", "v69", "v70", "v71", "v72", "v73", "v74", "v75", "v76", "v77", "v78", "v79", "v80", "v81", "v82", "v83", "v84", "v85", "v86", "v87", "v88", "v89", "v90", "v91", "v92", "v93", "v94", "v95", "v96", "v97", "v98", "v99", "v100", "v101", "v102", "v103", "v104", "v105", "v106", "v107", "v108", "v109", "v110", "v111", "v112", "v113", "v114", "v115", "v116", "v117", "v118", "v119", "v120", "v121", "v122", "v123", "v124", "v125", "v126", "v127", "v128", "v129", "v130", "v131", "v132", "v133", "v134", "v135", "v136", "v137", "v138", "v139", "v140", "v141", "v142", "v143", "v144", "v145", "v146", "v147", "v148", "v149", "v150", "v151", "v152", "v153", "v154", "v155", "v156", "v157", "v158", "v159", "v160", "v161", "v162", "v163", "v164", "v165", "v166", "v167", "v168", "v169", "v170", "v171", "v172", "v173", "v174", "v175", "v176", "v177", "v178", "v179", "v180", "v181", "v182", "v183", "v184", "v185", "v186", "v187", "v188", "v189", "v190", "v191", "v192", "v193", "v194", "v195", "v196", "v197", "v198", "v199", "v200", "v201", "v202", "v203", "v204", "v205", "v206", "v207", "v208", "v209", "v210", "v211", "v212", "v213", "v214", "v215", "v216", "v217", "v218", "v219", "v220", "v221", "v222", "v223", "v224", "v225", "v226", "v227", "v228", "v229", "v230", "v231", "v232", "v233", "v234", "v235", "v236", "v237", "v238", "v239", "v240", "v241", "v242", "v243", "v244", "v245", "v246", "v247", "v248", "v249", "v250", "v251", "v252", "v253", "v254", "v255", "a0", "a1", "a2", "a3", "a4", "a5", "a6", "a7", "a8", "a9", "a10", "a11", "a12", "a13", "a14", "a15", "a16", "a17", "a18", "a19", "a20", "a21", "a22", "a23", "a24", "a25", "a26", "a27", "a28", "a29", "a30", "a31", "a32", "a33", "a34", "a35", "a36", "a37", "a38", "a39", "a40", "a41", "a42", "a43", "a44", "a45", "a46", "a47", "a48", "a49", "a50", "a51", "a52", "a53", "a54", "a55", "a56", "a57", "a58", "a59", "a60", "a61", "a62", "a63", "a64", "a65", "a66", "a67", "a68", "a69", "a70", "a71", "a72", "a73", "a74", "a75", "a76", "a77", "a78", "a79", "a80", "a81", "a82", "a83", "a84", "a85", "a86", "a87", "a88", "a89", "a90", "a91", "a92", "a93", "a94", "a95", "a96", "a97", "a98", "a99", "a100", "a101", "a102", "a103", "a104", "a105", "a106", "a107", "a108", "a109", "a110", "a111", "a112", "a113", "a114", "a115", "a116", "a117", "a118", "a119", "a120", "a121", "a122", "a123", "a124", "a125", "a126", "a127", "a128", "a129", "a130", "a131", "a132", "a133", "a134", "a135", "a136", "a137", "a138", "a139", "a140", "a141", "a142", "a143", "a144", "a145", "a146", "a147", "a148", "a149", "a150", "a151", "a152", "a153", "a154", "a155", "a156", "a157", "a158", "a159", "a160", "a161", "a162", "a163", "a164", "a165", "a166", "a167", "a168", "a169", "a170", "a171", "a172", "a173", "a174", "a175", "a176", "a177", "a178", "a179", "a180", "a181", "a182", "a183", "a184", "a185", "a186", "a187", "a188", "a189", "a190", "a191", "a192", "a193", "a194", "a195", "a196", "a197", "a198", "a199", "a200", "a201", "a202", "a203", "a204", "a205", "a206", "a207", "a208", "a209", "a210", "a211", "a212", "a213", "a214", "a215", "a216", "a217", "a218", "a219", "a220", "a221", "a222", "a223", "a224", "a225", "a226", "a227", "a228", "a229", "a230", "a231", "a232", "a233", "a234", "a235", "a236", "a237", "a238", "a239", "a240", "a241", "a242", "a243", "a244", "a245", "a246", "a247", "a248", "a249", "a250", "a251", "a252", "a253", "a254", "a255", "s12", "s13", "s14", "s15", "s16", "s17", "s18", "s19", "s20", "s21", "s22", "s23", "s24", "s25", "s26", "s27", "s28", "s29", "s30", "s31", "s32", "s33", "s34", "s35", "s36", "s37", "s38", "s39", "s40", "s41", "s42", "s43", "s44", "s45", "s46", "s47", "s48", "s49", "s50", "s51", "s52", "s53", "s54", "s55", "s56", "s57", "s58", "s59", "s60", "s61", "s62", "s63", "s64", "s65", "s66", "s67", "s68", "s69", "s70", "s71", "s72", "s73", "s74", "s75", "s76", "s77", "s78", "s79", "s80", "s81", "s82", "s83", "s84", "s85", "s86", "s87", "s88", "s89", "s90", "s91", "s92", "s93", "s94", "s95", "s96", "s97", "s98", "s99", "s100", "s101", "vcc", "memory");
}

extern "C" void kernel_launch(void* const* d_in, const int* in_sizes, int n_in, void* d_out, int out_size, void* d_ws, size_t ws_size,
                              hipStream_t stream) {
  (void)in_sizes; (void)n_in; (void)out_size; (void)ws_size;
  (void)hipFuncSetAttribute((const void*)gm_main, hipFuncAttributeMaxDynamicSharedMemorySize, 163840);
  gm_main<<<256, 256, 163840, stream>>>(d_in[0], d_in[1], d_in[2], d_in[3], d_in[5], d_in[6], d_in[7], d_in[8], d_out, d_ws);
}
```
